```python
import torch

def _softmax(x, axis=-1):
    return torch.softmax(x, dim=axis)
def _gelu(x):
    return torch.nn.functional.gelu(x, approximate="tanh")
def _silu(x):
    return torch.nn.functional.silu(x)
def _sigmoid(x):
    return torch.sigmoid(x)
def _layernorm(x, axis=-1, eps=1e-5):
    m = x.mean(dim=axis, keepdim=True)
    v = x.var(dim=axis, keepdim=True, unbiased=False)
    return (x - m) / torch.sqrt(v + eps)
def _scatter_add(tgt, idx, src, axis=0):
    return tgt.clone().index_add_(axis, idx, src)
def _segment_sum(src, seg, n):
    out = torch.zeros((n,) + src.shape[1:], dtype=src.dtype)
    return out.index_add_(0, seg, src)
def _pad(x, pw):
    flat = tuple(v for pair in reversed(pw) for v in pair)
    return torch.nn.functional.pad(x, flat)

kernel_name = 'sequential_preLN__softmax_full_mha__glu_gated_swiglu__prompt'

def setup_inputs():

    _w = (lambda a: (lambda t: t.to(torch.bfloat16) if t.is_floating_point() else t)(torch.as_tensor(a)))
    def _A_setup():
        B, S, D = 1, 2048, 640
        return {
            "x": _w((lambda *s: torch.randn(*s, dtype=torch.bfloat16) * 0.05)(B, S, D)),
            "wqkv": _w((lambda *s: torch.randn(*s, dtype=torch.bfloat16) * 0.05)(D, 3 * D)),
            "wo": _w((lambda *s: torch.randn(*s, dtype=torch.bfloat16) * 0.05)(D, D)),
        }
    _A_in = _A_setup()

    def _B_setup():
        B, S, D, F = 1, 2048, 640, 1728
        return {
            "x": _w((lambda *s: torch.randn(*s, dtype=torch.bfloat16) * 0.05)(B, S, D)),
            "w1": _w((lambda *s: torch.randn(*s, dtype=torch.bfloat16) * 0.4)(D, F) * (2.0 / D) ** 0.5),
            "w3": _w((lambda *s: torch.randn(*s, dtype=torch.bfloat16) * 0.4)(D, F) * (2.0 / D) ** 0.5),
            "w2": _w((lambda *s: torch.randn(*s, dtype=torch.bfloat16) * 0.4)(F, D) * (2.0 / F) ** 0.5),
        }
    _B_in = _B_setup()

    return {
        "A_x": _A_in["x"],
        "A_wqkv": _A_in["wqkv"],
        "A_wo": _A_in["wo"],
        "B_x": _B_in["x"],
        "B_w1": _B_in["w1"],
        "B_w3": _B_in["w3"],
        "B_w2": _B_in["w2"],
    }

def reference(A_x, A_wqkv, A_wo, B_x, B_w1, B_w3, B_w2):
    def _A_ref(x, wqkv, wo):
        B, S, D = x.shape
        H = 32; Dh = D // H
        qkv = (lambda x, *d: x.permute(*d))((x @ wqkv).reshape(B, S, 3, H, Dh), 2, 0, 3, 1, 4)
        q, k, v = qkv[0], qkv[1], qkv[2]
        scale = Dh ** -0.5
        scores = torch.einsum("bhqd,bhkd->bhqk", q, k) * scale
        p = _softmax(scores, axis=-1)
        o = torch.einsum("bhqk,bhkd->bhqd", p, v)
        return (lambda x, *d: x.permute(*d))(o, 0, 2, 1, 3).reshape(B, S, D) @ wo

    def _B_ref(x, w1, w3, w2):
        gate = torch.einsum("bsd,df->bsf", x, w1)
        up = torch.einsum("bsd,df->bsf", x, w3)
        h = _silu(gate) * up
        out = torch.einsum("bsf,fd->bsd", h, w2)
        return out

    x = A_x
    h = x + _A_ref(_layernorm(x, axis=-1), A_wqkv, A_wo)
    h2 = h + _B_ref(_layernorm(h, axis=-1), B_w1, B_w3, B_w2)
    return h2
```

```cpp
#include <hip/hip_runtime.h>
#include <hip/hip_bf16.h>
#include <stdint.h>

#define DI __device__ __forceinline__
#define LAS __attribute__((address_space(3)))
typedef short bf16x8 __attribute__((ext_vector_type(8)));
typedef short s16x4 __attribute__((ext_vector_type(4)));
typedef float f32x2 __attribute__((ext_vector_type(2)));
typedef float f32x4 __attribute__((ext_vector_type(4)));
typedef float f32x16 __attribute__((ext_vector_type(16)));
typedef unsigned u32x2 __attribute__((ext_vector_type(2)));
typedef unsigned u32x4 __attribute__((ext_vector_type(4)));
typedef __bf16 bf16x2_t __attribute__((ext_vector_type(2)));
typedef short v4i16_t __attribute__((ext_vector_type(4)));
typedef LAS char* lds_ptr;

constexpr int S = 2048, D = 640, H = 32, DH = 20, F = 1728, NQKV = 3 * D;
constexpr float LN_EPS = 1e-5f;
constexpr float QSCALE = 0.22360679774997896f * 1.4426950408889634f;
constexpr float LOG2E = 1.4426950408889634f;

constexpr size_t WS_STAT1 = 65536;
constexpr size_t WS_CSQKV = WS_STAT1 + 16384;
constexpr size_t WS_PST = 131072;
constexpr size_t WS_Q = 524288;
constexpr size_t IMG = (size_t)S * D * 2;
constexpr size_t WS_K = WS_Q + IMG;
constexpr size_t WS_V = WS_K + IMG;
constexpr size_t WS_O = WS_V + IMG;
constexpr size_t WS_H = WS_O + IMG;
constexpr size_t WS_HH = WS_H + IMG;
constexpr size_t HH_BYTES = (size_t)S * F * 2;
constexpr size_t WS_WO = WS_HH + HH_BYTES;
constexpr size_t WS_W13 = WS_WO + (size_t)D * D * 2;
constexpr size_t WS_W2 = WS_W13 + (size_t)D * F * 4;
constexpr size_t WS_END = WS_W2 + (size_t)F * D * 2;

DI unsigned cvtpk(float lo, float hi) { f32x2 v = {lo, hi}; bf16x2_t b = __builtin_convertvector(v, bf16x2_t); return __builtin_bit_cast(unsigned, b); }
DI float bflo(unsigned u) { return __uint_as_float(u << 16); }
DI float bfhi(unsigned u) { return __uint_as_float(u & 0xffff0000u); }
DI f32x4 mfma16(bf16x8 a, bf16x8 b, f32x4 c) { return __builtin_amdgcn_mfma_f32_16x16x32_bf16(a, b, c, 0, 0, 0); }
DI f32x16 mfma32(bf16x8 a, bf16x8 b, f32x16 c) { return __builtin_amdgcn_mfma_f32_32x32x16_bf16(a, b, c, 0, 0, 0); }
DI int rfl(int x) { return __builtin_amdgcn_readfirstlane(x); }
DI void glds16(unsigned voff, const void* sbase, unsigned ldsdst) {
    unsigned keep;
    asm volatile("s_mov_b32 %0, m0\n\ts_mov_b32 m0, %3\n\ts_nop 0\n\tglobal_load_lds_dwordx4 %1, %2\n\ts_mov_b32 m0, %0"
                 : "=&s"(keep) : "v"(voff), "s"(sbase), "s"(ldsdst) : "memory");
}
#define WAIT_VM(n) asm volatile("s_waitcnt vmcnt(" #n ")" ::: "memory")
#define WAIT_BAR(n) asm volatile("s_waitcnt vmcnt(" #n ") lgkmcnt(0)\n\ts_barrier" ::: "memory")
#define BAR_L() asm volatile("s_waitcnt lgkmcnt(0)\n\ts_barrier" ::: "memory")
DI u32x4 lds_r128(lds_ptr p) { return *(const LAS u32x4*)p; }
DI u32x2 lds_r64(lds_ptr p) { return *(const LAS u32x2*)p; }
DI s16x4 lds_tr(lds_ptr p) { return __builtin_bit_cast(s16x4, __builtin_amdgcn_ds_read_tr16_b64_v4i16((LAS v4i16_t*)p)); }
DI bf16x8 as_frag(u32x4 v) { return __builtin_bit_cast(bf16x8, v); }
DI bf16x8 cat_frag(s16x4 a, s16x4 b) { return (bf16x8){a[0], a[1], a[2], a[3], b[0], b[1], b[2], b[3]}; }
DI float swap32_max(float m) { auto r = __builtin_amdgcn_permlane32_swap(__float_as_uint(m), __float_as_uint(m), false, false); return fmaxf(__uint_as_float(r[0]), __uint_as_float(r[1])); }

DI void pretile_unit(int u, const uint16_t* w1, const uint16_t* w3, const uint16_t* w2, const uint16_t* wo, char* ws, lds_ptr scr, int lane) {
    const int c = lane & 15, g = lane >> 4;
    if (u < 540) {
        const int s = u % 20, fb = u / 20;
        for (int i = 0; i < 4; ++i) {
            const int r = 8 * i + (lane >> 3), ch = lane & 7;
            const size_t go = (size_t)(32 * s + r) * F + fb * 64 + ch * 8;
            *(LAS u32x4*)(scr + r * 144 + ch * 16) = *(const u32x4*)(w1 + go);
            *(LAS u32x4*)(scr + 4608 + r * 144 + ch * 16) = *(const u32x4*)(w3 + go);
        }
        asm volatile("s_waitcnt lgkmcnt(0)" ::: "memory");
        for (int f = 0; f < 8; ++f) {
            const int col = 8 * f + 2 * (c >> 2) + (c & 1), m = (c >> 1) & 1;
            unsigned v[4];
#pragma unroll
            for (int j = 0; j < 4; ++j) {
                const unsigned lo = *(const LAS uint16_t*)(scr + m * 4608 + (8 * g + 2 * j) * 144 + col * 2);
                const unsigned hi = *(const LAS uint16_t*)(scr + m * 4608 + (8 * g + 2 * j + 1) * 144 + col * 2);
                v[j] = lo | (hi << 16);
            }
            *(u32x4*)(ws + WS_W13 + ((size_t)(fb * 8 + f) * 20 + s) * 1024 + lane * 16) = (u32x4){v[0], v[1], v[2], v[3]};
        }
        asm volatile("s_waitcnt lgkmcnt(0)" ::: "memory");
    } else {
        const bool is2 = u < 1080;
        const int uu = is2 ? u - 540 : u - 1080, s = uu / 10, nb = uu % 10;
        const uint16_t* w = is2 ? w2 : wo;
        for (int i = 0; i < 4; ++i) {
            const int r = 8 * i + (lane >> 3), ch = lane & 7;
            *(LAS u32x4*)(scr + r * 144 + ch * 16) = *(const u32x4*)(w + (size_t)(32 * s + r) * D + nb * 64 + ch * 8);
        }
        asm volatile("s_waitcnt lgkmcnt(0)" ::: "memory");
        for (int f = 0; f < 4; ++f) {
            const int cfg = nb * 4 + f, ct = cfg / 5, cf = cfg % 5, col = 16 * f + c;
            unsigned v[4];
#pragma unroll
            for (int j = 0; j < 4; ++j) {
                const unsigned lo = *(const LAS uint16_t*)(scr + (8 * g + 2 * j) * 144 + col * 2);
                const unsigned hi = *(const LAS uint16_t*)(scr + (8 * g + 2 * j + 1) * 144 + col * 2);
                v[j] = lo | (hi << 16);
            }
            char* dst = is2 ? ws + WS_W2 + ((size_t)(ct * 54 + s) * 5 + cf) * 1024 : ws + WS_WO + ((size_t)(ct * 20 + s) * 5 + cf) * 1024;
            *(u32x4*)(dst + lane * 16) = (u32x4){v[0], v[1], v[2], v[3]};
        }
        asm volatile("s_waitcnt lgkmcnt(0)" ::: "memory");
    }
}

__global__ __launch_bounds__(512) void k_p0(const uint16_t* __restrict__ x, const uint16_t* __restrict__ wqkv, const uint16_t* __restrict__ wo,
                                            const uint16_t* __restrict__ w1, const uint16_t* __restrict__ w3, const uint16_t* __restrict__ w2, char* __restrict__ ws) {
    extern __shared__ __attribute__((aligned(16))) char smem[];
    const int c = blockIdx.x, tid = threadIdx.x, lane = tid & 63, w = rfl(tid >> 6);
    {
        const unsigned* xr = (const unsigned*)(x + (size_t)(8 * c + w) * D);
        float s = 0.f, q = 0.f;
#pragma unroll
        for (int i = 0; i < 5; ++i) { const unsigned u = xr[lane + 64 * i]; const float a = bflo(u), b = bfhi(u); s += a + b; q += a * a + b * b; }
#pragma unroll
        for (int o = 32; o >= 1; o >>= 1) { s += __shfl_xor(s, o); q += __shfl_xor(q, o); }
        const float mean = s * (1.0f / D), var = fmaxf(q * (1.0f / D) - mean * mean, 0.f);
        if (lane == 0) ((f32x2*)(ws + WS_STAT1))[8 * c + w] = (f32x2){mean, 1.0f / sqrtf(var + LN_EPS)};
    }
    if (c >= 240 && c < 255) {
        const int cb = c - 240;
        float a[8] = {0, 0, 0, 0, 0, 0, 0, 0};
        for (int i = 0; i < 20; ++i) {
            const int r = w * 80 + i * 4 + (lane >> 4);
            const u32x4 v = *(const u32x4*)(wqkv + (size_t)r * NQKV + cb * 128 + (lane & 15) * 8);
#pragma unroll
            for (int j = 0; j < 4; ++j) { a[2 * j] += bflo(v[j]); a[2 * j + 1] += bfhi(v[j]); }
        }
        LAS float* red = (LAS float*)((lds_ptr)smem + 98304);
#pragma unroll
        for (int j = 0; j < 8; ++j) { a[j] += __shfl_xor(a[j], 16); a[j] += __shfl_xor(a[j], 32); }
        if (lane < 16) for (int j = 0; j < 8; ++j) red[w * 128 + lane * 8 + j] = a[j];
        __syncthreads();
        if (tid < 128) { float t = 0.f; for (int i = 0; i < 8; ++i) t += red[i * 128 + tid]; ((float*)(ws + WS_CSQKV))[cb * 128 + tid] = t; }
    }
    if (w < 5) pretile_unit(c + 256 * w, w1, w3, w2, wo, ws, (lds_ptr)smem + w * 9216, lane);
}

constexpr int P1_SLOT = 32768, P1_NS = 4;
DI void p1_phase(int rb, int cb, const uint16_t* x, const uint16_t* wqkv, char* ws, lds_ptr lds) {
    const int tid = threadIdx.x, lane = tid & 63, w = rfl(tid >> 6), th = w >> 2, cg = w & 3, t = lane & 15, g = lane >> 4;
    const unsigned lds0 = (unsigned)(uintptr_t)lds;
    unsigned voffA[2], voffW[2];
#pragma unroll
    for (int i = 0; i < 2; ++i) {
        const int ra = 8 * (2 * w + i) + (lane >> 3), ca = (lane & 7) ^ ((ra >> 1) & 7);
        voffA[i] = ra * (D * 2) + ca * 16;
        const int rw = 4 * (2 * w + i) + (lane >> 4), fw = (rw & 3) | (((rw >> 3) & 1) << 2), cw = (lane & 15) ^ (fw << 1);
        voffW[i] = rw * (NQKV * 2) + cw * 16;
    }
    const char* gA = (const char*)x + (size_t)rb * 128 * D * 2;
    const char* gW = (const char*)wqkv + cb * 256;
    auto stage = [&](int kt) {
        const unsigned sl = lds0 + (kt & (P1_NS - 1)) * P1_SLOT + w * 2048;
#pragma unroll
        for (int i = 0; i < 2; ++i) glds16(voffA[i], gA + kt * 128, sl + i * 1024);
#pragma unroll
        for (int i = 0; i < 2; ++i) glds16(voffW[i], gW + (size_t)kt * 64 * NQKV * 2, sl + 16384 + i * 1024);
    };
    stage(0); stage(1); stage(2);
    const int aoff = (th * 64 + t) * 128 + ((g ^ (t >> 1)) << 4);
    const int q4 = (lane >> 2) & 3, p = lane & 3, fw = q4 | ((g & 1) << 2);
    int woff[2];
#pragma unroll
    for (int n = 0; n < 2; ++n) woff[n] = 16384 + (8 * g + q4) * 256 + (((2 * cg + n) ^ fw) << 5) + p * 8;
    f32x4 acc[4][2];
#pragma unroll
    for (int m = 0; m < 4; ++m) { acc[m][0] = (f32x4){0, 0, 0, 0}; acc[m][1] = (f32x4){0, 0, 0, 0}; }
    const bool isv = cb >= 10;
    for (int kt = 0; kt < 10; ++kt) {
        if (kt < 8) WAIT_BAR(8); else if (kt == 8) WAIT_BAR(4); else WAIT_BAR(0);
        if (kt + 3 < 10) stage(kt + 3);
        const lds_ptr sl = lds + (kt & (P1_NS - 1)) * P1_SLOT;
#pragma unroll
        for (int ks = 0; ks < 2; ++ks) {
            bf16x8 a[4], b[2];
#pragma unroll
            for (int n = 0; n < 2; ++n) b[n] = cat_frag(lds_tr(sl + woff[n] + ks * 8192), lds_tr(sl + woff[n] + ks * 8192 + 1024));
#pragma unroll
            for (int m = 0; m < 4; ++m) a[m] = as_frag(lds_r128(sl + ((aoff ^ (ks << 6)) + m * 2048)));
            if (isv) {
#pragma unroll
                for (int m = 0; m < 4; ++m)
#pragma unroll
                    for (int n = 0; n < 2; ++n) acc[m][n] = mfma16(a[m], b[n], acc[m][n]);
            } else {
#pragma unroll
                for (int m = 0; m < 4; ++m)
#pragma unroll
                    for (int n = 0; n < 2; ++n) acc[m][n] = mfma16(b[n], a[m], acc[m][n]);
            }
        }
    }
    const f32x2* st = (const f32x2*)(ws + WS_STAT1) + rb * 128 + th * 64;
    const float* cs = (const float*)(ws + WS_CSQKV) + cb * 128 + cg * 32;
    if (!isv) {
        const float osc = cb < 5 ? QSCALE : 1.0f;
        char* img = ws + (cb < 5 ? WS_Q : WS_K);
        const int cbs = cb % 5;
#pragma unroll
        for (int m = 0; m < 4; ++m) {
            const f32x2 sm = st[m * 16 + t];
            const float r = sm.y * osc;
            const int tok = rb * 128 + th * 64 + m * 16 + t;
#pragma unroll
            for (int n = 0; n < 2; ++n) {
                const f32x4 c4 = *(const f32x4*)(cs + n * 16 + 4 * g);
                const f32x4 y = (acc[m][n] - c4 * sm.x) * r;
                const int cgi = cbs * 32 + cg * 8 + n * 4 + g;
                *(u32x2*)(img + (size_t)(tok >> 5) * 40960 + cgi * 256 + (tok & 31) * 8) = (u32x2){cvtpk(y[0], y[1]), cvtpk(y[2], y[3])};
            }
        }
    } else {
        char* img = ws + WS_V;
        const int pos = ((g & 1) << 1) | (g >> 1);
#pragma unroll
        for (int m = 0; m < 4; ++m) {
            f32x2 sm[4];
#pragma unroll
            for (int i = 0; i < 4; ++i) sm[i] = st[m * 16 + 4 * g + i];
            const int step = rb * 8 + th * 4 + m;
#pragma unroll
            for (int n = 0; n < 2; ++n) {
                const float c1 = cs[n * 16 + t];
                float y[4];
#pragma unroll
                for (int i = 0; i < 4; ++i) y[i] = (acc[m][n][i] - c1 * sm[i].x) * sm[i].y;
                const int col = (cb - 10) * 128 + cg * 32 + n * 16 + t;
                *(u32x2*)(img + (size_t)step * 20480 + col * 32 + pos * 8) = (u32x2){cvtpk(y[0], y[1]), cvtpk(y[2], y[3])};
            }
        }
    }
}
__global__ __launch_bounds__(512) void k_p1(const uint16_t* __restrict__ x, const uint16_t* __restrict__ wqkv, char* __restrict__ ws) {
    extern __shared__ __attribute__((aligned(16))) char smem[];
    const int c = blockIdx.x, xcd = c & 7, lb = c >> 3;
    const int rb = (xcd >> 2) * 8 + (lb & 7), cb = (xcd & 3) * 4 + (lb >> 3);
    if (cb < 15) p1_phase(rb, cb, x, wqkv, ws, (lds_ptr)smem);
}

constexpr int AT_BUF = 40960, AT_ONES = 2 * AT_BUF, AT_ONES_BYTES = 10752, AT_RED = AT_ONES + AT_ONES_BYTES, AT_LDS = AT_RED + 4 * 2 * 13 * 256 + 64;
template <bool SAFE>
DI void attn_pass(int h, int qb, const char* ws, lds_ptr lds, f32x16 (&o)[2], float (&mref)[2]) {
    const int tid = threadIdx.x, lane = tid & 63, w = rfl(tid >> 6), qg = w & 3, kh = w >> 2, r = lane & 31, hi = lane >> 5;
    const unsigned lds0 = (unsigned)(uintptr_t)lds;
    unsigned voff[5];
    const bool stV = (w >> 1) & 1;
#pragma unroll
    for (int i = 0; i < 5; ++i) {
        const int ci = 64 * (5 * (w & 1) + i) + lane;
        if (!stV) voff[i] = (ci / 80) * 40960 + (ci % 80) * 16;
        else { const int st = ci / 40, wi = ci % 40; voff[i] = st * 20480 + ((wi % 20) * 2 + wi / 20) * 16; }
    }
    const char* gsrc = ws + (stV ? WS_V + h * 640 : WS_K + h * 1280) + (size_t)kh * 32 * 40960;
    const unsigned ldst = lds0 + kh * 20480 + (stV ? 10240 : 0) + (w & 1) * 5120;
    auto stage = [&](int sidx) {
#pragma unroll
        for (int i = 0; i < 5; ++i) glds16(voff[i], gsrc + (size_t)sidx * 8 * 40960, ldst + (sidx & 1) * AT_BUF + i * 1024);
    };
    stage(0);
    u32x4 qf0[2], qf1[2];
#pragma unroll
    for (int i = 0; i < 2; ++i) {
        const char* qp = ws + WS_Q + (size_t)(qb * 8 + qg * 2 + i) * 40960 + h * 1280 + r * 8;
        const u32x2 a = *(const u32x2*)(qp + hi * 512), b = *(const u32x2*)(qp + hi * 512 + 256), c = *(const u32x2*)(qp + 1024);
        qf0[i] = (u32x4){a.x, a.y, b.x, b.y};
        qf1[i] = hi ? (u32x4){0, 0, 0, 0} : (u32x4){c.x, c.y, 0, 0};
    }
    const int kofs0 = hi * 512 + r * 8, kofs1 = 1024 + r * 8;
    int vofs[2];
#pragma unroll
    for (int b = 0; b < 2; ++b) {
        const int rr = r >= 28 ? r - 12 : r;
        vofs[b] = rr < 20 ? b * AT_BUF + kh * 20480 + 10240 + hi * 320 + rr * 16 : AT_ONES + 128;
    }
    o[0] = (f32x16){}; o[1] = (f32x16){};
    WAIT_BAR(0);
    const u32x2 kconst = {0x00003F80u, 0u};
    float mrun[2] = {-INFINITY, -INFINITY};
    if (!SAFE) {
        const u32x2 a = lds_r64(lds + kofs0), b = lds_r64(lds + kofs0 + 256), c = lds_r64(lds + kofs1);
        const bf16x8 k0 = as_frag((u32x4){a.x, a.y, b.x, b.y}), k1 = as_frag((u32x4){c.x, c.y, 0u, 0u});
#pragma unroll
        for (int i = 0; i < 2; ++i) {
            f32x16 s = mfma32(k0, as_frag(qf0[i]), (f32x16){});
            s = mfma32(k1, as_frag(qf1[i]), s);
            float m = s[0];
#pragma unroll
            for (int j = 1; j < 16; ++j) m = fmaxf(m, s[j]);
            m = swap32_max(m);
            const unsigned nb = cvtpk(-m, 0.f) & 0xffffu;
            mref[i] = -bflo(nb);
            if (!hi) qf1[i].z = nb;
        }
    }
    for (int sidx = 0; sidx < 4; ++sidx) {
        if (sidx) WAIT_BAR(0);
        if (sidx < 3) stage(sidx + 1);
        const lds_ptr kb = lds + (sidx & 1) * AT_BUF + kh * 20480;
        const lds_ptr vb = lds + vofs[sidx & 1];
#pragma unroll 2
        for (int kt = 0; kt < 8; ++kt) {
            const u32x2 a = lds_r64(kb + kofs0 + kt * 1280), b = lds_r64(kb + kofs0 + kt * 1280 + 256), c = lds_r64(kb + kofs1 + kt * 1280);
            const bf16x8 k0 = as_frag((u32x4){a.x, a.y, b.x, b.y}), k1 = as_frag((u32x4){c.x, c.y, kconst.x, kconst.y});
            const bf16x8 v0 = as_frag(lds_r128(vb + kt * 1280)), v1 = as_frag(lds_r128(vb + kt * 1280 + 640));
#pragma unroll
            for (int i = 0; i < 2; ++i) {
                f32x16 s = mfma32(k0, as_frag(qf0[i]), (f32x16){});
                s = mfma32(k1, as_frag(qf1[i]), s);
                if (SAFE) {
                    float m = s[0];
#pragma unroll
                    for (int j = 1; j < 16; ++j) m = fmaxf(m, s[j]);
                    m = fmaxf(swap32_max(m), mrun[i]);
                    const float alpha = __builtin_amdgcn_exp2f(mrun[i] - m);
                    mrun[i] = m;
#pragma unroll
                    for (int j = 0; j < 16; ++j) { o[i][j] *= alpha; s[j] -= m; }
                }
                unsigned pk[8];
#pragma unroll
                for (int j = 0; j < 8; ++j) pk[j] = cvtpk(__builtin_amdgcn_exp2f(s[2 * j]), __builtin_amdgcn_exp2f(s[2 * j + 1]));
                o[i] = mfma32(v0, as_frag((u32x4){pk[0], pk[1], pk[2], pk[3]}), o[i]);
                o[i] = mfma32(v1, as_frag((u32x4){pk[4], pk[5], pk[6], pk[7]}), o[i]);
            }
        }
    }
    if (SAFE) { mref[0] = mrun[0]; mref[1] = mrun[1]; }
}
DI void p2_phase(int h, int qb, char* ws, lds_ptr lds) {
    const int tid = threadIdx.x, lane = tid & 63, w = rfl(tid >> 6), qg = w & 3, kh = w >> 2, r = lane & 31, hi = lane >> 5;
    for (int i = tid; i < AT_ONES_BYTES / 4; i += 512) ((LAS unsigned*)(lds + AT_ONES))[i] = 0x3F803F80u;
    LAS float* red = (LAS float*)(lds + AT_RED) + qg * (2 * 13 * 64);
    LAS unsigned* flag = (LAS unsigned*)(lds + AT_RED + 4 * 2 * 13 * 256);
    if (tid == 0) *flag = 0;
    f32x16 o[2];
    float mref[2];
    attn_pass<false>(h, qb, ws, lds, o, mref);
    if (kh == 1) {
#pragma unroll
        for (int i = 0; i < 2; ++i)
#pragma unroll
            for (int j = 0; j < 12; ++j) red[(i * 13 + j) * 64 + lane] = o[i][j];
    }
    BAR_L();
    if (kh == 0) {
        bool bad = false;
#pragma unroll
        for (int i = 0; i < 2; ++i) {
#pragma unroll
            for (int j = 0; j < 12; ++j) o[i][j] += red[(i * 13 + j) * 64 + lane];
            bad |= hi && !(o[i][8] <= 1.152921504606847e18f);
        }
        if (__any(bad) && lane == 0) *flag = 1;
    }
    BAR_L();
    if (*flag) {
        BAR_L();
        attn_pass<true>(h, qb, ws, lds, o, mref);
        if (kh == 1) {
#pragma unroll
            for (int i = 0; i < 2; ++i) {
#pragma unroll
                for (int j = 0; j < 12; ++j) red[(i * 13 + j) * 64 + lane] = o[i][j];
                red[(i * 13 + 12) * 64 + lane] = mref[i];
            }
        }
        BAR_L();
        if (kh == 0) {
#pragma unroll
            for (int i = 0; i < 2; ++i) {
                const float m1 = red[(i * 13 + 12) * 64 + lane], m = fmaxf(mref[i], m1);
                const float a0 = __builtin_amdgcn_exp2f(mref[i] - m), a1 = __builtin_amdgcn_exp2f(m1 - m);
#pragma unroll
                for (int j = 0; j < 12; ++j) o[i][j] = o[i][j] * a0 + red[(i * 13 + j) * 64 + lane] * a1;
            }
        }
    }
    if (kh == 0) {
#pragma unroll
        for (int i = 0; i < 2; ++i) {
            auto lr = __builtin_amdgcn_permlane32_swap(__float_as_uint(o[i][8]), __float_as_uint(o[i][8]), false, false);
            const float inv = 1.0f / __uint_as_float(lr[1]);
            const int tok = qb * 256 + qg * 64 + i * 32 + r;
            char* base = ws + WS_O + (size_t)(tok >> 4) * 20480 + (tok & 15) * 16;
#pragma unroll
            for (int u = 0; u < 3; ++u) {
                if (u == 2 && hi) continue;
                const int col = h * 20 + 8 * u + 4 * hi;
                const u32x2 v = {cvtpk(o[i][4 * u] * inv, o[i][4 * u + 1] * inv), cvtpk(o[i][4 * u + 2] * inv, o[i][4 * u + 3] * inv)};
                *(u32x2*)(base + (col >> 5) * 1024 + ((col >> 3) & 3) * 256 + (col & 7) * 2) = v;
            }
        }
    }
}
__global__ __launch_bounds__(512) void k_p2(char* __restrict__ ws) {
    extern __shared__ __attribute__((aligned(16))) char smem[];
    const int c = blockIdx.x;
    p2_phase((c & 7) * 4 + ((c >> 3) & 3), c >> 5, ws, (lds_ptr)smem);
}

template <int KS>
DI void small_gemm(int rb64, int ct, const char* aimg, const char* wimg, lds_ptr lds, f32x4 (&acc)[5]) {
    const int tid = threadIdx.x, lane = tid & 63, w = rfl(tid >> 6), tt = w & 3, kh = w >> 2;
    constexpr int KH = KS / 2, CH = (KS == 20) ? 10 : 14;
    const unsigned lds0 = (unsigned)(uintptr_t)lds;
    const char* ap = aimg + (size_t)(rb64 * 4 + tt) * (KS * 1024) + (size_t)kh * KH * 1024 + lane * 16;
    const char* wp = wimg + (size_t)ct * KS * 5120;
#pragma unroll
    for (int cf = 0; cf < 5; ++cf) acc[cf] = (f32x4){0, 0, 0, 0};
    for (int s0 = 0; s0 < KH; s0 += CH) {
        const int n = (KH - s0 < CH) ? KH - s0 : CH;
        if (s0) BAR_L();
        for (int pc = w; pc < 2 * n * 5; pc += 8) {
            const int half = pc / (n * 5), rem = pc % (n * 5);
            glds16(lane * 16, wp + (size_t)(half * KH + s0) * 5120 + rem * 1024, lds0 + half * (CH * 5120) + rem * 1024);
        }
        u32x4 a[CH];
#pragma unroll
        for (int i = 0; i < CH; ++i) if (i < n) a[i] = *(const u32x4*)(ap + (s0 + i) * 1024);
        WAIT_BAR(0);
#pragma unroll
        for (int i = 0; i < CH; ++i) if (i < n) {
#pragma unroll
            for (int cf = 0; cf < 5; ++cf) acc[cf] = mfma16(as_frag(lds_r128(lds + kh * (CH * 5120) + i * 5120 + cf * 1024 + lane * 16)), as_frag(a[i]), acc[cf]);
        }
    }
    BAR_L();
    LAS float* red = (LAS float*)lds + tt * (20 * 64);
    if (kh == 1) {
#pragma unroll
        for (int cf = 0; cf < 5; ++cf)
#pragma unroll
            for (int i = 0; i < 4; ++i) red[(cf * 4 + i) * 64 + lane] = acc[cf][i];
    }
    BAR_L();
    if (kh == 0) {
#pragma unroll
        for (int cf = 0; cf < 5; ++cf)
#pragma unroll
            for (int i = 0; i < 4; ++i) acc[cf][i] += red[(cf * 4 + i) * 64 + lane];
    }
}
DI void p3_phase(int rb64, int ct, const uint16_t* x, char* ws, lds_ptr lds) {
    const int tid = threadIdx.x, lane = tid & 63, w = rfl(tid >> 6), tt = w & 3, kh = w >> 2, t = lane & 15, g = lane >> 4;
    f32x4 acc[5];
    small_gemm<20>(rb64, ct, ws + WS_O, ws + WS_WO, lds, acc);
    if (kh == 0) {
        const int tok = rb64 * 64 + tt * 16 + t;
        float s = 0.f, q = 0.f;
#pragma unroll
        for (int cf = 0; cf < 5; ++cf) {
            const int col = ct * 80 + cf * 16 + 4 * g;
            const u32x2 xv = *(const u32x2*)(x + (size_t)tok * D + col);
            const u32x2 hv = {cvtpk(acc[cf][0] + bflo(xv.x), acc[cf][1] + bfhi(xv.x)), cvtpk(acc[cf][2] + bflo(xv.y), acc[cf][3] + bfhi(xv.y))};
            const float h0 = bflo(hv.x), h1 = bfhi(hv.x), h2 = bflo(hv.y), h3 = bfhi(hv.y);
            s += (h0 + h1) + (h2 + h3); q += (h0 * h0 + h1 * h1) + (h2 * h2 + h3 * h3);
            *(u32x2*)(ws + WS_H + (size_t)(tok >> 4) * 20480 + (col >> 5) * 1024 + ((col >> 3) & 3) * 256 + t * 16 + (col & 7) * 2) = hv;
        }
        s += __shfl_xor(s, 16); s += __shfl_xor(s, 32); q += __shfl_xor(q, 16); q += __shfl_xor(q, 32);
        if (g == 0) ((f32x2*)(ws + WS_PST))[tok * 8 + ct] = (f32x2){s, q};
    }
}
DI void p5_phase(int rb64, int ct, char* ws, uint16_t* out, lds_ptr lds) {
    const int tid = threadIdx.x, lane = tid & 63, w = rfl(tid >> 6), tt = w & 3, kh = w >> 2, t = lane & 15, g = lane >> 4;
    f32x4 acc[5];
    small_gemm<54>(rb64, ct, ws + WS_HH, ws + WS_W2, lds, acc);
    if (kh == 0) {
        const int tok = rb64 * 64 + tt * 16 + t;
#pragma unroll
        for (int cf = 0; cf < 5; ++cf) {
            const int col = ct * 80 + cf * 16 + 4 * g;
            const u32x2 hv = *(const u32x2*)(ws + WS_H + (size_t)(tok >> 4) * 20480 + (col >> 5) * 1024 + ((col >> 3) & 3) * 256 + t * 16 + (col & 7) * 2);
            *(u32x2*)(out + (size_t)tok * D + col) = (u32x2){cvtpk(acc[cf][0] + bflo(hv.x), acc[cf][1] + bfhi(hv.x)), cvtpk(acc[cf][2] + bflo(hv.y), acc[cf][3] + bfhi(hv.y))};
        }
    }
}
__global__ __launch_bounds__(512) void k_p3(const uint16_t* __restrict__ x, char* __restrict__ ws) {
    extern __shared__ __attribute__((aligned(16))) char smem[];
    const int c = blockIdx.x, lb = c >> 3;
    p3_phase((c & 7) * 4 + (lb >> 3), lb & 7, x, ws, (lds_ptr)smem);
}
__global__ __launch_bounds__(512) void k_p5(char* __restrict__ ws, uint16_t* __restrict__ out) {
    extern __shared__ __attribute__((aligned(16))) char smem[];
    const int c = blockIdx.x, lb = c >> 3;
    p5_phase((c & 7) * 4 + (lb >> 3), lb & 7, ws, out, (lds_ptr)smem);
}

constexpr int P4_CS = 143360;
DI void p4_phase(int rb, int cb, char* ws, lds_ptr lds) {
    const int tid = threadIdx.x, lane = tid & 63, w = rfl(tid >> 6), t = lane & 15, g = lane >> 4;
    const int f0 = cb < 24 ? cb * 7 : 168 + (cb - 24) * 6, nf = cb < 24 ? 7 : 6;
    const unsigned lds0 = (unsigned)(uintptr_t)lds;
    for (int pc = w; pc < nf * 20; pc += 8) glds16(lane * 16, ws + WS_W13 + (size_t)f0 * 20480 + pc * 1024, lds0 + pc * 1024);
    float mean[2], rstd[2];
#pragma unroll
    for (int i = 0; i < 2; ++i) {
        const int tok = rb * 256 + w * 32 + i * 16 + t;
        const f32x4 pv = *(const f32x4*)(ws + WS_PST + (size_t)tok * 64 + g * 16);
        float s = pv[0] + pv[2], q = pv[1] + pv[3];
        s += __shfl_xor(s, 16); s += __shfl_xor(s, 32); q += __shfl_xor(q, 16); q += __shfl_xor(q, 32);
        mean[i] = s * (1.0f / D);
        rstd[i] = 1.0f / sqrtf(fmaxf(q * (1.0f / D) - mean[i] * mean[i], 0.f) + LN_EPS);
    }
    WAIT_BAR(0);
    if (w < nf) {
        f32x4 cs = {0, 0, 0, 0};
        const bf16x8 ones = {0x3F80, 0x3F80, 0x3F80, 0x3F80, 0x3F80, 0x3F80, 0x3F80, 0x3F80};
        for (int s = 0; s < 20; ++s) cs = mfma16(as_frag(lds_r128(lds + (w * 20 + s) * 1024 + lane * 16)), ones, cs);
        if (t == 0) *(LAS f32x4*)(lds + P4_CS + w * 64 + g * 16) = cs;
    }
    BAR_L();
    f32x4 acc[2][7];
#pragma unroll
    for (int f = 0; f < 7; ++f) {
        const f32x4 cs = *(const LAS f32x4*)(lds + P4_CS + f * 64 + g * 16);
        acc[0][f] = cs * (-mean[0]); acc[1][f] = cs * (-mean[1]);
    }
    const char* ap = ws + WS_H + (size_t)(rb * 16 + w * 2) * 20480 + lane * 16;
#pragma unroll 4
    for (int s = 0; s < 20; ++s) {
        const bf16x8 a0 = as_frag(*(const u32x4*)(ap + s * 1024)), a1 = as_frag(*(const u32x4*)(ap + 20480 + s * 1024));
#pragma unroll
        for (int f = 0; f < 7; ++f) if (f < nf) {
            const bf16x8 wf = as_frag(lds_r128(lds + (f * 20 + s) * 1024 + lane * 16));
            acc[0][f] = mfma16(wf, a0, acc[0][f]);
            acc[1][f] = mfma16(wf, a1, acc[1][f]);
        }
    }
#pragma unroll
    for (int i = 0; i < 2; ++i) {
        unsigned d[8];
#pragma unroll
        for (int f = 0; f < 7; ++f) {
            float o2[2];
#pragma unroll
            for (int j = 0; j < 2; ++j) {
                const float gt = acc[i][f][j] * rstd[i], up = acc[i][f][2 + j] * rstd[i];
                o2[j] = gt * up * __builtin_amdgcn_rcpf(1.0f + __builtin_amdgcn_exp2f(-LOG2E * gt));
            }
            d[f] = cvtpk(o2[0], o2[1]);
        }
        d[7] = 0;
        char* dst = ws + WS_HH + (size_t)(rb * 16 + w * 2 + i) * 55296 + (size_t)f0 * 256 + lane * 16;
#pragma unroll
        for (int u = 0; u < 2; ++u) {
            auto a = __builtin_amdgcn_permlane32_swap(d[4 * u], d[4 * u + 2], false, false);
            auto b = __builtin_amdgcn_permlane32_swap(d[4 * u + 1], d[4 * u + 3], false, false);
            auto e = __builtin_amdgcn_permlane16_swap(a[0], b[0], false, false);
            auto f = __builtin_amdgcn_permlane16_swap(a[1], b[1], false, false);
            if (4 * u + g < nf) *(u32x4*)(dst + u * 1024) = (u32x4){e[0], e[1], f[0], f[1]};
        }
    }
}
__global__ __launch_bounds__(512) void k_p4(char* __restrict__ ws) {
    extern __shared__ __attribute__((aligned(16))) char smem[];
    const int c = blockIdx.x, xcd = c & 7, lb = c >> 3;
    p4_phase((xcd >> 1) * 2 + (lb >> 4), (xcd & 1) * 16 + (lb & 15), ws, (lds_ptr)smem);
}

extern "C" void kernel_launch(void* const* d_in, const int* in_sizes, int n_in, void* d_out, int out_size, void* d_ws, size_t ws_size, hipStream_t stream) {
    const uint16_t* x = (const uint16_t*)d_in[0];
    const uint16_t* wqkv = (const uint16_t*)d_in[1];
    const uint16_t* wo = (const uint16_t*)d_in[2];
    const uint16_t* w1 = (const uint16_t*)d_in[4];
    const uint16_t* w3 = (const uint16_t*)d_in[5];
    const uint16_t* w2 = (const uint16_t*)d_in[6];
    char* ws = (char*)d_ws;
    constexpr int L0 = 102400, L1 = P1_NS * P1_SLOT, L2 = AT_LDS, L3 = 102400, L4 = P4_CS + 512, L5 = 2 * 14 * 5120;
    hipFuncSetAttribute((const void*)k_p0, hipFuncAttributeMaxDynamicSharedMemorySize, L0);
    hipFuncSetAttribute((const void*)k_p1, hipFuncAttributeMaxDynamicSharedMemorySize, L1);
    hipFuncSetAttribute((const void*)k_p2, hipFuncAttributeMaxDynamicSharedMemorySize, L2);
    hipFuncSetAttribute((const void*)k_p3, hipFuncAttributeMaxDynamicSharedMemorySize, L3);
    hipFuncSetAttribute((const void*)k_p4, hipFuncAttributeMaxDynamicSharedMemorySize, L4);
    hipFuncSetAttribute((const void*)k_p5, hipFuncAttributeMaxDynamicSharedMemorySize, L5);
    k_p0<<<256, 512, L0, stream>>>(x, wqkv, wo, w1, w3, w2, ws);
    k_p1<<<256, 512, L1, stream>>>(x, wqkv, ws);
    k_p2<<<256, 512, L2, stream>>>(ws);
    k_p3<<<256, 512, L3, stream>>>(x, ws);
    k_p4<<<256, 512, L4, stream>>>(ws);
    k_p5<<<256, 512, L5, stream>>>(ws, (uint16_t*)d_out);
}
```

```cpp
#include <hip/hip_runtime.h>
#include <hip/hip_bf16.h>
#include <stdint.h>

#define DI __device__ __forceinline__
#define LAS __attribute__((address_space(3)))
#define GAS __attribute__((address_space(1)))
typedef short bf16x8 __attribute__((ext_vector_type(8)));
typedef short s16x4 __attribute__((ext_vector_type(4)));
typedef float f32x2 __attribute__((ext_vector_type(2)));
typedef float f32x4 __attribute__((ext_vector_type(4)));
typedef float f32x16 __attribute__((ext_vector_type(16)));
typedef unsigned u32x2 __attribute__((ext_vector_type(2)));
typedef unsigned u32x4 __attribute__((ext_vector_type(4)));
typedef unsigned long long u64;
typedef __bf16 bf16x2_t __attribute__((ext_vector_type(2)));
typedef short v4i16_t __attribute__((ext_vector_type(4)));
typedef LAS char* lds_ptr;
typedef __amdgpu_buffer_rsrc_t rsrc_t;

constexpr int S = 2048, D = 640, H = 32, DH = 20, F = 1728, NQKV = 3 * D;
constexpr float LN_EPS = 1e-5f;
constexpr float LOG2E = 1.4426950408889634f;
constexpr float QSCALE = 0.22360679774997896f * LOG2E;

constexpr unsigned WS_EPOCH = 0;
constexpr unsigned FLAG_STRIDE = 64, FLAG_ARR = 256 * FLAG_STRIDE;
constexpr unsigned WS_F1 = 4096, WS_FP = WS_F1 + FLAG_ARR, WS_F2 = WS_FP + FLAG_ARR, WS_F3 = WS_F2 + FLAG_ARR, WS_F4 = WS_F3 + FLAG_ARR;
constexpr unsigned WS_ST1 = 131072;
constexpr unsigned WS_CSQ = WS_ST1 + 32768;
constexpr unsigned WS_PST = 262144;
constexpr unsigned WS_Q = 524288;
constexpr unsigned IMG = S * D * 2;
constexpr unsigned QK_TILE = 32 * 1536, QK_IMG = 64 * QK_TILE;
constexpr unsigned WS_K = WS_Q + QK_IMG;
constexpr unsigned WS_V = WS_K + QK_IMG;
constexpr unsigned WS_O = WS_V + IMG;
constexpr unsigned WS_H = WS_O + IMG;
constexpr unsigned WS_HH = WS_H + IMG;
constexpr unsigned WS_WO = WS_HH + S * F * 2;
constexpr unsigned WS_W13 = WS_WO + D * D * 2;
constexpr unsigned WS_W2 = WS_W13 + D * F * 4;
constexpr unsigned WS_END = WS_W2 + F * D * 2;

DI unsigned cvtpk(float lo, float hi) { f32x2 v = {lo, hi}; bf16x2_t b = __builtin_convertvector(v, bf16x2_t); return __builtin_bit_cast(unsigned, b); }
DI float bflo(unsigned u) { return __uint_as_float(u << 16); }
DI float bfhi(unsigned u) { return __uint_as_float(u & 0xffff0000u); }
DI f32x4 mfma16(bf16x8 a, bf16x8 b, f32x4 c) { return __builtin_amdgcn_mfma_f32_16x16x32_bf16(a, b, c, 0, 0, 0); }
DI f32x16 mfma32(bf16x8 a, bf16x8 b, f32x16 c) { return __builtin_amdgcn_mfma_f32_32x32x16_bf16(a, b, c, 0, 0, 0); }
DI int rfl(int x) { return __builtin_amdgcn_readfirstlane(x); }
template <bool SC1>
DI void glds16(unsigned voff, const void* sbase, unsigned ldsdst) {
    unsigned keep;
    if (SC1) asm volatile("s_mov_b32 %0, m0\n\ts_mov_b32 m0, %3\n\ts_nop 0\n\tglobal_load_lds_dwordx4 %1, %2 sc1\n\ts_mov_b32 m0, %0" : "=&s"(keep) : "v"(voff), "s"(sbase), "s"(ldsdst) : "memory");
    else asm volatile("s_mov_b32 %0, m0\n\ts_mov_b32 m0, %3\n\ts_nop 0\n\tglobal_load_lds_dwordx4 %1, %2\n\ts_mov_b32 m0, %0" : "=&s"(keep) : "v"(voff), "s"(sbase), "s"(ldsdst) : "memory");
}
DI void glds4(const void* gptr, unsigned ldsdst) {
    unsigned keep;
    asm volatile("s_mov_b32 %0, m0\n\ts_mov_b32 m0, %2\n\ts_nop 0\n\tglobal_load_lds_dword %1, off\n\ts_mov_b32 m0, %0" : "=&s"(keep) : "v"(gptr), "s"(ldsdst) : "memory");
}
#define WAIT_VM(n) asm volatile("s_waitcnt vmcnt(" #n ")" ::: "memory")
#define WAIT_BAR(n) asm volatile("s_waitcnt vmcnt(" #n ") lgkmcnt(0)\n\ts_barrier" ::: "memory")
#define BAR_L() asm volatile("s_waitcnt lgkmcnt(0)\n\ts_barrier" ::: "memory")
#define SBAR() __builtin_amdgcn_sched_barrier(0)
#define PIN(x) asm volatile("" : "+v"(x))
#define EX(v) __builtin_amdgcn_exp2f(v)
DI u32x4 lds_r128(lds_ptr p) { return *(const LAS u32x4*)p; }
DI s16x4 lds_tr(lds_ptr p) { return __builtin_bit_cast(s16x4, __builtin_amdgcn_ds_read_tr16_b64_v4i16((LAS v4i16_t*)p)); }
DI bf16x8 as_frag(u32x4 v) { return __builtin_bit_cast(bf16x8, v); }
DI bf16x8 cat_frag(s16x4 a, s16x4 b) { return (bf16x8){a[0], a[1], a[2], a[3], b[0], b[1], b[2], b[3]}; }
DI float swap32_max(float m) { auto r = __builtin_amdgcn_permlane32_swap(__float_as_uint(m), __float_as_uint(m), false, false); return fmaxf(__uint_as_float(r[0]), __uint_as_float(r[1])); }
DI u32x4 ld128(rsrc_t r, unsigned off) { return __builtin_amdgcn_raw_buffer_load_b128(r, off, 0, 16); }
DI u32x2 ld64(rsrc_t r, unsigned off) { return __builtin_amdgcn_raw_buffer_load_b64(r, off, 0, 16); }
DI unsigned ld32(rsrc_t r, unsigned off) { return __builtin_amdgcn_raw_buffer_load_b32(r, off, 0, 16); }
DI void st128(rsrc_t r, unsigned off, u32x4 v) { __builtin_amdgcn_raw_buffer_store_b128(v, r, off, 0, 16); }
DI void st64(rsrc_t r, unsigned off, u32x2 v) { __builtin_amdgcn_raw_buffer_store_b64(v, r, off, 0, 16); }

DI void ald128(u32x4& v, u32x4 rsw, unsigned voff, unsigned soff) { asm volatile("buffer_load_dwordx4 %0, %1, %2, %3 offen sc1" : "=&v"(v) : "v"(voff), "s"(rsw), "s"(soff) : "memory"); }
template <int N> DI void ald_wait(u32x4& v) { asm volatile("s_waitcnt vmcnt(%1)" : "+v"(v) : "i"(N) : "memory"); }
struct Ctx {
    const uint16_t *x, *wqkv, *wo, *w1, *w3, *w2;
    char* ws; uint16_t* out;
    rsrc_t rs; u32x4 rsw; u64 tag; lds_ptr lds; unsigned lds0;
    int c, xcd, lb, tid, lane, w;
    mutable unsigned sc, sr;
};

#define STAMP(i)
#define STAMP_FLUSH()

DI u64 ld_flag(const char* p) { return __hip_atomic_load((const GAS u64*)p, __ATOMIC_RELAXED, __HIP_MEMORY_SCOPE_AGENT); }
DI void st_flag(char* p, u64 v) { __hip_atomic_store((GAS u64*)p, v, __ATOMIC_RELAXED, __HIP_MEMORY_SCOPE_AGENT); }
constexpr unsigned SPIN_CAP = 1u << 18;
DI void poll_flags(const Ctx& k, unsigned off) {
    for (unsigned spins = 0; spins < SPIN_CAP; ++spins) {
        if (__all(ld_flag(k.ws + off) == k.tag)) break;
        __builtin_amdgcn_s_sleep(1);
    }
}
DI void poll_flags2(const Ctx& k, unsigned off0, unsigned off1) {
    for (unsigned spins = 0; spins < SPIN_CAP; ++spins) {
        const u64 a = ld_flag(k.ws + off0), b = ld_flag(k.ws + off1);
        if (__all(a == k.tag && b == k.tag)) break;
        __builtin_amdgcn_s_sleep(1);
    }
}
DI void poll_all(const Ctx& k, unsigned arr) {
    for (unsigned spins = 0; spins < SPIN_CAP; ++spins) {
        const unsigned o = arr + k.lane * FLAG_STRIDE;
        const u64 a = ld_flag(k.ws + o), b = ld_flag(k.ws + o + 64 * FLAG_STRIDE), c = ld_flag(k.ws + o + 128 * FLAG_STRIDE), d = ld_flag(k.ws + o + 192 * FLAG_STRIDE);
        if (__all(a == k.tag && b == k.tag && c == k.tag && d == k.tag)) break;
        __builtin_amdgcn_s_sleep(1);
    }
}

constexpr int SU_LDS = 131072;
DI void startup_load(const Ctx& k) { glds4(k.ws + WS_EPOCH + (k.lane & 1) * 4, k.lds0 + SU_LDS + k.w * 256); }
DI void startup_tag(Ctx& k) {
    const u32x2 e = *(const LAS u32x2*)(k.lds + SU_LDS + k.w * 256);
    k.tag = (((u64)(unsigned)rfl(e.y) << 32) | (unsigned)rfl(e.x)) + 1;
}
DI void ln1_stats(const Ctx& k, int j) {
    u32x4 v[2][10];
#pragma unroll
    for (int p = 0; p < 2; ++p)
#pragma unroll
        for (int i = 0; i < 10; ++i) v[p][i] = *(const u32x4*)(k.x + (size_t)(128 * j + 16 * k.w + 8 * p + (k.lane >> 3)) * D + ((k.lane & 7) + 8 * i) * 8);
#pragma unroll
    for (int p = 0; p < 2; ++p) {
        float s = 0.f, q = 0.f;
#pragma unroll
        for (int i = 0; i < 10; ++i)
#pragma unroll
            for (int e = 0; e < 4; ++e) { const float a = bflo(v[p][i][e]), b = bfhi(v[p][i][e]); s += a + b; q = fmaf(a, a, fmaf(b, b, q)); }
#pragma unroll
        for (int o = 4; o >= 1; o >>= 1) { s += __shfl_xor(s, o); q += __shfl_xor(q, o); }
        const float mean = s * (1.0f / D), var = fmaxf(q * (1.0f / D) - mean * mean, 0.f);
        const float val = (k.lane & 1) ? 1.0f / sqrtf(var + LN_EPS) : mean;
        const int row = 128 * j + 16 * k.w + 8 * p + (k.lane >> 3);
        if ((k.lane & 7) < 2) st64(k.rs, WS_ST1 + row * 16 + (k.lane & 1) * 8, (u32x2){__float_as_uint(val), (unsigned)k.tag});
    }
}
DI void colsum_qkv(const Ctx& k, int cb) {
    float a[8] = {0, 0, 0, 0, 0, 0, 0, 0};
    u32x4 v[20];
#pragma unroll
    for (int i = 0; i < 20; ++i) v[i] = *(const u32x4*)(k.wqkv + (size_t)(k.w * 80 + i * 4 + (k.lane >> 4)) * NQKV + cb * 128 + (k.lane & 15) * 8);
#pragma unroll
    for (int i = 0; i < 20; ++i)
#pragma unroll
        for (int j = 0; j < 4; ++j) { a[2 * j] += bflo(v[i][j]); a[2 * j + 1] += bfhi(v[i][j]); }
    LAS float* red = (LAS float*)k.lds;
#pragma unroll
    for (int j = 0; j < 8; ++j) { a[j] += __shfl_xor(a[j], 16); a[j] += __shfl_xor(a[j], 32); }
    if (k.lane < 16) {
#pragma unroll
        for (int j = 0; j < 8; ++j) red[k.w * 128 + k.lane * 8 + j] = a[j];
    }
    BAR_L();
    if (k.tid < 128) {
        float t = 0.f;
#pragma unroll
        for (int i = 0; i < 8; ++i) t += red[i * 128 + k.tid];
        st64(k.rs, WS_CSQ + (cb * 128 + k.tid) * 8, (u32x2){__float_as_uint(t), (unsigned)k.tag});
    }
}

constexpr int PT_LDS = 131072;
DI int pt_off(int i) { return PT_LDS + (i < 3 ? i * 8192 : 24576 + (i - 3) * 4096); }
DI void pretile_load(const Ctx& k) {
#pragma unroll
    for (int i = 0; i < 5; ++i) {
        const int u = k.c + 256 * i;
        if (u < 540) {
            const int s = u % 20, fb = u / 20;
            const uint16_t* src = ((k.lane & 1) ? k.w3 : k.w1) + fb * 64 + (k.lane >> 1) * 2;
#pragma unroll
            for (int j = 0; j < 4; ++j) { const int r = 4 * k.w + j; glds4(src + (size_t)(32 * s + r) * F, k.lds0 + pt_off(i) + r * 256); }
        } else if ((k.w >> 2) == (i & 1)) {
            const bool is2 = u < 1080;
            const int uu = is2 ? u - 540 : u - 1080, s = uu / 10, nb = uu % 10, r = 8 * (k.w & 3) + (k.lane >> 3);
            glds16<false>((32 * s + r) * (D * 2) + nb * 128 + (k.lane & 7) * 16, is2 ? k.w2 : k.wo, k.lds0 + pt_off(i) + (k.w & 3) * 1024);
        }
    }
}
struct PreTile { u32x4 v[5]; };
DI void pretile_read(const Ctx& k, PreTile& p) {
    const int g = k.lane >> 4, q = (k.lane >> 2) & 3, pp = k.lane & 3;
#pragma unroll
    for (int i = 0; i < 5; ++i) {
        const int u = k.c + 256 * i;
        const bool wide = u < 540;
        const int rs = wide ? 256 : 128, f = wide ? k.w : (k.w & 3);
        const lds_ptr a = k.lds + pt_off(i) + (8 * g + q) * rs + f * 32 + pp * 8;
        p.v[i] = __builtin_bit_cast(u32x4, cat_frag(lds_tr(a), lds_tr(a + 4 * rs)));
    }
}
DI void pretile_store(const Ctx& k, const PreTile& p) {
#pragma unroll
    for (int i = 0; i < 5; ++i) {
        const int u = k.c + 256 * i;
        if (u < 540) st128(k.rs, WS_W13 + (((u / 20) * 8 + k.w) * 20 + u % 20) * 1024 + k.lane * 16, p.v[i]);
        else if ((k.w >> 2) == (i & 1)) {
            const bool is2 = u < 1080;
            const int uu = is2 ? u - 540 : u - 1080, s = uu / 10, cfg = (uu % 10) * 4 + (k.w & 3), ct = cfg / 5, cf = cfg % 5;
            st128(k.rs, (is2 ? WS_W2 + ((ct * 54 + s) * 5 + cf) * 1024 : WS_WO + ((ct * 20 + s) * 5 + cf) * 1024) + k.lane * 16, p.v[i]);
        }
    }
}

constexpr int P1_SLOT = 32768;
struct P1Frag { bf16x8 a[4], b[2]; };
template <bool ISV> DI void p1_mma(f32x4 (&acc)[4][2], const P1Frag& f, int m, int n) { acc[m][n] = ISV ? mfma16(f.a[m], f.b[n], acc[m][n]) : mfma16(f.b[n], f.a[m], acc[m][n]); }
template <bool ISV>
DI void p1_body(Ctx& k, int rb, int cb) {
    const int lane = k.lane, w = k.w, th = w >> 2, cg = w & 3, t = lane & 15, g = lane >> 4;
    unsigned voffA[2], voffW[2];
#pragma unroll
    for (int i = 0; i < 2; ++i) {
        const int ra = 8 * (2 * w + i) + (lane >> 3), ca = (lane & 7) ^ ((ra >> 1) & 7);
        voffA[i] = ra * (D * 2) + ca * 16;
        const int rw = 4 * (2 * w + i) + (lane >> 4), fw = (rw & 3) | (((rw >> 3) & 1) << 2), cw = (lane & 15) ^ (fw << 1);
        voffW[i] = rw * (NQKV * 2) + cw * 16;
    }
    const char* gA = (const char*)k.x + (size_t)rb * 128 * D * 2;
    const char* gW = (const char*)k.wqkv + cb * 256;
    auto stage = [&](int kt) {
        const unsigned sl = k.lds0 + (kt & 3) * P1_SLOT + w * 2048;
#pragma unroll
        for (int i = 0; i < 2; ++i) glds16<false>(voffA[i], gA + kt * 128, sl + i * 1024);
#pragma unroll
        for (int i = 0; i < 2; ++i) glds16<false>(voffW[i], gW + (size_t)kt * 64 * NQKV * 2, sl + 16384 + i * 1024);
    };
    stage(0); stage(1); stage(2); stage(3);
    int aoff[2], woff[2];
    aoff[0] = (th * 64 + t) * 128 + ((g ^ (t >> 1)) << 4); aoff[1] = aoff[0] ^ 64;
    const int q4 = (lane >> 2) & 3, p = lane & 3, fw = q4 | ((g & 1) << 2);
#pragma unroll
    for (int n = 0; n < 2; ++n) woff[n] = 16384 + (8 * g + q4) * 256 + (((2 * cg + n) ^ fw) << 5) + p * 8;
    f32x4 acc[4][2];
#pragma unroll
    for (int m = 0; m < 4; ++m) { acc[m][0] = (f32x4){0, 0, 0, 0}; acc[m][1] = (f32x4){0, 0, 0, 0}; }
    P1Frag f0, f1;
#define P1_RA(F_, sl, ks, m) F_.a[m] = as_frag(lds_r128(sl + aoff[ks] + (m) * 2048))
#define P1_RB(F_, sl, ks, n) F_.b[n] = cat_frag(lds_tr(sl + woff[n] + (ks) * 8192), lds_tr(sl + woff[n] + (ks) * 8192 + 1024))
#define P1_STEP(RD, FN, sl, ks, FC) do { \
        if (RD) P1_RB(FN, sl, ks, 0); p1_mma<ISV>(acc, FC, 0, 0); SBAR(); \
        if (RD) P1_RA(FN, sl, ks, 0); p1_mma<ISV>(acc, FC, 0, 1); SBAR(); \
        if (RD) P1_RA(FN, sl, ks, 1); p1_mma<ISV>(acc, FC, 1, 0); SBAR(); \
        if (RD) P1_RB(FN, sl, ks, 1); p1_mma<ISV>(acc, FC, 1, 1); SBAR(); \
        if (RD) P1_RA(FN, sl, ks, 2); p1_mma<ISV>(acc, FC, 2, 0); SBAR(); \
        if (RD) P1_RA(FN, sl, ks, 3); p1_mma<ISV>(acc, FC, 2, 1); SBAR(); \
        p1_mma<ISV>(acc, FC, 3, 0); p1_mma<ISV>(acc, FC, 3, 1); SBAR(); } while (0)
    WAIT_BAR(12);
    STAMP(1);
    startup_tag(k);
    { const lds_ptr sl = k.lds; P1_RB(f0, sl, 0, 0); P1_RB(f0, sl, 0, 1); P1_RA(f0, sl, 0, 0); P1_RA(f0, sl, 0, 1); P1_RA(f0, sl, 0, 2); P1_RA(f0, sl, 0, 3); SBAR(); }
    u32x2 gr = {0, 0};
    const unsigned groff = k.tid < 256 ? WS_ST1 + (rb * 128) * 16 + k.tid * 8 : WS_CSQ + (cb * 128 + (k.tid & 127)) * 8;
#pragma unroll
    for (int kt = 0; kt < 10; ++kt) {
        const lds_ptr sl = k.lds + (kt & 3) * P1_SLOT, sn = k.lds + ((kt + 1) & 3) * P1_SLOT;
        P1_STEP(true, f1, sl, 1, f0);
        if (kt < 9) {
            if (kt < 7) WAIT_BAR(8); else if (kt == 7) WAIT_BAR(4); else WAIT_BAR(0);
            if (kt == 6) gr = ld64(k.rs, groff);
            if (kt + 4 < 10) stage(kt + 4);
            SBAR();
        }
        P1_STEP(kt < 9, f0, sn, 0, f1);
    }
#undef P1_RA
#undef P1_RB
#undef P1_STEP
    STAMP(2);
    for (unsigned spins = 0; spins < SPIN_CAP; ++spins) {
        if (__all(gr.y == (unsigned)k.tag)) break;
        gr = ld64(k.rs, groff);
    }
    LAS float* sst = (LAS float*)k.lds;
    if (k.tid < 384) sst[k.tid] = __uint_as_float(gr.x);
    BAR_L();
    const LAS f32x2* st = (const LAS f32x2*)sst + th * 64;
    const LAS float* cs = sst + 256 + cg * 32;
    if (!ISV) {
        const float osc = cb < 5 ? QSCALE : 1.0f;
        const unsigned img = cb < 5 ? WS_Q : WS_K;
        const int cbs = cb % 5;
        unsigned dst[2]; bool tail[2]; f32x4 c4[2];
#pragma unroll
        for (int n = 0; n < 2; ++n) {
            const int cgi = cbs * 32 + cg * 8 + n * 4 + g, hd = cgi / 5, gi = cgi - 5 * hd;
            const int blk = gi == 4 ? 4 : ((gi & 1) << 1) | (gi >> 1);
            dst[n] = img + (rb * 4 + th * 2) * QK_TILE + hd * 1536 + blk * 256 + t * 8;
            tail[n] = gi == 4;
            c4[n] = *(const LAS f32x4*)(cs + n * 16 + 4 * g);
        }
#pragma unroll
        for (int m = 0; m < 4; ++m) {
            const f32x2 sm = st[m * 16 + t];
            const float r = sm.y * osc;
#pragma unroll
            for (int n = 0; n < 2; ++n) {
                const f32x4 y = (acc[m][n] - c4[n] * sm.x) * r;
                const unsigned o = dst[n] + (m >> 1) * QK_TILE + (m & 1) * 128;
                st64(k.rs, o, (u32x2){cvtpk(y[0], y[1]), cvtpk(y[2], y[3])});
                if (tail[n]) st64(k.rs, o + 256, (u32x2){0x00003F80u, 0u});
            }
        }
    } else {
        const int pos = ((g & 1) << 1) | (g >> 1);
        const unsigned dst = WS_V + (rb * 8 + th * 4) * 20480 + ((cb - 10) * 128 + cg * 32 + t) * 32 + pos * 8;
        const float c1[2] = {cs[t], cs[16 + t]};
#pragma unroll
        for (int m = 0; m < 4; ++m) {
            f32x2 sm[4];
#pragma unroll
            for (int i = 0; i < 4; ++i) sm[i] = st[m * 16 + 4 * g + i];
#pragma unroll
            for (int n = 0; n < 2; ++n) {
                float y[4];
#pragma unroll
                for (int i = 0; i < 4; ++i) y[i] = (acc[m][n][i] - c1[n] * sm[i].x) * sm[i].y;
                st64(k.rs, dst + m * 20480 + n * 512, (u32x2){cvtpk(y[0], y[1]), cvtpk(y[2], y[3])});
            }
        }
    }
}

constexpr int AT_KB = 8 * 1536, AT_HALF = AT_KB + 10240, AT_BUF = 2 * AT_HALF;
constexpr int AT_ONES = AT_BUF, AT_ONES_BYTES = 10752, AT_FLAG = AT_ONES + AT_ONES_BYTES, WO_LDS = 56320, AT_RED = 0;
DI int at_slot(int i) { return i == 0 ? 0 : (i == 1 ? 73728 : 118784); }
struct AttnCtx {
    unsigned voff[6]; const char* gsrc; unsigned ldst, sstride; int npc;
    u32x4 qf0[2]; u32x2 qf1[2];
    int kofs, vlane; bool vdata;
};
DI void attn_stage(const AttnCtx& c, int sidx, int slot) {
#pragma unroll
    for (int i = 0; i < 5; ++i) glds16<true>(c.voff[i], c.gsrc + sidx * c.sstride, c.ldst + at_slot(slot) + i * 1024);
    if (c.npc == 6) glds16<true>(c.voff[5], c.gsrc + sidx * c.sstride, c.ldst + at_slot(slot) + 5 * 1024);
}
DI void attn_setup(const Ctx& k, AttnCtx& c, int h) {
    const int lane = k.lane, w = k.w, kh = w >> 2, r = lane & 31, hi = lane >> 5;
    const bool stV = (w >> 1) & 1;
    c.npc = stV ? 5 : 6;
#pragma unroll
    for (int i = 0; i < 6; ++i) {
        const int ci = 64 * (c.npc * (w & 1) + i) + lane;
        if (!stV) c.voff[i] = (ci / 96) * QK_TILE + (ci % 96) * 16;
        else { const int st = ci / 40, wi = ci % 40; c.voff[i] = st * 20480 + ((wi % 20) * 2 + wi / 20) * 16; }
    }
    c.gsrc = k.ws + (stV ? WS_V + h * 640 + kh * 64 * 20480 : WS_K + h * 1536 + kh * 32 * QK_TILE);
    c.sstride = stV ? 16 * 20480 : 8 * QK_TILE;
    c.ldst = k.lds0 + kh * AT_HALF + (stV ? AT_KB : 0) + (w & 1) * c.npc * 1024;
    c.kofs = kh * AT_HALF + hi * 256 + r * 8;
    const int rr = r >= 28 ? r - 12 : r;
    c.vdata = rr < 20;
    c.vlane = c.vdata ? kh * AT_HALF + AT_KB + hi * 320 + rr * 16 : AT_ONES + 128;
    for (int i = k.tid; i < AT_ONES_BYTES / 4; i += 512) ((LAS unsigned*)(k.lds + AT_ONES))[i] = 0x3F803F80u;
    if (k.tid == 0) *(LAS unsigned*)(k.lds + AT_FLAG) = 0;
}
DI void attn_first_loads(const Ctx& k, AttnCtx& c, int h, int qb) {
    const int qg = k.w & 3, r = k.lane & 31, hi = k.lane >> 5;
    attn_stage(c, 0, 0);
#pragma unroll
    for (int i = 0; i < 2; ++i) {
        const unsigned qp = WS_Q + (qb * 8 + qg * 2 + i) * QK_TILE + h * 1536 + r * 8;
        const u32x2 a = ld64(k.rs, qp + hi * 256), b = ld64(k.rs, qp + hi * 256 + 512), e = ld64(k.rs, qp + 1024);
        c.qf0[i] = (u32x4){a.x, a.y, b.x, b.y};
        c.qf1[i] = hi ? (u32x2){0, 0} : e;
    }
    attn_stage(c, 1, 1);
}
DI bf16x8 kfrag0(lds_ptr p) { const u32x2 a = *(const LAS u32x2*)p, b = *(const LAS u32x2*)(p + 512); return as_frag((u32x4){a.x, a.y, b.x, b.y}); }
DI s16x4 kfrag1(lds_ptr p) { return __builtin_bit_cast(s16x4, *(const LAS u32x2*)(p + 1024)); }
DI s16x4 as_frag4(u32x2 v) { return __builtin_bit_cast(s16x4, v); }
DI f32x16 mfma32x8(s16x4 a, s16x4 b, f32x16 c) { return __builtin_amdgcn_mfma_f32_32x32x8bf16_1k(a, b, c, 0, 0, 0); }
DI void wo_prefetch(const Ctx& k, int ct) {
    for (int pc = k.w; pc < 100; pc += 8) glds16<true>(k.lane * 16, k.ws + WS_WO + ct * 102400 + pc * 1024, k.lds0 + WO_LDS + pc * 1024);
}
DI void attn_fast(const Ctx& k, AttnCtx& c, f32x16& oA, f32x16& oB, int ct3, int f4) {
    const lds_ptr lds = k.lds;
    const int hi = k.lane >> 5, kh = k.w >> 2;
    {
        const bf16x8 k0 = kfrag0(lds + c.kofs - kh * AT_HALF); const s16x4 k1 = kfrag1(lds + c.kofs - kh * AT_HALF);
#pragma unroll
        for (int i = 0; i < 2; ++i) {
            f32x16 s = mfma32(k0, as_frag(c.qf0[i]), (f32x16){});
            s = mfma32x8(k1, as_frag4(c.qf1[i]), s);
            float m = s[0];
#pragma unroll
            for (int j = 1; j < 16; ++j) m = fmaxf(m, s[j]);
            m = swap32_max(m);
            if (hi) c.qf1[i].x = cvtpk(-m, 0.f) & 0xffffu;
        }
    }
    const bf16x8 qA0 = as_frag(c.qf0[0]), qB0 = as_frag(c.qf0[1]); const s16x4 qA1 = as_frag4(c.qf1[0]), qB1 = as_frag4(c.qf1[1]);
    f32x16 sA, sB, nA, nB;
    oA = (f32x16){}; oB = (f32x16){};
    bf16x8 kf0 = kfrag0(lds + c.kofs); s16x4 kf1 = kfrag1(lds + c.kofs);
    sA = mfma32(kf0, qA0, (f32x16){}); sB = mfma32(kf0, qB0, (f32x16){});
    sA = mfma32x8(kf1, qA1, sA); sB = mfma32x8(kf1, qB1, sB);
    kf0 = kfrag0(lds + c.kofs + 1536); kf1 = kfrag1(lds + c.kofs + 1536);
    u32x4 pA0, pA1, pB0, pB1 = {0, 0, 0, 0};
    bf16x8 vf0, vf1 = {0, 0, 0, 0, 0, 0, 0, 0};
    unsigned warm = 0;
    u32x2 fa = {0, 0}, fb = fa, fc = fa, fd = fa;
#define GAP(MF, X, i, P, j) do { MF; SBAR(); X[i] = EX(X[i]); X[i + 1] = EX(X[i + 1]); X[i + 2] = EX(X[i + 2]); X[i + 3] = EX(X[i + 3]); \
        P[j] = cvtpk(X[i], X[i + 1]); P[j + 1] = cvtpk(X[i + 2], X[i + 3]); PIN(P); SBAR(); } while (0)
#define STEP(SA, SB, NA, NB, VP, KP) do { \
        GAP(NA = mfma32(kf0, qA0, (f32x16){}), SA, 0, pA0, 0); vf0 = as_frag(lds_r128(VP)); SBAR(); \
        GAP(oB = mfma32(vf1, as_frag(pB1), oB), SA, 4, pA0, 2); vf1 = as_frag(lds_r128((VP) + 640)); SBAR(); \
        GAP(NB = mfma32(kf0, qB0, (f32x16){}), SA, 8, pA1, 0); \
        GAP(NA = mfma32x8(kf1, qA1, NA), SA, 12, pA1, 2); \
        GAP(NB = mfma32x8(kf1, qB1, NB), SB, 0, pB0, 0); kf0 = kfrag0(KP); kf1 = kfrag1(KP); SBAR(); \
        GAP(oA = mfma32(vf0, as_frag(pA0), oA), SB, 4, pB0, 2); \
        GAP(oA = mfma32(vf1, as_frag(pA1), oA), SB, 8, pB1, 0); \
        GAP(oB = mfma32(vf0, as_frag(pB0), oB), SB, 12, pB1, 2); \
    } while (0)
    int slot = 0;
    for (int sidx = 0; sidx < 4; ++sidx) {
        const int nslot = slot == 2 ? 0 : slot + 1;
        const int sb = at_slot(slot), sn = at_slot(nslot);
        const lds_ptr vb = lds + (c.vdata ? c.vlane + sb : c.vlane);
        lds_ptr k0 = lds + c.kofs + sb, k0n = lds + c.kofs + sn;
        PIN(k0); PIN(k0n);
        STEP(sA, sB, nA, nB, vb, k0 + 2 * 1536);
        STEP(nA, nB, sA, sB, vb + 1280, k0 + 3 * 1536);
        STEP(sA, sB, nA, nB, vb + 2 * 1280, k0 + 4 * 1536);
        if (sidx == 1 && k.w == 0) {
            const unsigned o = WS_FP + k.lane * FLAG_STRIDE;
            fa = ld64(k.rs, o); fb = ld64(k.rs, o + 64 * FLAG_STRIDE); fc = ld64(k.rs, o + 128 * FLAG_STRIDE); fd = ld64(k.rs, o + 192 * FLAG_STRIDE);
        }
        STEP(nA, nB, sA, sB, vb + 3 * 1280, k0 + 5 * 1536);
        STEP(sA, sB, nA, nB, vb + 4 * 1280, k0 + 6 * 1536);
        STEP(nA, nB, sA, sB, vb + 5 * 1280, k0 + 7 * 1536);
        if (sidx == 1 && k.w == 0) {
            const unsigned tl = (unsigned)k.tag, th = (unsigned)(k.tag >> 32);
            if (!__all(fa.x == tl && fa.y == th && fb.x == tl && fb.y == th && fc.x == tl && fc.y == th && fd.x == tl && fd.y == th)) poll_all(k, WS_FP);
        }
        if (sidx == 0) STAMP(20); else if (sidx == 1) STAMP(22); else if (sidx == 2) STAMP(24); else STAMP(26);
        WAIT_BAR(0);
        if (sidx == 0) STAMP(21); else if (sidx == 1) STAMP(23); else if (sidx == 2) STAMP(25); else STAMP(27);
        if (sidx < 2) attn_stage(c, sidx + 2, nslot == 2 ? 0 : nslot + 1);
        if (sidx == 0 && k.tid == 0) st_flag(k.ws + WS_FP + k.c * FLAG_STRIDE, k.tag);
        if (sidx == 1) {
            const unsigned o = WS_W13 + f4 * 20480 + (k.lb >> 4) * 71680 + (k.w * 64 + k.lane) * 128;
            warm = ld32(k.rs, o);
            if (k.w == 0 && k.lane < 48) warm ^= ld32(k.rs, o + 65536);
        }
        if (sidx == 3) wo_prefetch(k, ct3);
        STEP(sA, sB, nA, nB, vb + 6 * 1280, k0n);
        STEP(nA, nB, sA, sB, vb + 7 * 1280, k0n + 1536);
        slot = nslot;
    }
    oB = mfma32(vf1, as_frag(pB1), oB);
    asm volatile("" :: "v"(warm));
#undef GAP
#undef STEP
}
DI void attn_safe(const Ctx& k, AttnCtx& c, f32x16 (&o)[2], float (&mrun)[2]) {
    const lds_ptr lds = k.lds;
    attn_stage(c, 0, 0);
    if (k.lane >> 5) { c.qf1[0].x = 0; c.qf1[1].x = 0; }
    o[0] = (f32x16){}; o[1] = (f32x16){};
    mrun[0] = -INFINITY; mrun[1] = -INFINITY;
    for (int sidx = 0; sidx < 4; ++sidx) {
        const int sb = at_slot(sidx & 1);
        WAIT_BAR(0);
        if (sidx < 3) attn_stage(c, sidx + 1, (sidx + 1) & 1);
        const lds_ptr vb = lds + (c.vdata ? c.vlane + sb : c.vlane);
        for (int kt = 0; kt < 8; ++kt) {
            const bf16x8 k0 = kfrag0(lds + c.kofs + sb + kt * 1536); const s16x4 k1 = kfrag1(lds + c.kofs + sb + kt * 1536);
            const bf16x8 v0 = as_frag(lds_r128(vb + kt * 1280)), v1 = as_frag(lds_r128(vb + kt * 1280 + 640));
#pragma unroll
            for (int i = 0; i < 2; ++i) {
                f32x16 s = mfma32(k0, as_frag(c.qf0[i]), (f32x16){});
                s = mfma32x8(k1, as_frag4(c.qf1[i]), s);
                float m = s[0];
#pragma unroll
                for (int j = 1; j < 16; ++j) m = fmaxf(m, s[j]);
                m = fmaxf(swap32_max(m), mrun[i]);
                const float alpha = EX(mrun[i] - m);
                mrun[i] = m;
                unsigned pk[8];
#pragma unroll
                for (int j = 0; j < 16; ++j) o[i][j] *= alpha;
#pragma unroll
                for (int j = 0; j < 8; ++j) pk[j] = cvtpk(EX(s[2 * j] - m), EX(s[2 * j + 1] - m));
                o[i] = mfma32(v0, as_frag((u32x4){pk[0], pk[1], pk[2], pk[3]}), o[i]);
                o[i] = mfma32(v1, as_frag((u32x4){pk[4], pk[5], pk[6], pk[7]}), o[i]);
            }
        }
    }
}
DI void p2_phase(const Ctx& k, AttnCtx& c, int h, int qb, int ct3, int f4) {
    const int lane = k.lane, qg = k.w & 3, kh = k.w >> 2, r = lane & 31, hi = lane >> 5;
    const lds_ptr lds = k.lds;
    attn_first_loads(k, c, h, qb);
    PreTile pt;
    pretile_read(k, pt);
    LAS float* red = (LAS float*)(lds + AT_RED) + qg * (2 * 13 * 64);
    LAS unsigned* flag = (LAS unsigned*)(lds + AT_FLAG);
    if (c.npc == 6) WAIT_VM(6); else WAIT_VM(5);
    BAR_L();
    STAMP(6);
    pretile_store(k, pt);
    f32x16 o[2];
    attn_fast(k, c, o[0], o[1], ct3, f4);
    STAMP(7);
    BAR_L();
    if (kh == 1) {
#pragma unroll
        for (int i = 0; i < 2; ++i)
#pragma unroll
            for (int j = 0; j < 12; ++j) red[(i * 13 + j) * 64 + lane] = o[i][j];
    }
    BAR_L();
    if (kh == 0) {
        bool bad = false;
#pragma unroll
        for (int i = 0; i < 2; ++i) {
#pragma unroll
            for (int j = 0; j < 12; ++j) o[i][j] += red[(i * 13 + j) * 64 + lane];
            bad |= hi && !(o[i][8] <= 1.152921504606847e18f);
        }
        if (__any(bad) && lane == 0) *flag = 1;
    }
    BAR_L();
    if (*flag) {
        float mref[2];
        WAIT_BAR(0);
        attn_safe(k, c, o, mref);
        BAR_L();
        if (kh == 1) {
#pragma unroll
            for (int i = 0; i < 2; ++i) {
#pragma unroll
                for (int j = 0; j < 12; ++j) red[(i * 13 + j) * 64 + lane] = o[i][j];
                red[(i * 13 + 12) * 64 + lane] = mref[i];
            }
        }
        BAR_L();
        wo_prefetch(k, ct3);
        if (kh == 0) {
#pragma unroll
            for (int i = 0; i < 2; ++i) {
                const float m1 = red[(i * 13 + 12) * 64 + lane], m = fmaxf(mref[i], m1);
                const float a0 = EX(mref[i] - m), a1 = EX(m1 - m);
#pragma unroll
                for (int j = 0; j < 12; ++j) o[i][j] = o[i][j] * a0 + red[(i * 13 + j) * 64 + lane] * a1;
            }
        }
    }
    if (kh == 0) {
#pragma unroll
        for (int i = 0; i < 2; ++i) {
            auto lr = __builtin_amdgcn_permlane32_swap(__float_as_uint(o[i][8]), __float_as_uint(o[i][8]), false, false);
            const float inv = 1.0f / __uint_as_float(lr[1]);
            const int tok = qb * 256 + qg * 64 + i * 32 + r;
            const unsigned base = WS_O + (tok >> 4) * 20480 + (tok & 15) * 16;
            unsigned pk[3][2];
#pragma unroll
            for (int u = 0; u < 3; ++u) { pk[u][0] = cvtpk(o[i][4 * u] * inv, o[i][4 * u + 1] * inv); pk[u][1] = cvtpk(o[i][4 * u + 2] * inv, o[i][4 * u + 3] * inv); }
            auto oaddr = [&](int col) { return base + (col >> 5) * 1024 + ((col >> 3) & 3) * 256 + (col & 7) * 2; };
            if ((h & 1) == 0) {
                auto s0 = __builtin_amdgcn_permlane32_swap(pk[0][0], pk[1][0], false, false);
                auto s1 = __builtin_amdgcn_permlane32_swap(pk[0][1], pk[1][1], false, false);
                st128(k.rs, oaddr(h * 20 + 8 * hi), (u32x4){s0[0], s1[0], s0[1], s1[1]});
                if (!hi) st64(k.rs, oaddr(h * 20 + 16), (u32x2){pk[2][0], pk[2][1]});
            } else {
                auto s0 = __builtin_amdgcn_permlane32_swap(pk[0][0], pk[2][0], false, false);
                auto s1 = __builtin_amdgcn_permlane32_swap(pk[0][1], pk[2][1], false, false);
                st128(k.rs, oaddr(h * 20 + 4 + 8 * hi), hi ? (u32x4){pk[1][0], pk[1][1], s0[0], s1[0]} : (u32x4){s0[1], s1[1], pk[1][0], pk[1][1]});
                if (!hi) st64(k.rs, oaddr(h * 20), (u32x2){s0[0], s1[0]});
            }
        }
    }
}

DI int own_cf(int kh, int u) { return kh ? (u < 2 ? 3 + u : 4) : u; }
DI void pair_reduce(lds_ptr red0, int tt, int kh, int lane, const f32x4 (&acc)[5], f32x4 (&own)[3]) {
    const lds_ptr red = red0 + tt * 5120 + lane * 16;
    if (kh == 0) { *(LAS f32x4*)(red + 3072) = acc[3]; *(LAS f32x4*)(red + 4096) = acc[4]; }
    else { *(LAS f32x4*)(red) = acc[0]; *(LAS f32x4*)(red + 1024) = acc[1]; *(LAS f32x4*)(red + 2048) = acc[2]; }
    BAR_L();
    if (kh == 0) {
#pragma unroll
        for (int u = 0; u < 3; ++u) own[u] = acc[u] + *(const LAS f32x4*)(red + u * 1024);
    } else {
        own[0] = acc[3] + *(const LAS f32x4*)(red + 3072); own[1] = acc[4] + *(const LAS f32x4*)(red + 4096); own[2] = own[1];
    }
}
struct P3Pre { u32x2 xv[3]; };
DI void p3_pre(const Ctx& k, int rb64, int ct, P3Pre& p) {
    const int tt = k.w & 3, kh = k.w >> 2, t = k.lane & 15, g = k.lane >> 4;
#pragma unroll
    for (int u = 0; u < 3; ++u) p.xv[u] = *(const u32x2*)(k.x + (size_t)(rb64 * 64 + tt * 16 + t) * D + ct * 80 + own_cf(kh, u) * 16 + 4 * g);
}
DI void p3_phase(const Ctx& k, int rb64, int ct, const P3Pre& pre, u32x2 (&hv)[3]) {
    const int lane = k.lane, tt = k.w & 3, kh = k.w >> 2, t = lane & 15, g = lane >> 4;
    const unsigned ap = WS_O + (rb64 * 4 + tt) * 20480 + kh * 10240 + lane * 16;
    const lds_ptr wl = k.lds + WO_LDS + kh * 51200 + lane * 16;
    constexpr int WIN = 4;
    u32x4 a[10];
#pragma unroll
    for (int i = 0; i < WIN; ++i) a[i] = ld128(k.rs, ap + i * 1024);
    bf16x8 wf[2][5];
#pragma unroll
    for (int cf = 0; cf < 5; ++cf) wf[0][cf] = as_frag(lds_r128(wl + cf * 1024));
    f32x4 acc[5];
#pragma unroll
    for (int cf = 0; cf < 5; ++cf) acc[cf] = (f32x4){0, 0, 0, 0};
    SBAR();
#pragma unroll
    for (int i = 0; i < 10; ++i) {
        if (i + WIN < 10) { a[i + WIN] = ld128(k.rs, ap + (i + WIN) * 1024); SBAR(); }
#pragma unroll
        for (int cf = 0; cf < 5; ++cf) {
            if (i + 1 < 10) wf[(i + 1) & 1][cf] = as_frag(lds_r128(wl + (i + 1) * 5120 + cf * 1024));
            acc[cf] = mfma16(wf[i & 1][cf], as_frag(a[i]), acc[cf]);
            SBAR();
        }
    }
    STAMP(10);
    f32x4 own[3];
    pair_reduce(k.lds, tt, kh, lane, acc, own);
    const int tok = rb64 * 64 + tt * 16 + t;
    float s = 0.f, q = 0.f;
#pragma unroll
    for (int u = 0; u < 3; ++u) {
        hv[u] = (u32x2){cvtpk(own[u][0] + bflo(pre.xv[u].x), own[u][1] + bfhi(pre.xv[u].x)), cvtpk(own[u][2] + bflo(pre.xv[u].y), own[u][3] + bfhi(pre.xv[u].y))};
        if (u < 2 || kh == 0) {
            const int col = ct * 80 + own_cf(kh, u) * 16 + 4 * g;
            st64(k.rs, WS_H + (tok >> 4) * 20480 + (col >> 5) * 1024 + ((col >> 3) & 3) * 256 + t * 16 + (col & 7) * 2, hv[u]);
            const float h0 = bflo(hv[u].x), h1 = bfhi(hv[u].x), h2 = bflo(hv[u].y), h3 = bfhi(hv[u].y);
            s += (h0 + h1) + (h2 + h3); q += (h0 * h0 + h1 * h1) + (h2 * h2 + h3 * h3);
        }
    }
    s += __shfl_xor(s, 16); q += __shfl_xor(q, 16); s += __shfl_xor(s, 32); q += __shfl_xor(q, 32);
    if (g == 0) st64(k.rs, WS_PST + tok * 128 + (ct * 2 + kh) * 8, (u32x2){__float_as_uint(s), __float_as_uint(q)});
}
template <int N, int NP>
DI void w2_stage(const Ctx& k, int ct, int ldsoff, int s0) {
#pragma unroll
    for (int j = 0; j < NP; ++j) {
        int pc = k.w + 8 * j;
        if (pc >= 10 * N) pc -= 8;
        const int hf = pc >= 5 * N, rem = pc - hf * 5 * N;
        glds16<true>(k.lane * 16, k.ws + WS_W2 + (ct * 54 + hf * 27 + s0) * 5120 + rem * 1024, k.lds0 + ldsoff + pc * 1024);
    }
}
constexpr int P5_SLA = 0, P5_SLB = 71680, P5_RED = 143360;
DI void w2_prefetch(const Ctx& k, int ct) {
#pragma unroll 4
    for (int j = 0; j < 20; ++j) {
        const int pc = (k.w - 1) * 20 + j, blk = pc / 35;
        glds16<true>(k.lane * 16, k.ws + WS_W2 + (ct * 54 + (blk & 1) * 27 + (blk >> 1) * 7) * 5120 + (pc - blk * 35) * 1024, k.lds0 + pc * 1024);
    }
}
DI void p5_phase(const Ctx& k, int rb64, int ct, const u32x2 (&hv)[3]) {
    const int lane = k.lane, tt = k.w & 3, kh = k.w >> 2, t = lane & 15, g = lane >> 4;
    const unsigned ap = WS_HH + (rb64 * 4 + tt) * 55296 + kh * 27 * 1024 + lane * 16;
    constexpr int WIN = 6;
    u32x4 a[27];
#pragma unroll
    for (int i = 0; i < WIN; ++i) ald128(a[i], k.rsw, ap, i * 1024);
    const lds_ptr wa = k.lds + P5_SLA + kh * 35840 + lane * 16, wb = k.lds + P5_SLB + kh * 35840 + lane * 16;
    const lds_ptr wb6 = k.lds + P5_SLB + kh * 30720 + lane * 16;
    bf16x8 wf[2][5];
#pragma unroll
    for (int cf = 0; cf < 5; ++cf) wf[0][cf] = as_frag(lds_r128(wa + cf * 1024));
    f32x4 acc[5];
#pragma unroll
    for (int cf = 0; cf < 5; ++cf) acc[cf] = (f32x4){0, 0, 0, 0};
    SBAR();
#define P5_STEP(i, NW) do { \
        if ((i) == 7) { BAR_L(); w2_stage<7, 9>(k, ct, P5_SLA, 14); } \
        if ((i) == 13) { WAIT_BAR(6); w2_stage<6, 8>(k, ct, P5_SLB, 21); } \
        if ((i) == 20) { WAIT_BAR(6); } \
        if ((i) + WIN < 27) ald128(a[(i) + WIN], k.rsw, ap, ((i) + WIN) * 1024); \
        ald_wait<NW>(a[i]); SBAR(); \
        _Pragma("unroll") for (int cf = 0; cf < 5; ++cf) { \
            if ((i) + 1 < 27) { const int n_ = (i) + 1; wf[n_ & 1][cf] = as_frag(lds_r128((n_ < 7 ? wa + n_ * 5120 : n_ < 14 ? wb + (n_ - 7) * 5120 : n_ < 21 ? wa + (n_ - 14) * 5120 : wb6 + (n_ - 21) * 5120) + cf * 1024)); } \
            acc[cf] = mfma16(wf[(i) & 1][cf], as_frag(a[i]), acc[cf]); SBAR(); } } while (0)
    P5_STEP(0, 6); P5_STEP(1, 6); P5_STEP(2, 6); P5_STEP(3, 6); P5_STEP(4, 6); P5_STEP(5, 6); P5_STEP(6, 6);
    P5_STEP(7, 15); P5_STEP(8, 15); P5_STEP(9, 15); P5_STEP(10, 15); P5_STEP(11, 15); P5_STEP(12, 15);
    P5_STEP(13, 14); P5_STEP(14, 14); P5_STEP(15, 14); P5_STEP(16, 14); P5_STEP(17, 14); P5_STEP(18, 14); P5_STEP(19, 6);
    P5_STEP(20, 6); P5_STEP(21, 5); P5_STEP(22, 4); P5_STEP(23, 3); P5_STEP(24, 2); P5_STEP(25, 1); P5_STEP(26, 0);
#undef P5_STEP
    STAMP(16);
    f32x4 own[3];
    pair_reduce(k.lds + P5_RED, tt, kh, lane, acc, own);
    const int tok = rb64 * 64 + tt * 16 + t;
#pragma unroll
    for (int u = 0; u < 3; ++u) {
        if (u < 2 || kh == 0) {
            const int col = ct * 80 + own_cf(kh, u) * 16 + 4 * g;
            *(u32x2*)(k.out + (size_t)tok * D + col) = (u32x2){cvtpk(own[u][0] + bflo(hv[u].x), own[u][1] + bfhi(hv[u].x)), cvtpk(own[u][2] + bflo(hv[u].y), own[u][3] + bfhi(hv[u].y))};
        }
    }
}

constexpr int P4_CS = 143360;
DI void w13_prefetch(const Ctx& k, int f0) {
#pragma unroll 4
    for (int j = 0; j < 20; ++j) { const int pc = (k.w - 1) * 20 + j; glds16<true>(k.lane * 16, k.ws + WS_W13 + f0 * 20480 + pc * 1024, k.lds0 + pc * 1024); }
}
DI void p4_colsum(const Ctx& k, int f) {
    const int t = k.lane & 15, g = k.lane >> 4;
    u32x4 wv[20];
#pragma unroll
    for (int s = 0; s < 20; ++s) wv[s] = lds_r128(k.lds + (f * 20 + s) * 1024 + k.lane * 16);
    f32x4 c0 = {0, 0, 0, 0}, c1 = {0, 0, 0, 0};
    const bf16x8 ones = {0x3F80, 0x3F80, 0x3F80, 0x3F80, 0x3F80, 0x3F80, 0x3F80, 0x3F80};
#pragma unroll
    for (int s = 0; s < 20; s += 2) { c0 = mfma16(as_frag(wv[s]), ones, c0); c1 = mfma16(as_frag(wv[s + 1]), ones, c1); }
    if (t == 0) *(LAS f32x4*)(k.lds + P4_CS + f * 64 + g * 16) = c0 + c1;
}
DI void p4_phase(const Ctx& k, int rb, int f0, int nf, int ct5) {
    const int lane = k.lane, w = k.w, t = lane & 15, g = lane >> 4;
    const lds_ptr lds = k.lds;
    f32x4 pv[2][2];
#pragma unroll
    for (int i = 0; i < 2; ++i) {
        const unsigned o = WS_PST + (rb * 256 + w * 32 + i * 16 + t) * 128 + g * 32;
        pv[i][0] = __builtin_bit_cast(f32x4, ld128(k.rs, o)); pv[i][1] = __builtin_bit_cast(f32x4, ld128(k.rs, o + 16));
    }
    const unsigned ap = WS_H + (rb * 16 + w * 2) * 20480 + lane * 16;
    constexpr int WIN = 4;
    u32x4 a0[20], a1[20];
#pragma unroll
    for (int s = 0; s < WIN; ++s) { a0[s] = ld128(k.rs, ap + s * 1024); a1[s] = ld128(k.rs, ap + 20480 + s * 1024); }
    SBAR();
    if (w < 7) p4_colsum(k, w);
    BAR_L();
    lds_ptr wb[3] = {lds + lane * 16, lds + lane * 16 + 61440, lds + lane * 16 + 122880};
    PIN(wb[1]); PIN(wb[2]);
    bf16x8 wf[2][7];
#pragma unroll
    for (int f = 0; f < 7; ++f) wf[0][f] = as_frag(lds_r128(wb[f / 3] + (f % 3) * 20480));
    f32x4 cs[7];
#pragma unroll
    for (int f = 0; f < 7; ++f) cs[f] = *(const LAS f32x4*)(lds + P4_CS + f * 64 + g * 16);
    SBAR();
    float mean[2], rstd[2];
#pragma unroll
    for (int i = 0; i < 2; ++i) {
        float s = (pv[i][0][0] + pv[i][0][2]) + (pv[i][1][0] + pv[i][1][2]), q = (pv[i][0][1] + pv[i][0][3]) + (pv[i][1][1] + pv[i][1][3]);
        s += __shfl_xor(s, 16); q += __shfl_xor(q, 16); s += __shfl_xor(s, 32); q += __shfl_xor(q, 32);
        mean[i] = s * (1.0f / D);
        rstd[i] = __builtin_amdgcn_rsqf(fmaxf(q * (1.0f / D) - mean[i] * mean[i], 0.f) + LN_EPS);
    }
    f32x4 acc[2][7];
#pragma unroll
    for (int f = 0; f < 7; ++f) { acc[0][f] = cs[f] * (-mean[0]); acc[1][f] = cs[f] * (-mean[1]); }
    SBAR();
    STAMP(13);
    unsigned warm = 0;
#pragma unroll
    for (int s = 0; s < 20; ++s) {
        if (s + WIN < 20) { a0[s + WIN] = ld128(k.rs, ap + (s + WIN) * 1024); a1[s + WIN] = ld128(k.rs, ap + 20480 + (s + WIN) * 1024); SBAR(); }
        if (s == 16) {
            warm = ld32(k.rs, WS_W2 + ct5 * 276480 + (k.lb >> 3) * 69120 + (w * 64 + lane) * 128);
            if (w == 0 && lane < 28) warm ^= ld32(k.rs, WS_W2 + ct5 * 276480 + (k.lb >> 3) * 69120 + 65536 + lane * 128);
            SBAR();
        }
#pragma unroll
        for (int f = 0; f < 7; ++f) {
            if (s + 1 < 20) wf[(s + 1) & 1][f] = as_frag(lds_r128(wb[f / 3] + (f % 3) * 20480 + (s + 1) * 1024));
            acc[0][f] = mfma16(wf[s & 1][f], as_frag(a0[s]), acc[0][f]);
            acc[1][f] = mfma16(wf[s & 1][f], as_frag(a1[s]), acc[1][f]);
            SBAR();
        }
    }
    STAMP(14);
#pragma unroll
    for (int i = 0; i < 2; ++i) {
        const float ce = -LOG2E * rstd[i], r2 = rstd[i] * rstd[i];
        unsigned d[8];
#pragma unroll
        for (int f = 0; f < 7; ++f) {
            float o2[2];
#pragma unroll
            for (int j = 0; j < 2; ++j) {
                const float gt = acc[i][f][j], up = acc[i][f][2 + j];
                o2[j] = (gt * up) * r2 * __builtin_amdgcn_rcpf(1.0f + EX(ce * gt));
            }
            d[f] = cvtpk(o2[0], o2[1]);
        }
        d[7] = 0;
        const unsigned dst = WS_HH + (rb * 16 + w * 2 + i) * 55296 + f0 * 256 + lane * 16;
#pragma unroll
        for (int u = 0; u < 2; ++u) {
            auto a = __builtin_amdgcn_permlane32_swap(d[4 * u], d[4 * u + 2], false, false);
            auto b = __builtin_amdgcn_permlane32_swap(d[4 * u + 1], d[4 * u + 3], false, false);
            auto e = __builtin_amdgcn_permlane16_swap(a[0], b[0], false, false);
            auto f = __builtin_amdgcn_permlane16_swap(a[1], b[1], false, false);
            if (4 * u + g < nf) st128(k.rs, dst + u * 1024, (u32x4){e[0], e[1], f[0], f[1]});
        }
    }
    asm volatile("" :: "v"(warm));
}

constexpr int LDS_BYTES = 163840;
__global__ __launch_bounds__(512, 2) void fused_block(const uint16_t* __restrict__ x, const uint16_t* __restrict__ wqkv, const uint16_t* __restrict__ wo,
                                                      const uint16_t* __restrict__ w1, const uint16_t* __restrict__ w3, const uint16_t* __restrict__ w2,
                                                      char* __restrict__ ws, uint16_t* __restrict__ out) {
    extern __shared__ __attribute__((aligned(16))) char smem[];
    Ctx k;
    k.x = x; k.wqkv = wqkv; k.wo = wo; k.w1 = w1; k.w3 = w3; k.w2 = w2; k.ws = ws; k.out = out;
    k.rs = __builtin_amdgcn_make_buffer_rsrc(ws, 0, WS_END + 20480, 0x00020000);
    k.lds = (lds_ptr)smem; k.lds0 = (unsigned)(uintptr_t)smem;
    k.c = blockIdx.x; k.xcd = k.c & 7; k.lb = k.c >> 3;
    k.tid = threadIdx.x; k.lane = k.tid & 63; k.w = rfl(k.tid >> 6);
    k.rsw = (u32x4){(unsigned)(uintptr_t)ws, (unsigned)((uintptr_t)ws >> 32) & 0xffffu, WS_END + 20480, 0x00020000u};
    k.tag = 0;
    k.sc = 0; k.sr = 0;
    STAMP(0);
    startup_load(k);
    const int rb1 = (k.xcd >> 2) * 8 + (k.lb & 7), cb1 = (k.xcd & 3) * 4 + (k.lb >> 3);
    if (cb1 < 15) {
        if (cb1 >= 10) p1_body<true>(k, rb1, cb1); else p1_body<false>(k, rb1, cb1);
        STAMP(3);
        WAIT_BAR(0);
        if (k.tid == 0) st_flag(ws + WS_F1 + (rb1 * 16 + cb1) * FLAG_STRIDE, k.tag);
    } else {
        WAIT_VM(0);
        startup_tag(k);
        ln1_stats(k, rb1);
        if (rb1 < 15) colsum_qkv(k, rb1);
        WAIT_BAR(0);
    }
    STAMP(4);
    pretile_load(k);
    const int h = k.xcd * 4 + (k.lb & 3), qb = k.lb >> 2;
    const int rb64 = k.xcd * 4 + (k.lb >> 3), ct = k.lb & 7;
    const int rb4 = (k.xcd >> 1) * 2 + (k.lb >> 4), cb4 = (k.xcd & 1) * 16 + (k.lb & 15);
    const int f0 = cb4 < 24 ? cb4 * 7 : 168 + (cb4 - 24) * 6, nf = cb4 < 24 ? 7 : 6;
    AttnCtx ac;
    attn_setup(k, ac, h);
    if (k.w == 0) {
        const int c0 = (20 * h) >> 7, c1 = (20 * h + 19) >> 7, j = k.lane & 15, sel = k.lane >> 4;
        const int cbk = (sel & 1) ? c1 : c0;
        const unsigned o0 = WS_F1 + (j * 16 + (sel < 2 ? 5 : 10) + cbk) * FLAG_STRIDE;
        const unsigned o1 = WS_F1 + ((2 * qb + (k.lane & 1)) * 16 + ((k.lane & 2) ? c1 : c0)) * FLAG_STRIDE;
        poll_flags2(k, o0, o1);
    }
    WAIT_BAR(0);
    STAMP(5);
    p2_phase(k, ac, h, qb, ct, f0);
    STAMP(8);
    WAIT_BAR(0);
    if (k.tid == 0) st_flag(ws + WS_F2 + (qb * 32 + h) * FLAG_STRIDE, k.tag);
    P3Pre pre;
    p3_pre(k, rb64, ct, pre);
    if (k.w == 0) poll_flags(k, WS_F2 + (k.xcd * 32 + (k.lane & 31)) * FLAG_STRIDE);
    WAIT_BAR(0);
    if (k.c == 0 && k.tid == 0) st_flag(ws + WS_EPOCH, k.tag);
    STAMP(9);
    u32x2 hv[3];
    p3_phase(k, rb64, ct, pre, hv);
    STAMP(11);
    WAIT_BAR(0);
    if (k.tid == 0) st_flag(ws + WS_F3 + (k.xcd * 32 + k.lb) * FLAG_STRIDE, k.tag);
    if (k.w == 0) poll_flags(k, WS_F3 + (rb4 * 32 + (k.lane & 31)) * FLAG_STRIDE);
    else w13_prefetch(k, f0);
    WAIT_BAR(0);
    STAMP(12);
    p4_phase(k, rb4, f0, nf, ct);
    STAMP(15);
    WAIT_BAR(0);
    if (k.tid == 0) st_flag(ws + WS_F4 + (rb4 * 32 + cb4) * FLAG_STRIDE, k.tag);
    if (k.w == 0) poll_flags(k, WS_F4 + ((rb64 >> 2) * 32 + (k.lane & 31)) * FLAG_STRIDE);
    else w2_prefetch(k, ct);
    WAIT_BAR(0);
    STAMP(17);
    p5_phase(k, rb64, ct, hv);
    STAMP(18);
    STAMP_FLUSH();
}

extern "C" void kernel_launch(void* const* d_in, const int* in_sizes, int n_in, void* d_out, int out_size, void* d_ws, size_t ws_size, hipStream_t stream) {
    (void)hipFuncSetAttribute((const void*)fused_block, hipFuncAttributeMaxDynamicSharedMemorySize, LDS_BYTES);
    fused_block<<<256, 512, LDS_BYTES, stream>>>((const uint16_t*)d_in[0], (const uint16_t*)d_in[1], (const uint16_t*)d_in[2], (const uint16_t*)d_in[4],
                                                 (const uint16_t*)d_in[5], (const uint16_t*)d_in[6], (char*)d_ws, (uint16_t*)d_out);
}
```

```cpp
#include <hip/hip_runtime.h>
#include <hip/hip_bf16.h>
#include <stdint.h>

#define DI __device__ __forceinline__
#define LAS __attribute__((address_space(3)))
#define GAS __attribute__((address_space(1)))
typedef short bf16x8 __attribute__((ext_vector_type(8)));
typedef short s16x4 __attribute__((ext_vector_type(4)));
typedef float f32x2 __attribute__((ext_vector_type(2)));
typedef float f32x4 __attribute__((ext_vector_type(4)));
typedef float f32x16 __attribute__((ext_vector_type(16)));
typedef unsigned u32x2 __attribute__((ext_vector_type(2)));
typedef unsigned u32x4 __attribute__((ext_vector_type(4)));
typedef unsigned long long u64;
typedef __bf16 bf16x2_t __attribute__((ext_vector_type(2)));
typedef short v4i16_t __attribute__((ext_vector_type(4)));
typedef LAS char* lds_ptr;
typedef __amdgpu_buffer_rsrc_t rsrc_t;

constexpr int S = 2048, D = 640, H = 32, DH = 20, F = 1728, NQKV = 3 * D;
constexpr float LN_EPS = 1e-5f;
constexpr float LOG2E = 1.4426950408889634f;
constexpr float QSCALE = 0.22360679774997896f * LOG2E;

constexpr unsigned WS_EPOCH = 0;
constexpr unsigned FLAG_STRIDE = 64, FLAG_ARR = 256 * FLAG_STRIDE;
constexpr unsigned WS_F1 = 4096, WS_FP = WS_F1 + FLAG_ARR, WS_F2 = WS_FP + FLAG_ARR, WS_F3 = WS_F2 + FLAG_ARR, WS_F4 = WS_F3 + FLAG_ARR;
constexpr unsigned WS_ST1 = 131072;
constexpr unsigned WS_CSQ = WS_ST1 + 32768;
constexpr unsigned WS_PST = 262144;
constexpr unsigned WS_Q = 524288;
constexpr unsigned IMG = S * D * 2;
constexpr unsigned QK_TILE = 32 * 1280, QK_IMG = 64 * QK_TILE;
constexpr unsigned WS_K = WS_Q + QK_IMG;
constexpr unsigned WS_V = WS_K + QK_IMG;
constexpr unsigned OH_TILE = 21504, HH_TILE = 56320;
constexpr unsigned WS_O = WS_V + IMG;
constexpr unsigned WS_H = WS_O + 128 * OH_TILE;
constexpr unsigned WS_HH = WS_H + 128 * OH_TILE;
constexpr unsigned WS_WO = WS_HH + 128 * HH_TILE;
constexpr unsigned WS_W13 = WS_WO + D * D * 2;
constexpr unsigned WS_W2 = WS_W13 + D * F * 4;
constexpr unsigned WS_END = WS_W2 + F * D * 2;

DI unsigned cvtpk(float lo, float hi) { f32x2 v = {lo, hi}; bf16x2_t b = __builtin_convertvector(v, bf16x2_t); return __builtin_bit_cast(unsigned, b); }
DI float bflo(unsigned u) { return __uint_as_float(u << 16); }
DI float bfhi(unsigned u) { return __uint_as_float(u & 0xffff0000u); }
DI f32x4 mfma16(bf16x8 a, bf16x8 b, f32x4 c) { return __builtin_amdgcn_mfma_f32_16x16x32_bf16(a, b, c, 0, 0, 0); }
DI void mfma16_acc(f32x4& c, bf16x8 a, bf16x8 b) { asm volatile("v_mfma_f32_16x16x32_bf16 %0, %1, %2, %0" : "+v"(c) : "v"(a), "v"(b)); }
DI void mfma_fence() { __builtin_amdgcn_sched_barrier(0); asm volatile("s_nop 7\n\ts_nop 1" ::: "memory"); __builtin_amdgcn_sched_barrier(0); }
DI f32x16 mfma32(bf16x8 a, bf16x8 b, f32x16 c) { return __builtin_amdgcn_mfma_f32_32x32x16_bf16(a, b, c, 0, 0, 0); }
DI int rfl(int x) { return __builtin_amdgcn_readfirstlane(x); }
template <bool SC1>
DI void glds16(unsigned voff, const void* sbase, unsigned ldsdst) {
    unsigned keep;
    if (SC1) asm volatile("s_mov_b32 %0, m0\n\ts_mov_b32 m0, %3\n\ts_nop 0\n\tglobal_load_lds_dwordx4 %1, %2 sc1\n\ts_mov_b32 m0, %0" : "=&s"(keep) : "v"(voff), "s"(sbase), "s"(ldsdst) : "memory");
    else asm volatile("s_mov_b32 %0, m0\n\ts_mov_b32 m0, %3\n\ts_nop 0\n\tglobal_load_lds_dwordx4 %1, %2\n\ts_mov_b32 m0, %0" : "=&s"(keep) : "v"(voff), "s"(sbase), "s"(ldsdst) : "memory");
}
DI void glds4(const void* gptr, unsigned ldsdst) {
    unsigned keep;
    asm volatile("s_mov_b32 %0, m0\n\ts_mov_b32 m0, %2\n\ts_nop 0\n\tglobal_load_lds_dword %1, off\n\ts_mov_b32 m0, %0" : "=&s"(keep) : "v"(gptr), "s"(ldsdst) : "memory");
}
#define WAIT_VM(n) asm volatile("s_waitcnt vmcnt(" #n ")" ::: "memory")
#define WAIT_BAR(n) asm volatile("s_waitcnt vmcnt(" #n ") lgkmcnt(0)\n\ts_barrier" ::: "memory")
#define BAR_L() asm volatile("s_waitcnt lgkmcnt(0)\n\ts_barrier" ::: "memory")
#define SBAR() __builtin_amdgcn_sched_barrier(0)
#define PIN(x) asm volatile("" : "+v"(x))
#define PRIO(n) __builtin_amdgcn_s_setprio(n)
#define EX(v) __builtin_amdgcn_exp2f(v)
DI u32x4 lds_r128(lds_ptr p) { return *(const LAS u32x4*)p; }
DI s16x4 lds_tr(lds_ptr p) { return __builtin_bit_cast(s16x4, __builtin_amdgcn_ds_read_tr16_b64_v4i16((LAS v4i16_t*)p)); }
DI bf16x8 as_frag(u32x4 v) { return __builtin_bit_cast(bf16x8, v); }
DI bf16x8 cat_frag(s16x4 a, s16x4 b) { return (bf16x8){a[0], a[1], a[2], a[3], b[0], b[1], b[2], b[3]}; }
DI float swap32_max(float m) { auto r = __builtin_amdgcn_permlane32_swap(__float_as_uint(m), __float_as_uint(m), false, false); return fmaxf(__uint_as_float(r[0]), __uint_as_float(r[1])); }
DI float sum_x16(float v) { auto r = __builtin_amdgcn_permlane16_swap(__float_as_uint(v), __float_as_uint(v), false, false); return __uint_as_float(r[0]) + __uint_as_float(r[1]); }
DI float sum_x32(float v) { auto r = __builtin_amdgcn_permlane32_swap(__float_as_uint(v), __float_as_uint(v), false, false); return __uint_as_float(r[0]) + __uint_as_float(r[1]); }
DI u32x4 ld128(rsrc_t r, unsigned off) { return __builtin_amdgcn_raw_buffer_load_b128(r, off, 0, 16); }
DI u32x2 ld64(rsrc_t r, unsigned off) { return __builtin_amdgcn_raw_buffer_load_b64(r, off, 0, 16); }
DI unsigned ld32(rsrc_t r, unsigned off) { return __builtin_amdgcn_raw_buffer_load_b32(r, off, 0, 16); }
DI void st128(rsrc_t r, unsigned off, u32x4 v) { __builtin_amdgcn_raw_buffer_store_b128(v, r, off, 0, 16); }
DI void st64(rsrc_t r, unsigned off, u32x2 v) { __builtin_amdgcn_raw_buffer_store_b64(v, r, off, 0, 16); }

DI void ald128(u32x4& v, u32x4 rsw, unsigned voff, unsigned soff) { asm volatile("buffer_load_dwordx4 %0, %1, %2, %3 offen sc1" : "=&v"(v) : "v"(voff), "s"(rsw), "s"(soff) : "memory"); }
template <int N> DI void ald_wait(u32x4& v) { asm volatile("s_waitcnt vmcnt(%1)" : "+v"(v) : "i"(N) : "memory"); }
struct Ctx {
    const uint16_t *x, *wqkv, *wo, *w1, *w3, *w2;
    char* ws; uint16_t* out;
    rsrc_t rs; u32x4 rsw; u64 tag; lds_ptr lds; unsigned lds0;
    int c, xcd, lb, tid, lane, w;
    mutable unsigned sc, sr;
};

#define STAMP(i)
#define STAMP_FLUSH()

DI u64 ld_flag(const char* p) { return __hip_atomic_load((const GAS u64*)p, __ATOMIC_RELAXED, __HIP_MEMORY_SCOPE_AGENT); }
DI void st_flag(char* p, u64 v) { __hip_atomic_store((GAS u64*)p, v, __ATOMIC_RELAXED, __HIP_MEMORY_SCOPE_AGENT); }
constexpr unsigned SPIN_CAP = 1u << 18;
DI void poll_flags(const Ctx& k, unsigned off) {
    for (unsigned spins = 0; spins < SPIN_CAP; ++spins) {
        if (__all(ld_flag(k.ws + off) == k.tag)) break;
        __builtin_amdgcn_s_sleep(1);
    }
}
DI void poll_flags2(const Ctx& k, unsigned off0, unsigned off1) {
    for (unsigned spins = 0; spins < SPIN_CAP; ++spins) {
        const u64 a = ld_flag(k.ws + off0), b = ld_flag(k.ws + off1);
        if (__all(a == k.tag && b == k.tag)) break;
        __builtin_amdgcn_s_sleep(1);
    }
}
DI void poll_all(const Ctx& k, unsigned arr) {
    for (unsigned spins = 0; spins < SPIN_CAP; ++spins) {
        const unsigned o = arr + k.lane * FLAG_STRIDE;
        const u64 a = ld_flag(k.ws + o), b = ld_flag(k.ws + o + 64 * FLAG_STRIDE), c = ld_flag(k.ws + o + 128 * FLAG_STRIDE), d = ld_flag(k.ws + o + 192 * FLAG_STRIDE);
        if (__all(a == k.tag && b == k.tag && c == k.tag && d == k.tag)) break;
        __builtin_amdgcn_s_sleep(1);
    }
}

constexpr int SU_LDS = 131072;
DI void startup_load(const Ctx& k) { glds4(k.ws + WS_EPOCH + (k.lane & 1) * 4, k.lds0 + SU_LDS + k.w * 256); }
DI void startup_tag(Ctx& k) {
    const u32x2 e = *(const LAS u32x2*)(k.lds + SU_LDS + k.w * 256);
    k.tag = (((u64)(unsigned)rfl(e.y) << 32) | (unsigned)rfl(e.x)) + 1;
}
DI void ln1_stats(const Ctx& k, int j) {
    u32x4 v[2][10];
#pragma unroll
    for (int p = 0; p < 2; ++p)
#pragma unroll
        for (int i = 0; i < 10; ++i) v[p][i] = *(const u32x4*)(k.x + (size_t)(128 * j + 16 * k.w + 8 * p + (k.lane >> 3)) * D + ((k.lane & 7) + 8 * i) * 8);
#pragma unroll
    for (int p = 0; p < 2; ++p) {
        float s = 0.f, q = 0.f;
#pragma unroll
        for (int i = 0; i < 10; ++i)
#pragma unroll
            for (int e = 0; e < 4; ++e) { const float a = bflo(v[p][i][e]), b = bfhi(v[p][i][e]); s += a + b; q = fmaf(a, a, fmaf(b, b, q)); }
#pragma unroll
        for (int o = 4; o >= 1; o >>= 1) { s += __shfl_xor(s, o); q += __shfl_xor(q, o); }
        const float mean = s * (1.0f / D), var = fmaxf(q * (1.0f / D) - mean * mean, 0.f);
        const float val = (k.lane & 1) ? 1.0f / sqrtf(var + LN_EPS) : mean;
        const int row = 128 * j + 16 * k.w + 8 * p + (k.lane >> 3);
        if ((k.lane & 7) < 2) st64(k.rs, WS_ST1 + row * 16 + (k.lane & 1) * 8, (u32x2){__float_as_uint(val), (unsigned)k.tag});
    }
}
DI void colsum_qkv(const Ctx& k, int cb) {
    float a[8] = {0, 0, 0, 0, 0, 0, 0, 0};
    u32x4 v[20];
#pragma unroll
    for (int i = 0; i < 20; ++i) v[i] = *(const u32x4*)(k.wqkv + (size_t)(k.w * 80 + i * 4 + (k.lane >> 4)) * NQKV + cb * 128 + (k.lane & 15) * 8);
#pragma unroll
    for (int i = 0; i < 20; ++i)
#pragma unroll
        for (int j = 0; j < 4; ++j) { a[2 * j] += bflo(v[i][j]); a[2 * j + 1] += bfhi(v[i][j]); }
    LAS float* red = (LAS float*)k.lds;
#pragma unroll
    for (int j = 0; j < 8; ++j) { a[j] += __shfl_xor(a[j], 16); a[j] += __shfl_xor(a[j], 32); }
    if (k.lane < 16) {
#pragma unroll
        for (int j = 0; j < 8; ++j) red[k.w * 128 + k.lane * 8 + j] = a[j];
    }
    BAR_L();
    if (k.tid < 128) {
        float t = 0.f;
#pragma unroll
        for (int i = 0; i < 8; ++i) t += red[i * 128 + k.tid];
        st64(k.rs, WS_CSQ + (cb * 128 + k.tid) * 8, (u32x2){__float_as_uint(t), (unsigned)k.tag});
    }
}

constexpr int PT_LDS = 131072;
DI int pt_off(int i) { return PT_LDS + (i < 3 ? i * 8192 : 24576 + (i - 3) * 4096); }
DI void pretile_load(const Ctx& k) {
    const int wv = k.w - 1;
#pragma unroll
    for (int i = 0; i < 5; ++i) {
        const int u = k.c + 256 * i;
        if (u < 540) {
            const int s = u % 20, fb = u / 20;
            const uint16_t* src = ((k.lane & 1) ? k.w3 : k.w1) + fb * 64 + (k.lane >> 1) * 2 + (size_t)32 * s * F;
            for (int r = wv; r < 32; r += 7) glds4(src + (size_t)r * F, k.lds0 + pt_off(i) + r * 256);
        } else {
            const int pc = (i & 1) ? wv - 3 : wv;
            if (pc >= 0 && pc < 4) {
                const bool is2 = u < 1080;
                const int uu = is2 ? u - 540 : u - 1080, s = uu / 10, nb = uu % 10, r = 8 * pc + (k.lane >> 3);
                glds16<false>((32 * s + r) * (D * 2) + nb * 128 + (k.lane & 7) * 16, is2 ? k.w2 : k.wo, k.lds0 + pt_off(i) + pc * 1024);
            }
        }
    }
}
struct PreTile { u32x4 v[5]; };
DI void pretile_read(const Ctx& k, PreTile& p) {
    const int g = k.lane >> 4, q = (k.lane >> 2) & 3, pp = k.lane & 3;
#pragma unroll
    for (int i = 0; i < 5; ++i) {
        const int u = k.c + 256 * i;
        const bool wide = u < 540;
        const int rs = wide ? 256 : 128, f = wide ? k.w : (k.w & 3);
        const lds_ptr a = k.lds + pt_off(i) + (8 * g + q) * rs + f * 32 + pp * 8;
        p.v[i] = __builtin_bit_cast(u32x4, cat_frag(lds_tr(a), lds_tr(a + 4 * rs)));
    }
}
DI void pretile_store(const Ctx& k, const PreTile& p) {
#pragma unroll
    for (int i = 0; i < 5; ++i) {
        const int u = k.c + 256 * i;
        if (u < 540) st128(k.rs, WS_W13 + (((u / 20) * 8 + k.w) * 20 + u % 20) * 1024 + k.lane * 16, p.v[i]);
        else if ((k.w >> 2) == (i & 1)) {
            const bool is2 = u < 1080;
            const int uu = is2 ? u - 540 : u - 1080, s = uu / 10, cfg = (uu % 10) * 4 + (k.w & 3), ct = cfg / 5, cf = cfg % 5;
            st128(k.rs, (is2 ? WS_W2 + ((ct * 54 + s) * 5 + cf) * 1024 : WS_WO + ((ct * 20 + s) * 5 + cf) * 1024) + k.lane * 16, p.v[i]);
        }
    }
}

constexpr int P1_SLOT = 32768;
struct P1Frag { bf16x8 a[4], b[2]; };
template <bool ISV> DI void p1_mma(f32x4 (&acc)[4][2], const P1Frag& f, int m, int n) { if (ISV) mfma16_acc(acc[m][n], f.a[m], f.b[n]); else mfma16_acc(acc[m][n], f.b[n], f.a[m]); }
template <bool ISV>
DI void p1_body(Ctx& k, int rb, int cb) {
    const int lane = k.lane, w = k.w, th = w >> 2, cg = w & 3, t = lane & 15, g = lane >> 4;
    unsigned voffA[2], voffW[2];
#pragma unroll
    for (int i = 0; i < 2; ++i) {
        const int ra = 8 * (2 * w + i) + (lane >> 3), ca = (lane & 7) ^ ((ra >> 1) & 7);
        voffA[i] = ra * (D * 2) + ca * 16;
        const int rw = 4 * (2 * w + i) + (lane >> 4), fw = (rw & 3) | (((rw >> 3) & 1) << 2), cw = (lane & 15) ^ (fw << 1);
        voffW[i] = rw * (NQKV * 2) + cw * 16;
    }
    const char* gA = (const char*)k.x + (size_t)rb * 128 * D * 2;
    const char* gW = (const char*)k.wqkv + cb * 256;
    auto stage = [&](int kt) {
        const unsigned sl = k.lds0 + (kt & 3) * P1_SLOT + w * 2048;
#pragma unroll
        for (int i = 0; i < 2; ++i) glds16<false>(voffA[i], gA + kt * 128, sl + i * 1024);
#pragma unroll
        for (int i = 0; i < 2; ++i) glds16<false>(voffW[i], gW + (size_t)kt * 64 * NQKV * 2, sl + 16384 + i * 1024);
    };
    stage(0); stage(1); stage(2); stage(3);
    int aoff[2], woff[2];
    aoff[0] = (th * 64 + t) * 128 + ((g ^ (t >> 1)) << 4); aoff[1] = aoff[0] ^ 64;
    const int q4 = (lane >> 2) & 3, p = lane & 3, fw = q4 | ((g & 1) << 2);
#pragma unroll
    for (int n = 0; n < 2; ++n) woff[n] = 16384 + (8 * g + q4) * 256 + (((2 * cg + n) ^ fw) << 5) + p * 8;
    f32x4 acc[4][2];
#pragma unroll
    for (int m = 0; m < 4; ++m) { acc[m][0] = (f32x4){0, 0, 0, 0}; acc[m][1] = (f32x4){0, 0, 0, 0}; PIN(acc[m][0]); PIN(acc[m][1]); }
    mfma_fence();
    P1Frag f0, f1;
#define P1_RA(F_, sl, ks, m) F_.a[m] = as_frag(lds_r128(sl + aoff[ks] + (m) * 2048))
#define P1_RB(F_, sl, ks, n) F_.b[n] = cat_frag(lds_tr(sl + woff[n] + (ks) * 8192), lds_tr(sl + woff[n] + (ks) * 8192 + 1024))
#define P1_STEP(RD, FN, sl, ks, FC) do { \
        if (RD) P1_RB(FN, sl, ks, 0); p1_mma<ISV>(acc, FC, 0, 0); SBAR(); \
        if (RD) P1_RA(FN, sl, ks, 0); p1_mma<ISV>(acc, FC, 0, 1); SBAR(); \
        if (RD) P1_RA(FN, sl, ks, 1); p1_mma<ISV>(acc, FC, 1, 0); SBAR(); \
        if (RD) P1_RB(FN, sl, ks, 1); p1_mma<ISV>(acc, FC, 1, 1); SBAR(); \
        if (RD) P1_RA(FN, sl, ks, 2); p1_mma<ISV>(acc, FC, 2, 0); SBAR(); \
        if (RD) P1_RA(FN, sl, ks, 3); p1_mma<ISV>(acc, FC, 2, 1); SBAR(); \
        p1_mma<ISV>(acc, FC, 3, 0); p1_mma<ISV>(acc, FC, 3, 1); SBAR(); } while (0)
    WAIT_BAR(12);
    STAMP(1);
    startup_tag(k);
    { const lds_ptr sl = k.lds; P1_RB(f0, sl, 0, 0); P1_RB(f0, sl, 0, 1); P1_RA(f0, sl, 0, 0); P1_RA(f0, sl, 0, 1); P1_RA(f0, sl, 0, 2); P1_RA(f0, sl, 0, 3); SBAR(); }
    u32x2 gr = {0, 0};
    PRIO(1);
    const unsigned groff = k.tid < 256 ? WS_ST1 + (rb * 128) * 16 + k.tid * 8 : WS_CSQ + (cb * 128 + (k.tid & 127)) * 8;
#pragma unroll
    for (int kt = 0; kt < 10; ++kt) {
        const lds_ptr sl = k.lds + (kt & 3) * P1_SLOT, sn = k.lds + ((kt + 1) & 3) * P1_SLOT;
        P1_STEP(true, f1, sl, 1, f0);
        if (kt < 9) {
            if (kt < 7) WAIT_BAR(8); else if (kt == 7) WAIT_BAR(4); else WAIT_BAR(0);
            if (kt == 6) gr = ld64(k.rs, groff);
            if (kt + 4 < 10) stage(kt + 4);
            SBAR();
        }
        P1_STEP(kt < 9, f0, sn, 0, f1);
    }
#undef P1_RA
#undef P1_RB
#undef P1_STEP
    PRIO(0);
    mfma_fence();
    STAMP(2);
    for (unsigned spins = 0; spins < SPIN_CAP; ++spins) {
        if (__all(gr.y == (unsigned)k.tag)) break;
        gr = ld64(k.rs, groff);
    }
    LAS float* sst = (LAS float*)k.lds;
    if (k.tid < 384) sst[k.tid] = __uint_as_float(gr.x);
    BAR_L();
    const LAS f32x2* st = (const LAS f32x2*)sst + th * 64;
    const LAS float* cs = sst + 256 + cg * 32;
    if (!ISV) {
        const float osc = cb < 5 ? QSCALE : 1.0f;
        const unsigned img = cb < 5 ? WS_Q : WS_K;
        const int cbs = cb % 5;
        unsigned dst[2]; f32x4 c4[2];
#pragma unroll
        for (int n = 0; n < 2; ++n) {
            const int cgi = cbs * 32 + cg * 8 + n * 4 + g, hd = cgi / 5, gi = cgi - 5 * hd;
            const int blk = gi == 4 ? 4 : ((gi & 1) << 1) | (gi >> 1);
            dst[n] = img + (rb * 4 + th * 2) * QK_TILE + hd * 1280 + blk * 256 + t * 8;
            c4[n] = *(const LAS f32x4*)(cs + n * 16 + 4 * g);
        }
#pragma unroll
        for (int m = 0; m < 4; ++m) {
            const f32x2 sm = st[m * 16 + t];
            const float r = sm.y * osc;
#pragma unroll
            for (int n = 0; n < 2; ++n) {
                const f32x4 y = (acc[m][n] - c4[n] * sm.x) * r;
                const unsigned o = dst[n] + (m >> 1) * QK_TILE + (m & 1) * 128;
                st64(k.rs, o, (u32x2){cvtpk(y[0], y[1]), cvtpk(y[2], y[3])});
            }
        }
    } else {
        const int pos = ((g & 1) << 1) | (g >> 1);
        const unsigned dst = WS_V + (rb * 8 + th * 4) * 20480 + ((cb - 10) * 128 + cg * 32 + t) * 32 + pos * 8;
        const float c1[2] = {cs[t], cs[16 + t]};
#pragma unroll
        for (int m = 0; m < 4; ++m) {
            f32x2 sm[4];
#pragma unroll
            for (int i = 0; i < 4; ++i) sm[i] = st[m * 16 + 4 * g + i];
#pragma unroll
            for (int n = 0; n < 2; ++n) {
                float y[4];
#pragma unroll
                for (int i = 0; i < 4; ++i) y[i] = (acc[m][n][i] - c1[n] * sm[i].x) * sm[i].y;
                st64(k.rs, dst + m * 20480 + n * 512, (u32x2){cvtpk(y[0], y[1]), cvtpk(y[2], y[3])});
            }
        }
    }
}

constexpr int AT_KT = 8192, AT_KB = 10240, AT_HALF = AT_KB + 10240, AT_BUF = 2 * AT_HALF;
constexpr int AT_ONES = AT_BUF, AT_ONES_BYTES = 10752, AT_KC = AT_ONES + AT_ONES_BYTES, AT_KC_BYTES = 2048, AT_FLAG = AT_KC + AT_KC_BYTES, WO_LDS = 61440, AT_RED = 0;
DI int at_slot(int i) { return i == 0 ? 0 : (i == 1 ? 61440 : 61440 + AT_BUF); }
struct AttnCtx {
    unsigned voff[5]; const char* gsrc; unsigned ldst, sstride;
    u32x4 qf0[2]; u32x2 qf1[2];
    int kofs, kt, vlane; bool vdata, hi;
};
DI void attn_stage(const AttnCtx& c, int sidx, int slot) {
#pragma unroll
    for (int i = 0; i < 5; ++i) glds16<true>(c.voff[i], c.gsrc + sidx * c.sstride, c.ldst + at_slot(slot) + i * 1024);
}
DI void attn_setup(const Ctx& k, AttnCtx& c, int h) {
    const int lane = k.lane, w = k.w, kh = w >> 2, r = lane & 31, hi = lane >> 5;
    const bool stV = (w >> 1) & 1;
#pragma unroll
    for (int i = 0; i < 5; ++i) {
        const int pc = 5 * (w & 1) + i;
        if (!stV) c.voff[i] = pc < 8 ? pc * QK_TILE + lane * 16 : ((pc - 8) * 4 + (lane >> 4)) * QK_TILE + 1024 + (lane & 15) * 16;
        else { const int ci = 64 * pc + lane, st = ci / 40, wi = ci % 40; c.voff[i] = st * 20480 + ((wi % 20) * 2 + wi / 20) * 16; }
    }
    c.gsrc = k.ws + (stV ? WS_V + h * 640 + kh * 64 * 20480 : WS_K + h * 1280 + kh * 32 * QK_TILE);
    c.sstride = stV ? 16 * 20480 : 8 * QK_TILE;
    c.ldst = k.lds0 + kh * AT_HALF + (stV ? AT_KB : 0) + (w & 1) * 5120;
    c.kofs = kh * AT_HALF + hi * 256 + r * 8;
    c.hi = hi;
    c.kt = hi ? AT_KC + r * 8 : kh * AT_HALF + AT_KT + r * 8;
    const int rr = r >= 28 ? r - 12 : r;
    c.vdata = rr < 20;
    c.vlane = c.vdata ? kh * AT_HALF + AT_KB + hi * 320 + rr * 16 : AT_ONES + 128;
    for (int i = k.tid; i < AT_ONES_BYTES / 4; i += 512) ((LAS unsigned*)(k.lds + AT_ONES))[i] = 0x3F803F80u;
    ((LAS unsigned*)(k.lds + AT_KC))[k.tid] = (k.tid & 1) ? 0u : 0x00003F80u;
    if (k.tid == 0) *(LAS unsigned*)(k.lds + AT_FLAG) = 0;
}
DI void attn_first_loads(const Ctx& k, AttnCtx& c, int h, int qb) {
    const int qg = k.w & 3, r = k.lane & 31, hi = k.lane >> 5;
    attn_stage(c, 0, 0);
#pragma unroll
    for (int i = 0; i < 2; ++i) {
        const unsigned qp = WS_Q + (qb * 8 + qg * 2 + i) * QK_TILE + h * 1280 + r * 8;
        const u32x2 a = ld64(k.rs, qp + hi * 256), b = ld64(k.rs, qp + hi * 256 + 512), e = ld64(k.rs, qp + 1024);
        c.qf0[i] = (u32x4){a.x, a.y, b.x, b.y};
        c.qf1[i] = hi ? (u32x2){0, 0} : e;
    }
    attn_stage(c, 1, 1);
}
DI bf16x8 kfrag0(lds_ptr p) { const u32x2 a = *(const LAS u32x2*)p, b = *(const LAS u32x2*)(p + 512); return as_frag((u32x4){a.x, a.y, b.x, b.y}); }
DI s16x4 kfrag1(lds_ptr p) { return __builtin_bit_cast(s16x4, *(const LAS u32x2*)p); }
DI s16x4 as_frag4(u32x2 v) { return __builtin_bit_cast(s16x4, v); }
DI f32x16 mfma32x8(s16x4 a, s16x4 b, f32x16 c) { return __builtin_amdgcn_mfma_f32_32x32x8bf16_1k(a, b, c, 0, 0, 0); }
DI void wo_prefetch(const Ctx& k, int ct) {
    for (int pc = k.w; pc < 100; pc += 8) glds16<true>(k.lane * 16, k.ws + WS_WO + ct * 102400 + pc * 1024, k.lds0 + WO_LDS + pc * 1024);
}
DI void attn_fast(const Ctx& k, AttnCtx& c, f32x16& oA, f32x16& oB, int ct3, int f4) {
    const lds_ptr lds = k.lds;
    const int hi = k.lane >> 5, kh = k.w >> 2;
    {
        const bf16x8 k0 = kfrag0(lds + c.kofs - kh * AT_HALF); const s16x4 k1 = kfrag1(lds + (hi ? c.kt : c.kt - kh * AT_HALF));
#pragma unroll
        for (int i = 0; i < 2; ++i) {
            f32x16 s = mfma32(k0, as_frag(c.qf0[i]), (f32x16){});
            s = mfma32x8(k1, as_frag4(c.qf1[i]), s);
            float m = s[0];
#pragma unroll
            for (int j = 1; j < 16; ++j) m = fmaxf(m, s[j]);
            m = swap32_max(m);
            if (hi) c.qf1[i].x = cvtpk(-m, 0.f) & 0xffffu;
        }
    }
    const bf16x8 qA0 = as_frag(c.qf0[0]), qB0 = as_frag(c.qf0[1]); const s16x4 qA1 = as_frag4(c.qf1[0]), qB1 = as_frag4(c.qf1[1]);
    f32x16 sA, sB, nA, nB;
    oA = (f32x16){}; oB = (f32x16){};
    bf16x8 kf0 = kfrag0(lds + c.kofs); s16x4 kf1 = kfrag1(lds + c.kt);
    sA = mfma32(kf0, qA0, (f32x16){}); sB = mfma32(kf0, qB0, (f32x16){});
    sA = mfma32x8(kf1, qA1, sA); sB = mfma32x8(kf1, qB1, sB);
    kf0 = kfrag0(lds + c.kofs + 1024); kf1 = kfrag1(lds + c.kt + 256);
    u32x4 pA0, pA1, pB0, pB1 = {0, 0, 0, 0};
    bf16x8 vf0, vf1 = {0, 0, 0, 0, 0, 0, 0, 0};
    unsigned warm = 0;
    u32x2 fa = {0, 0}, fb = fa, fc = fa, fd = fa;
#define GAP(MF, X, i, P, j) do { MF; SBAR(); X[i] = EX(X[i]); X[i + 1] = EX(X[i + 1]); X[i + 2] = EX(X[i + 2]); X[i + 3] = EX(X[i + 3]); \
        P[j] = cvtpk(X[i], X[i + 1]); P[j + 1] = cvtpk(X[i + 2], X[i + 3]); PIN(P); SBAR(); } while (0)
#define STEP(SA, SB, NA, NB, VP, KP, KT) do { \
        GAP(NA = mfma32(kf0, qA0, (f32x16){}), SA, 0, pA0, 0); vf0 = as_frag(lds_r128(VP)); SBAR(); \
        GAP(oB = mfma32(vf1, as_frag(pB1), oB), SA, 4, pA0, 2); vf1 = as_frag(lds_r128((VP) + 640)); SBAR(); \
        GAP(NB = mfma32(kf0, qB0, (f32x16){}), SA, 8, pA1, 0); \
        GAP(NA = mfma32x8(kf1, qA1, NA), SA, 12, pA1, 2); \
        GAP(NB = mfma32x8(kf1, qB1, NB), SB, 0, pB0, 0); kf0 = kfrag0(KP); kf1 = kfrag1(KT); SBAR(); \
        GAP(oA = mfma32(vf0, as_frag(pA0), oA), SB, 4, pB0, 2); \
        GAP(oA = mfma32(vf1, as_frag(pA1), oA), SB, 8, pB1, 0); \
        GAP(oB = mfma32(vf0, as_frag(pB0), oB), SB, 12, pB1, 2); \
    } while (0)
    int slot = 0;
    PRIO(1);
    for (int sidx = 0; sidx < 4; ++sidx) {
        const int nslot = slot == 2 ? 0 : slot + 1;
        const int sb = at_slot(slot), sn = at_slot(nslot);
        const lds_ptr vb = lds + (c.vdata ? c.vlane + sb : c.vlane);
        lds_ptr k0 = lds + c.kofs + sb, k0n = lds + c.kofs + sn, k1 = lds + (hi ? c.kt : c.kt + sb), k1n = lds + (hi ? c.kt : c.kt + sn);
        PIN(k0); PIN(k0n); PIN(k1); PIN(k1n);
        STEP(sA, sB, nA, nB, vb, k0 + 2 * 1024, k1 + 2 * 256);
        STEP(nA, nB, sA, sB, vb + 1280, k0 + 3 * 1024, k1 + 3 * 256);
        STEP(sA, sB, nA, nB, vb + 2 * 1280, k0 + 4 * 1024, k1 + 4 * 256);
        if (sidx == 1 && k.w == 0) {
            const unsigned o = WS_FP + k.lane * FLAG_STRIDE;
            fa = ld64(k.rs, o); fb = ld64(k.rs, o + 64 * FLAG_STRIDE); fc = ld64(k.rs, o + 128 * FLAG_STRIDE); fd = ld64(k.rs, o + 192 * FLAG_STRIDE);
        }
        STEP(nA, nB, sA, sB, vb + 3 * 1280, k0 + 5 * 1024, k1 + 5 * 256);
        STEP(sA, sB, nA, nB, vb + 4 * 1280, k0 + 6 * 1024, k1 + 6 * 256);
        STEP(nA, nB, sA, sB, vb + 5 * 1280, k0 + 7 * 1024, k1 + 7 * 256);
        if (sidx == 1 && k.w == 0) {
            const unsigned tl = (unsigned)k.tag, th = (unsigned)(k.tag >> 32);
            if (!__all(fa.x == tl && fa.y == th && fb.x == tl && fb.y == th && fc.x == tl && fc.y == th && fd.x == tl && fd.y == th)) poll_all(k, WS_FP);
        }
        if (sidx == 0) STAMP(20); else if (sidx == 1) STAMP(22); else if (sidx == 2) STAMP(24); else STAMP(26);
        WAIT_BAR(0);
        if (sidx == 0) STAMP(21); else if (sidx == 1) STAMP(23); else if (sidx == 2) STAMP(25); else STAMP(27);
        if (sidx < 2) attn_stage(c, sidx + 2, nslot == 2 ? 0 : nslot + 1);
        if (sidx == 0 && k.tid == 0) st_flag(k.ws + WS_FP + k.c * FLAG_STRIDE, k.tag);
        if (sidx == 1) {
            const unsigned o = WS_W13 + f4 * 20480 + (k.lb >> 4) * 71680 + (k.w * 64 + k.lane) * 128;
            warm = ld32(k.rs, o);
            if (k.w == 0 && k.lane < 48) warm ^= ld32(k.rs, o + 65536);
        }
        if (sidx == 3) wo_prefetch(k, ct3);
        STEP(sA, sB, nA, nB, vb + 6 * 1280, k0n, k1n);
        STEP(nA, nB, sA, sB, vb + 7 * 1280, k0n + 1024, k1n + 256);
        slot = nslot;
    }
    PRIO(0);
    oB = mfma32(vf1, as_frag(pB1), oB);
    asm volatile("" :: "v"(warm));
#undef GAP
#undef STEP
}
DI void attn_safe(const Ctx& k, AttnCtx& c, f32x16 (&o)[2], float (&mrun)[2]) {
    const lds_ptr lds = k.lds;
    attn_stage(c, 0, 0);
    if (k.lane >> 5) { c.qf1[0].x = 0; c.qf1[1].x = 0; }
    o[0] = (f32x16){}; o[1] = (f32x16){};
    mrun[0] = -INFINITY; mrun[1] = -INFINITY;
    for (int sidx = 0; sidx < 4; ++sidx) {
        const int sb = at_slot(sidx & 1);
        WAIT_BAR(0);
        if (sidx < 3) attn_stage(c, sidx + 1, (sidx + 1) & 1);
        const lds_ptr vb = lds + (c.vdata ? c.vlane + sb : c.vlane);
        for (int kt = 0; kt < 8; ++kt) {
            const bf16x8 k0 = kfrag0(lds + c.kofs + sb + kt * 1024); const s16x4 k1 = kfrag1(lds + (c.hi ? c.kt : c.kt + sb) + kt * 256);
            const bf16x8 v0 = as_frag(lds_r128(vb + kt * 1280)), v1 = as_frag(lds_r128(vb + kt * 1280 + 640));
#pragma unroll
            for (int i = 0; i < 2; ++i) {
                f32x16 s = mfma32(k0, as_frag(c.qf0[i]), (f32x16){});
                s = mfma32x8(k1, as_frag4(c.qf1[i]), s);
                float m = s[0];
#pragma unroll
                for (int j = 1; j < 16; ++j) m = fmaxf(m, s[j]);
                m = fmaxf(swap32_max(m), mrun[i]);
                const float alpha = EX(mrun[i] - m);
                mrun[i] = m;
                unsigned pk[8];
#pragma unroll
                for (int j = 0; j < 16; ++j) o[i][j] *= alpha;
#pragma unroll
                for (int j = 0; j < 8; ++j) pk[j] = cvtpk(EX(s[2 * j] - m), EX(s[2 * j + 1] - m));
                o[i] = mfma32(v0, as_frag((u32x4){pk[0], pk[1], pk[2], pk[3]}), o[i]);
                o[i] = mfma32(v1, as_frag((u32x4){pk[4], pk[5], pk[6], pk[7]}), o[i]);
            }
        }
    }
}
DI void p2_phase(const Ctx& k, AttnCtx& c, int h, int qb, int ct3, int f4) {
    const int lane = k.lane, qg = k.w & 3, kh = k.w >> 2, r = lane & 31, hi = lane >> 5;
    const lds_ptr lds = k.lds;
    attn_first_loads(k, c, h, qb);
    PreTile pt;
    pretile_read(k, pt);
    WAIT_BAR(5);
    STAMP(6);
    pretile_store(k, pt);
    f32x16 o[2];
    attn_fast(k, c, o[0], o[1], ct3, f4);
    STAMP(7);
    LAS unsigned* flag = (LAS unsigned*)(lds + AT_FLAG);
    if (__any(hi && !(o[0][8] <= 5.764607523034235e17f && o[1][8] <= 5.764607523034235e17f)) && lane == 0) *flag = 1;
    BAR_L();
    const lds_ptr red = lds + AT_RED + qg * 8192 + lane * 16;
    auto put = [&](const f32x16& v, int slot) {
#pragma unroll
        for (int u = 0; u < 3; ++u) *(LAS f32x4*)(red + slot * 4096 + u * 1024) = (f32x4){v[4 * u], v[4 * u + 1], v[4 * u + 2], v[4 * u + 3]};
    };
    auto get = [&](int slot, int u) { return *(const LAS f32x4*)(red + slot * 4096 + u * 1024); };
    f32x4 of[3];
    if (*flag == 0) {
        if (kh) put(o[0], 0); else put(o[1], 1);
        BAR_L();
#pragma unroll
        for (int u = 0; u < 3; ++u) {
            const f32x4 p = get(kh, u);
#pragma unroll
            for (int j = 0; j < 4; ++j) of[u][j] = p[j] + (kh ? o[1][4 * u + j] : o[0][4 * u + j]);
        }
    } else {
        float mref[2];
        WAIT_BAR(0);
        attn_safe(k, c, o, mref);
        BAR_L();
        if (kh) put(o[0], 0); else put(o[1], 1);
        *(LAS float*)(red + (kh ? 0 : 4096) + 3072) = kh ? mref[0] : mref[1];
        BAR_L();
        const float m0 = kh ? mref[1] : mref[0], m1 = *(const LAS float*)(red + kh * 4096 + 3072), m = fmaxf(m0, m1);
        const float a0 = EX(m0 - m), a1 = EX(m1 - m);
#pragma unroll
        for (int u = 0; u < 3; ++u) {
            const f32x4 p = get(kh, u);
#pragma unroll
            for (int j = 0; j < 4; ++j) of[u][j] = p[j] * a1 + (kh ? o[1][4 * u + j] : o[0][4 * u + j]) * a0;
        }
        BAR_L();
        wo_prefetch(k, ct3);
    }
    {
        auto lr = __builtin_amdgcn_permlane32_swap(__float_as_uint(of[2][0]), __float_as_uint(of[2][0]), false, false);
        const float inv = 1.0f / __uint_as_float(lr[1]);
        const int tok = qb * 256 + qg * 64 + kh * 32 + r;
        const unsigned base = WS_O + (tok >> 4) * OH_TILE + (tok & 15) * 16;
        unsigned pk[3][2];
#pragma unroll
        for (int u = 0; u < 3; ++u) { pk[u][0] = cvtpk(of[u][0] * inv, of[u][1] * inv); pk[u][1] = cvtpk(of[u][2] * inv, of[u][3] * inv); }
        auto oaddr = [&](int col) { return base + (col >> 5) * 1024 + ((col >> 3) & 3) * 256 + (col & 7) * 2; };
        if ((h & 1) == 0) {
            auto s0 = __builtin_amdgcn_permlane32_swap(pk[0][0], pk[1][0], false, false);
            auto s1 = __builtin_amdgcn_permlane32_swap(pk[0][1], pk[1][1], false, false);
            st128(k.rs, oaddr(h * 20 + 8 * hi), (u32x4){s0[0], s1[0], s0[1], s1[1]});
            if (!hi) st64(k.rs, oaddr(h * 20 + 16), (u32x2){pk[2][0], pk[2][1]});
        } else {
            auto s0 = __builtin_amdgcn_permlane32_swap(pk[0][0], pk[2][0], false, false);
            auto s1 = __builtin_amdgcn_permlane32_swap(pk[0][1], pk[2][1], false, false);
            st128(k.rs, oaddr(h * 20 + 4 + 8 * hi), hi ? (u32x4){pk[1][0], pk[1][1], s0[0], s1[0]} : (u32x4){s0[1], s1[1], pk[1][0], pk[1][1]});
            if (!hi) st64(k.rs, oaddr(h * 20), (u32x2){s0[0], s1[0]});
        }
    }
}

DI int own_cf(int kh, int u) { return kh ? (u < 2 ? 3 + u : 4) : u; }
constexpr int RED_LDS = 143360;
struct P3Pre { u32x2 xv[5]; };
DI void p3_pre(const Ctx& k, int rb64, int ct, P3Pre& p) {
    const int tt = k.w & 3, t = k.lane & 15, g = k.lane >> 4;
    if (k.w < 4) {
#pragma unroll
        for (int cf = 0; cf < 5; ++cf) p.xv[cf] = *(const u32x2*)(k.x + (size_t)(rb64 * 64 + tt * 16 + t) * D + ct * 80 + cf * 16 + 4 * g);
    }
}
DI void w13_prefetch(const Ctx& k, int f0, int f);
DI void p3_phase(const Ctx& k, int rb64, int ct, int f0, const P3Pre& pre, u32x2 (&hv)[3]) {
    const int lane = k.lane, tt = k.w & 3, kh = k.w >> 2, t = lane & 15, g = lane >> 4;
    const unsigned ap = WS_O + (rb64 * 4 + tt) * OH_TILE + kh * 10240 + lane * 16;
    const lds_ptr wl = k.lds + WO_LDS + kh * 51200 + lane * 16;
    constexpr int WIN = 5;
    u32x4 a[10];
#pragma unroll
    for (int i = 0; i < WIN; ++i) a[i] = ld128(k.rs, ap + i * 1024);
    bf16x8 wf[2][5];
#pragma unroll
    for (int cf = 0; cf < 5; ++cf) wf[0][cf] = as_frag(lds_r128(wl + cf * 1024));
    f32x4 acc[5];
#pragma unroll
    for (int cf = 0; cf < 5; ++cf) { acc[cf] = (f32x4){0, 0, 0, 0}; PIN(acc[cf]); }
    mfma_fence();
    PRIO(1);
#pragma unroll
    for (int i = 0; i < 10; ++i) {
        if (i + WIN < 10) { a[i + WIN] = ld128(k.rs, ap + (i + WIN) * 1024); SBAR(); }
#pragma unroll
        for (int cf = 0; cf < 5; ++cf) {
            if (i + 1 < 10) wf[(i + 1) & 1][cf] = as_frag(lds_r128(wl + (i + 1) * 5120 + cf * 1024));
            mfma16_acc(acc[cf], wf[i & 1][cf], as_frag(a[i]));
            SBAR();
        }
    }
    PRIO(0);
    mfma_fence();
    STAMP(10);
    BAR_L();
    const lds_ptr red = k.lds + RED_LDS + tt * 5120 + lane * 16;
    if (kh) {
#pragma unroll
        for (int cf = 0; cf < 5; ++cf) *(LAS f32x4*)(red + cf * 1024) = acc[cf];
        BAR_L();
        w13_prefetch(k, f0, k.w - 1);
    } else {
        BAR_L();
        const int tok = rb64 * 64 + tt * 16 + t;
        float s = 0.f, q = 0.f;
        u32x2 hq[5];
#pragma unroll
        for (int cf = 0; cf < 5; ++cf) {
            const f32x4 v = acc[cf] + *(const LAS f32x4*)(red + cf * 1024);
            hq[cf] = (u32x2){cvtpk(v[0] + bflo(pre.xv[cf].x), v[1] + bfhi(pre.xv[cf].x)), cvtpk(v[2] + bflo(pre.xv[cf].y), v[3] + bfhi(pre.xv[cf].y))};
            const int col = ct * 80 + cf * 16 + 4 * g;
            st64(k.rs, WS_H + (tok >> 4) * OH_TILE + (col >> 5) * 1024 + ((col >> 3) & 3) * 256 + t * 16 + (col & 7) * 2, hq[cf]);
            const float h0 = bflo(hq[cf].x), h1 = bfhi(hq[cf].x), h2 = bflo(hq[cf].y), h3 = bfhi(hq[cf].y);
            s += (h0 + h1) + (h2 + h3); q += (h0 * h0 + h1 * h1) + (h2 * h2 + h3 * h3);
        }
        s = sum_x32(sum_x16(s)); q = sum_x32(sum_x16(q));
        if (g == 0) st64(k.rs, WS_PST + (tok >> 4) * 2048 + (ct * 2) * 128 + t * 8, (u32x2){__float_as_uint(s), __float_as_uint(q)});
        if (g == 1) st64(k.rs, WS_PST + (tok >> 4) * 2048 + (ct * 2 + 1) * 128 + t * 8, (u32x2){0u, 0u});
        hv[0] = hq[0]; hv[1] = hq[1]; hv[2] = hq[2];
        const lds_ptr stash = k.lds + RED_LDS + tt * 5120 + lane * 8;
        *(LAS u32x2*)(stash + 3072) = hq[3]; *(LAS u32x2*)(stash + 4096) = hq[4];
    }
}
DI void pair_reduce(lds_ptr red0, int tt, int kh, int lane, const f32x4 (&acc)[5], f32x4 (&own)[3]) {
    const lds_ptr red = red0 + tt * 5120 + lane * 16;
    if (kh == 0) { *(LAS f32x4*)(red + 3072) = acc[3]; *(LAS f32x4*)(red + 4096) = acc[4]; }
    else { *(LAS f32x4*)(red) = acc[0]; *(LAS f32x4*)(red + 1024) = acc[1]; *(LAS f32x4*)(red + 2048) = acc[2]; }
    BAR_L();
    if (kh == 0) {
#pragma unroll
        for (int u = 0; u < 3; ++u) own[u] = acc[u] + *(const LAS f32x4*)(red + u * 1024);
    } else {
        own[0] = acc[3] + *(const LAS f32x4*)(red + 3072); own[1] = acc[4] + *(const LAS f32x4*)(red + 4096); own[2] = own[1];
    }
}
template <int N, int NP>
DI void w2_stage(const Ctx& k, int ct, int b, int s0, int w0) {
    const int nw = 8 - w0;
#pragma unroll
    for (int j = 0; j < NP; ++j) {
        int q = k.w - w0 + nw * j;
        if (q >= 10 * N) q -= nw;
        const int hf = q >= 5 * N, rem = q - hf * 5 * N, n = rem / 5;
        glds16<true>(k.lane * 16, k.ws + WS_W2 + (ct * 54 + hf * 27 + s0) * 5120 + rem * 1024, k.lds0 + n * 20480 + b * 10240 + hf * 5120 + (rem - 5 * n) * 1024);
    }
}
DI void p5_phase(const Ctx& k, int rb64, int ct, u32x2 (&hv)[3]) {
    const int lane = k.lane, tt = k.w & 3, kh = k.w >> 2, t = lane & 15, g = lane >> 4;
    const unsigned ap = WS_HH + (rb64 * 4 + tt) * HH_TILE + kh * 27 * 1024 + lane * 16;
    constexpr int WIN = 6;
    u32x4 a[27];
#pragma unroll
    for (int i = 0; i < WIN; ++i) ald128(a[i], k.rsw, ap, i * 1024);
    if (kh) {
        const lds_ptr stash = k.lds + RED_LDS + tt * 5120 + lane * 8;
        hv[0] = *(const LAS u32x2*)(stash + 3072); hv[1] = *(const LAS u32x2*)(stash + 4096); hv[2] = hv[1];
    }
    lds_ptr wb[3] = {k.lds + lane * 16 + kh * 5120, k.lds + lane * 16 + kh * 5120 + 61440, k.lds + lane * 16 + kh * 5120 + 122880};
    PIN(wb[0]); PIN(wb[1]); PIN(wb[2]);
    auto wread = [&](int n, int cf) {
        const int m = n % 7;
        return as_frag(lds_r128(wb[m / 3] + (m % 3) * 20480 + ((n / 7) & 1) * 10240 + cf * 1024));
    };
    bf16x8 wf[2][5];
#pragma unroll
    for (int cf = 0; cf < 5; ++cf) wf[0][cf] = wread(0, cf);
    f32x4 acc[5];
#pragma unroll
    for (int cf = 0; cf < 5; ++cf) { acc[cf] = (f32x4){0, 0, 0, 0}; PIN(acc[cf]); }
    mfma_fence();
    PRIO(1);
#define P5_STEP(i, NW) do { \
        if ((i) == 7) { BAR_L(); w2_stage<7, 9>(k, ct, 0, 14, 0); } \
        if ((i) == 13) { WAIT_BAR(6); w2_stage<6, 8>(k, ct, 1, 21, 0); } \
        if ((i) == 20) { WAIT_BAR(6); } \
        if ((i) + WIN < 27) ald128(a[(i) + WIN], k.rsw, ap, ((i) + WIN) * 1024); \
        ald_wait<NW>(a[i]); SBAR(); \
        _Pragma("unroll") for (int cf = 0; cf < 5; ++cf) { \
            if ((i) + 1 < 27) wf[((i) + 1) & 1][cf] = wread((i) + 1, cf); \
            mfma16_acc(acc[cf], wf[(i) & 1][cf], as_frag(a[i])); SBAR(); } } while (0)
    P5_STEP(0, 6); P5_STEP(1, 6); P5_STEP(2, 6); P5_STEP(3, 6); P5_STEP(4, 6); P5_STEP(5, 6); P5_STEP(6, 6);
    P5_STEP(7, 15); P5_STEP(8, 15); P5_STEP(9, 15); P5_STEP(10, 15); P5_STEP(11, 15); P5_STEP(12, 15);
    P5_STEP(13, 14); P5_STEP(14, 14); P5_STEP(15, 14); P5_STEP(16, 14); P5_STEP(17, 14); P5_STEP(18, 14); P5_STEP(19, 6);
    P5_STEP(20, 6); P5_STEP(21, 5); P5_STEP(22, 4); P5_STEP(23, 3); P5_STEP(24, 2); P5_STEP(25, 1); P5_STEP(26, 0);
#undef P5_STEP
    PRIO(0);
    mfma_fence();
    STAMP(16);
    f32x4 own[3];
    pair_reduce(k.lds + RED_LDS, tt, kh, lane, acc, own);
    const int tok = rb64 * 64 + tt * 16 + t;
#pragma unroll
    for (int u = 0; u < 3; ++u) {
        if (u < 2 || kh == 0) {
            const int col = ct * 80 + own_cf(kh, u) * 16 + 4 * g;
            *(u32x2*)(k.out + (size_t)tok * D + col) = (u32x2){cvtpk(own[u][0] + bflo(hv[u].x), own[u][1] + bfhi(hv[u].x)), cvtpk(own[u][2] + bflo(hv[u].y), own[u][3] + bfhi(hv[u].y))};
        }
    }
}

constexpr int P4_CS = 143360;
DI void w13_prefetch(const Ctx& k, int f0, int f) {
    const int p0 = f * 20;
#pragma unroll 4
    for (int j = 0; j < 20; ++j) glds16<true>(k.lane * 16, k.ws + WS_W13 + f0 * 20480 + (p0 + j) * 1024, k.lds0 + (p0 + j) * 1024);
}
DI void p4_colsum(const Ctx& k, int f) {
    const int t = k.lane & 15, g = k.lane >> 4;
    u32x4 wv[20];
#pragma unroll
    for (int s = 0; s < 20; ++s) wv[s] = lds_r128(k.lds + (f * 20 + s) * 1024 + k.lane * 16);
    f32x4 c0 = {0, 0, 0, 0}, c1 = {0, 0, 0, 0};
    const bf16x8 ones = {0x3F80, 0x3F80, 0x3F80, 0x3F80, 0x3F80, 0x3F80, 0x3F80, 0x3F80};
#pragma unroll
    for (int s = 0; s < 20; s += 2) { c0 = mfma16(as_frag(wv[s]), ones, c0); c1 = mfma16(as_frag(wv[s + 1]), ones, c1); }
    if (t == 0) *(LAS f32x4*)(k.lds + P4_CS + f * 64 + g * 16) = c0 + c1;
}
DI void p4_phase(const Ctx& k, int rb, int f0, int nf, int ct5) {
    const int lane = k.lane, w = k.w, t = lane & 15, g = lane >> 4;
    const lds_ptr lds = k.lds;
    const unsigned ap = WS_H + (rb * 16 + w * 2) * OH_TILE + lane * 16;
    u32x4 a0[20], a1[20];
#define P4_LD(s) do { a0[s] = ld128(k.rs, ap + (s) * 1024); a1[s] = ld128(k.rs, ap + OH_TILE + (s) * 1024); } while (0)
    u32x2 pv[2][4];
#pragma unroll
    for (int i = 0; i < 2; ++i)
#pragma unroll
        for (int j = 0; j < 4; ++j) pv[i][j] = ld64(k.rs, WS_PST + (rb * 16 + w * 2 + i) * 2048 + (4 * g + j) * 128 + t * 8);
    P4_LD(0); P4_LD(1); P4_LD(2);
    lds_ptr wb[3] = {lds + lane * 16, lds + lane * 16 + 61440, lds + lane * 16 + 122880};
    PIN(wb[1]); PIN(wb[2]);
    bf16x8 wf[2][7];
#pragma unroll
    for (int f = 0; f < 7; ++f) wf[0][f] = as_frag(lds_r128(wb[f / 3] + (f % 3) * 20480));
    f32x4 acc[2][7];
#pragma unroll
    for (int f = 0; f < 7; ++f) { acc[0][f] = (f32x4){0, 0, 0, 0}; acc[1][f] = (f32x4){0, 0, 0, 0}; PIN(acc[0][f]); PIN(acc[1][f]); }
    float mean[2], rstd[2], vpe[2];
    mfma_fence();
    STAMP(13);
    PRIO(1);
#pragma unroll
    for (int s = 0; s < 20; ++s) {
        if (s < 6) { P4_LD(2 * s + 3); P4_LD(2 * s + 4); } else if (s + 9 < 20) P4_LD(s + 9);
        SBAR();
        if (s == 10) { __builtin_amdgcn_s_barrier(); w2_stage<7, 9>(k, ct5, 0, 0, 0); SBAR(); }
        if (s == 4) {
#pragma unroll
            for (int i = 0; i < 2; ++i) {
                float sm = (__uint_as_float(pv[i][0].x) + __uint_as_float(pv[i][1].x)) + (__uint_as_float(pv[i][2].x) + __uint_as_float(pv[i][3].x));
                float q = (__uint_as_float(pv[i][0].y) + __uint_as_float(pv[i][1].y)) + (__uint_as_float(pv[i][2].y) + __uint_as_float(pv[i][3].y));
                sm = sum_x32(sum_x16(sm)); q = sum_x32(sum_x16(q));
                mean[i] = sm * (1.0f / D);
                vpe[i] = fmaxf(q * (1.0f / D) - mean[i] * mean[i], 0.f) + LN_EPS;
                rstd[i] = __builtin_amdgcn_rsqf(vpe[i]);
            }
            PIN(mean[0]); PIN(mean[1]); PIN(rstd[0]); PIN(rstd[1]); PIN(vpe[0]); PIN(vpe[1]);
            SBAR();
        }
#pragma unroll
        for (int f = 0; f < 7; ++f) {
            if (s + 1 < 20) wf[(s + 1) & 1][f] = as_frag(lds_r128(wb[f / 3] + (f % 3) * 20480 + (s + 1) * 1024));
            mfma16_acc(acc[0][f], wf[s & 1][f], as_frag(a0[s]));
            mfma16_acc(acc[1][f], wf[s & 1][f], as_frag(a1[s]));
            SBAR();
        }
    }
#undef P4_LD
    PRIO(0);
    mfma_fence();
    STAMP(14);
    f32x4 cs[7];
#pragma unroll
    for (int f = 0; f < 7; ++f) cs[f] = *(const LAS f32x4*)(lds + P4_CS + f * 64 + g * 16);
#pragma unroll
    for (int i = 0; i < 2; ++i) {
        const float ce = -LOG2E * rstd[i], ir2 = vpe[i], nm = -mean[i];
        const f32x2 ce2 = {ce, ce}, ir22 = {ir2, ir2}, nm2 = {nm, nm};
        unsigned d[8];
#pragma unroll
        for (int f = 0; f < 7; ++f) {
            const f32x2 ga = __builtin_elementwise_fma(nm2, __builtin_shufflevector(cs[f], cs[f], 0, 1), __builtin_shufflevector(acc[i][f], acc[i][f], 0, 1));
            const f32x2 ua = __builtin_elementwise_fma(nm2, __builtin_shufflevector(cs[f], cs[f], 2, 3), __builtin_shufflevector(acc[i][f], acc[i][f], 2, 3));
            const f32x2 t2 = ga * ce2;
            const f32x2 e2 = {EX(t2.x), EX(t2.y)};
            const f32x2 d2 = __builtin_elementwise_fma(e2, ir22, ir22);
            const f32x2 r2 = {__builtin_amdgcn_rcpf(d2.x), __builtin_amdgcn_rcpf(d2.y)};
            const f32x2 o2 = (ga * ua) * r2;
            d[f] = cvtpk(o2.x, o2.y);
        }
        d[7] = 0;
        const unsigned dst = WS_HH + (rb * 16 + w * 2 + i) * HH_TILE + f0 * 256 + lane * 16;
#pragma unroll
        for (int u = 0; u < 2; ++u) {
            auto a = __builtin_amdgcn_permlane32_swap(d[4 * u], d[4 * u + 2], false, false);
            auto b = __builtin_amdgcn_permlane32_swap(d[4 * u + 1], d[4 * u + 3], false, false);
            auto e = __builtin_amdgcn_permlane16_swap(a[0], b[0], false, false);
            auto f = __builtin_amdgcn_permlane16_swap(a[1], b[1], false, false);
            if (4 * u + g < nf) st128(k.rs, dst + u * 1024, (u32x4){e[0], e[1], f[0], f[1]});
        }
    }
}

constexpr int LDS_BYTES = 163840;
__global__ __launch_bounds__(512, 2) void fused_block(const uint16_t* __restrict__ x, const uint16_t* __restrict__ wqkv, const uint16_t* __restrict__ wo,
                                                      const uint16_t* __restrict__ w1, const uint16_t* __restrict__ w3, const uint16_t* __restrict__ w2,
                                                      char* __restrict__ ws, uint16_t* __restrict__ out) {
    extern __shared__ __attribute__((aligned(16))) char smem[];
    Ctx k;
    k.x = x; k.wqkv = wqkv; k.wo = wo; k.w1 = w1; k.w3 = w3; k.w2 = w2; k.ws = ws; k.out = out;
    k.rs = __builtin_amdgcn_make_buffer_rsrc(ws, 0, WS_END + 20480, 0x00020000);
    k.lds = (lds_ptr)smem; k.lds0 = (unsigned)(uintptr_t)smem;
    k.c = blockIdx.x; k.xcd = k.c & 7; k.lb = k.c >> 3;
    k.tid = threadIdx.x; k.lane = k.tid & 63; k.w = rfl(k.tid >> 6);
    k.rsw = (u32x4){(unsigned)(uintptr_t)ws, (unsigned)((uintptr_t)ws >> 32) & 0xffffu, WS_END + 20480, 0x00020000u};
    k.tag = 0;
    k.sc = 0; k.sr = 0;
    STAMP(0);
    startup_load(k);
    const int rb1 = (k.xcd >> 2) * 8 + (k.lb & 7), cb1 = (k.xcd & 3) * 4 + (k.lb >> 3);
    if (cb1 < 15) {
        if (cb1 >= 10) p1_body<true>(k, rb1, cb1); else p1_body<false>(k, rb1, cb1);
        STAMP(3);
        WAIT_BAR(0);
        if (k.tid == 0) st_flag(ws + WS_F1 + (rb1 * 16 + cb1) * FLAG_STRIDE, k.tag);
    } else {
        WAIT_VM(0);
        startup_tag(k);
        ln1_stats(k, rb1);
        if (rb1 < 15) colsum_qkv(k, rb1);
        WAIT_BAR(0);
    }
    STAMP(4);
    if (k.w) pretile_load(k);
    const int h = k.xcd * 4 + (k.lb & 3), qb = k.lb >> 2;
    const int rb64 = k.xcd * 4 + (k.lb >> 3), ct = k.lb & 7;
    const int rb4 = (k.xcd >> 1) * 2 + (k.lb >> 4), cb4 = (k.xcd & 1) * 16 + (k.lb & 15);
    const int f0 = cb4 < 24 ? cb4 * 7 : 168 + (cb4 - 24) * 6, nf = cb4 < 24 ? 7 : 6;
    AttnCtx ac;
    attn_setup(k, ac, h);
    if (k.w == 0) {
        const int c0 = (20 * h) >> 7, c1 = (20 * h + 19) >> 7, j = k.lane & 15, sel = k.lane >> 4;
        const int cbk = (sel & 1) ? c1 : c0;
        const unsigned o0 = WS_F1 + (j * 16 + (sel < 2 ? 5 : 10) + cbk) * FLAG_STRIDE;
        const unsigned o1 = WS_F1 + ((2 * qb + (k.lane & 1)) * 16 + ((k.lane & 2) ? c1 : c0)) * FLAG_STRIDE;
        poll_flags2(k, o0, o1);
    }
    WAIT_BAR(0);
    STAMP(5);
    p2_phase(k, ac, h, qb, ct, f0);
    STAMP(8);
    WAIT_BAR(0);
    if (k.tid == 0) st_flag(ws + WS_F2 + (qb * 32 + h) * FLAG_STRIDE, k.tag);
    P3Pre pre;
    p3_pre(k, rb64, ct, pre);
    if (k.w == 0) poll_flags(k, WS_F2 + (k.xcd * 32 + (k.lane & 31)) * FLAG_STRIDE);
    else if (k.w <= 3) w13_prefetch(k, f0, k.w - 1);
    WAIT_BAR(0);
    if (k.c == 0 && k.tid == 0) st_flag(ws + WS_EPOCH, k.tag);
    STAMP(9);
    u32x2 hv[3];
    p3_phase(k, rb64, ct, f0, pre, hv);
    STAMP(11);
    if (k.w < 4) WAIT_VM(0);
    BAR_L();
    if (k.tid == 0) st_flag(ws + WS_F3 + (k.xcd * 32 + k.lb) * FLAG_STRIDE, k.tag);
    if (k.w == 0) poll_flags(k, WS_F3 + (rb4 * 32 + (k.lane & 31)) * FLAG_STRIDE);
    else {
        WAIT_VM(0);
        p4_colsum(k, k.w - 1);
    }
    WAIT_BAR(0);
    STAMP(12);
    p4_phase(k, rb4, f0, nf, ct);
    STAMP(15);
    WAIT_BAR(0);
    if (k.tid == 0) st_flag(ws + WS_F4 + (rb4 * 32 + cb4) * FLAG_STRIDE, k.tag);
    if (k.w == 0) poll_flags(k, WS_F4 + ((rb64 >> 2) * 32 + (k.lane & 31)) * FLAG_STRIDE);
    else {
        w2_stage<7, 10>(k, ct, 1, 7, 1);
        const int li = (k.w - 1) * 64 + k.lane;
        if (k.w <= 5) {
            const int ln = (k.lb >> 3) * 260 + (li < 260 ? li : li - 64), hf = ln >= 520;
            const unsigned warm = ld32(k.rs, WS_W2 + (ct * 54 + hf * 27 + 14) * 5120 + (ln - hf * 520) * 128);
            asm volatile("" :: "v"(warm));
        }
    }
    WAIT_BAR(0);
    STAMP(17);
    p5_phase(k, rb64, ct, hv);
    STAMP(18);
    STAMP_FLUSH();
}

extern "C" void kernel_launch(void* const* d_in, const int* in_sizes, int n_in, void* d_out, int out_size, void* d_ws, size_t ws_size, hipStream_t stream) {
    (void)hipFuncSetAttribute((const void*)fused_block, hipFuncAttributeMaxDynamicSharedMemorySize, LDS_BYTES);
    fused_block<<<256, 512, LDS_BYTES, stream>>>((const uint16_t*)d_in[0], (const uint16_t*)d_in[1], (const uint16_t*)d_in[2], (const uint16_t*)d_in[4],
                                                 (const uint16_t*)d_in[5], (const uint16_t*)d_in[6], (char*)d_ws, (uint16_t*)d_out);
}
```

```cpp
#include <hip/hip_runtime.h>
#include <hip/hip_bf16.h>
#include <stdint.h>

#define DI __device__ __forceinline__
#define LAS __attribute__((address_space(3)))
#define GAS __attribute__((address_space(1)))
typedef short bf16x8 __attribute__((ext_vector_type(8)));
typedef short s16x4 __attribute__((ext_vector_type(4)));
typedef float f32x2 __attribute__((ext_vector_type(2)));
typedef float f32x4 __attribute__((ext_vector_type(4)));
typedef float f32x16 __attribute__((ext_vector_type(16)));
typedef unsigned u32x2 __attribute__((ext_vector_type(2)));
typedef unsigned u32x4 __attribute__((ext_vector_type(4)));
typedef unsigned long long u64;
typedef __bf16 bf16x2_t __attribute__((ext_vector_type(2)));
typedef short v4i16_t __attribute__((ext_vector_type(4)));
typedef LAS char* lds_ptr;
typedef __amdgpu_buffer_rsrc_t rsrc_t;

constexpr int S = 2048, D = 640, H = 32, DH = 20, F = 1728, NQKV = 3 * D;
constexpr float LN_EPS = 1e-5f;
constexpr float LOG2E = 1.4426950408889634f;
constexpr float QSCALE = 0.22360679774997896f * LOG2E;

constexpr unsigned WS_EPOCH = 0;
constexpr unsigned FLAG_STRIDE = 64, FLAG_ARR = 256 * FLAG_STRIDE;
constexpr unsigned WS_F1 = 4096, WS_FP = WS_F1 + FLAG_ARR, WS_F2 = WS_FP + FLAG_ARR, WS_F3 = WS_F2 + FLAG_ARR, WS_F4 = WS_F3 + FLAG_ARR;
constexpr unsigned WS_ST1 = 131072;
constexpr unsigned WS_CSQ = WS_ST1 + 32768;
constexpr unsigned WS_PST = 262144;
constexpr unsigned WS_Q = 524288;
constexpr unsigned IMG = S * D * 2;
constexpr unsigned QK_TILE = 32 * 1280, QK_IMG = 64 * QK_TILE;
constexpr unsigned WS_K = WS_Q + QK_IMG;
constexpr unsigned WS_V = WS_K + QK_IMG;
constexpr unsigned OH_TILE = 21504, HH_TILE = 56320;
constexpr unsigned WS_O = WS_V + IMG;
constexpr unsigned WS_H = WS_O + 128 * OH_TILE;
constexpr unsigned WS_HH = WS_H + 128 * OH_TILE;
constexpr unsigned WS_WO = WS_HH + 128 * HH_TILE;
constexpr unsigned WS_W13 = WS_WO + D * D * 2;
constexpr unsigned WS_W2 = WS_W13 + D * F * 4;
constexpr unsigned WS_END = WS_W2 + F * D * 2;

DI unsigned cvtpk(float lo, float hi) { f32x2 v = {lo, hi}; bf16x2_t b = __builtin_convertvector(v, bf16x2_t); return __builtin_bit_cast(unsigned, b); }
DI float bflo(unsigned u) { return __uint_as_float(u << 16); }
DI float bfhi(unsigned u) { return __uint_as_float(u & 0xffff0000u); }
DI f32x4 mfma16(bf16x8 a, bf16x8 b, f32x4 c) { return __builtin_amdgcn_mfma_f32_16x16x32_bf16(a, b, c, 0, 0, 0); }
DI void mfma16_acc(f32x4& c, bf16x8 a, bf16x8 b) { asm volatile("v_mfma_f32_16x16x32_bf16 %0, %1, %2, %0" : "+v"(c) : "v"(a), "v"(b)); }
DI void mfma_fence() { __builtin_amdgcn_sched_barrier(0); asm volatile("s_nop 7\n\ts_nop 1" ::: "memory"); __builtin_amdgcn_sched_barrier(0); }
DI f32x16 mfma32(bf16x8 a, bf16x8 b, f32x16 c) { return __builtin_amdgcn_mfma_f32_32x32x16_bf16(a, b, c, 0, 0, 0); }
DI int rfl(int x) { return __builtin_amdgcn_readfirstlane(x); }
template <bool SC1>
DI void glds16(unsigned voff, const void* sbase, unsigned ldsdst) {
    unsigned keep;
    if (SC1) asm volatile("s_mov_b32 %0, m0\n\ts_mov_b32 m0, %3\n\ts_nop 0\n\tglobal_load_lds_dwordx4 %1, %2 sc1\n\ts_mov_b32 m0, %0" : "=&s"(keep) : "v"(voff), "s"(sbase), "s"(ldsdst) : "memory");
    else asm volatile("s_mov_b32 %0, m0\n\ts_mov_b32 m0, %3\n\ts_nop 0\n\tglobal_load_lds_dwordx4 %1, %2\n\ts_mov_b32 m0, %0" : "=&s"(keep) : "v"(voff), "s"(sbase), "s"(ldsdst) : "memory");
}
DI void glds4(const void* gptr, unsigned ldsdst) {
    unsigned keep;
    asm volatile("s_mov_b32 %0, m0\n\ts_mov_b32 m0, %2\n\ts_nop 0\n\tglobal_load_lds_dword %1, off\n\ts_mov_b32 m0, %0" : "=&s"(keep) : "v"(gptr), "s"(ldsdst) : "memory");
}
#define WAIT_VM(n) asm volatile("s_waitcnt vmcnt(" #n ")" ::: "memory")
#define WAIT_BAR(n) asm volatile("s_waitcnt vmcnt(" #n ") lgkmcnt(0)\n\ts_barrier" ::: "memory")
#define BAR_L() asm volatile("s_waitcnt lgkmcnt(0)\n\ts_barrier" ::: "memory")
#define SBAR() __builtin_amdgcn_sched_barrier(0)
#define PIN(x) asm volatile("" : "+v"(x))
#define PRIO(n) __builtin_amdgcn_s_setprio(n)
#define EX(v) __builtin_amdgcn_exp2f(v)
DI u32x4 lds_r128(lds_ptr p) { return *(const LAS u32x4*)p; }
DI s16x4 lds_tr(lds_ptr p) { return __builtin_bit_cast(s16x4, __builtin_amdgcn_ds_read_tr16_b64_v4i16((LAS v4i16_t*)p)); }
DI bf16x8 as_frag(u32x4 v) { return __builtin_bit_cast(bf16x8, v); }
DI bf16x8 cat_frag(s16x4 a, s16x4 b) { return (bf16x8){a[0], a[1], a[2], a[3], b[0], b[1], b[2], b[3]}; }
DI float swap32_max(float m) { auto r = __builtin_amdgcn_permlane32_swap(__float_as_uint(m), __float_as_uint(m), false, false); return fmaxf(__uint_as_float(r[0]), __uint_as_float(r[1])); }
DI float sum_x16(float v) { auto r = __builtin_amdgcn_permlane16_swap(__float_as_uint(v), __float_as_uint(v), false, false); return __uint_as_float(r[0]) + __uint_as_float(r[1]); }
DI float sum_x32(float v) { auto r = __builtin_amdgcn_permlane32_swap(__float_as_uint(v), __float_as_uint(v), false, false); return __uint_as_float(r[0]) + __uint_as_float(r[1]); }
DI u32x4 ld128(rsrc_t r, unsigned off) { return __builtin_amdgcn_raw_buffer_load_b128(r, off, 0, 16); }
DI u32x2 ld64(rsrc_t r, unsigned off) { return __builtin_amdgcn_raw_buffer_load_b64(r, off, 0, 16); }
DI unsigned ld32(rsrc_t r, unsigned off) { return __builtin_amdgcn_raw_buffer_load_b32(r, off, 0, 16); }
DI void st128(rsrc_t r, unsigned off, u32x4 v) { __builtin_amdgcn_raw_buffer_store_b128(v, r, off, 0, 16); }
DI void st64(rsrc_t r, unsigned off, u32x2 v) { __builtin_amdgcn_raw_buffer_store_b64(v, r, off, 0, 16); }

DI void ald128(u32x4& v, u32x4 rsw, unsigned voff, unsigned soff) { asm volatile("buffer_load_dwordx4 %0, %1, %2, %3 offen sc1" : "=&v"(v) : "v"(voff), "s"(rsw), "s"(soff) : "memory"); }
template <int N> DI void ald_wait(u32x4& v) { asm volatile("s_waitcnt vmcnt(%1)" : "+v"(v) : "i"(N) : "memory"); }
struct Ctx {
    const uint16_t *x, *wqkv, *wo, *w1, *w3, *w2;
    char* ws; uint16_t* out;
    rsrc_t rs; u32x4 rsw; u64 tag; lds_ptr lds; unsigned lds0;
    int c, xcd, lb, tid, lane, w;
    mutable unsigned sc, sr;
};

#define STAMP(i)
#define STAMP_FLUSH()

DI u64 ld_flag(const char* p) { return __hip_atomic_load((const GAS u64*)p, __ATOMIC_RELAXED, __HIP_MEMORY_SCOPE_AGENT); }
DI void st_flag(char* p, u64 v) { __hip_atomic_store((GAS u64*)p, v, __ATOMIC_RELAXED, __HIP_MEMORY_SCOPE_AGENT); }
constexpr unsigned SPIN_CAP = 1u << 18;
DI void poll_flags(const Ctx& k, unsigned off) {
    for (unsigned spins = 0; spins < SPIN_CAP; ++spins) {
        if (__all(ld_flag(k.ws + off) == k.tag)) break;
        __builtin_amdgcn_s_sleep(1);
    }
}
DI void poll_flags2(const Ctx& k, unsigned off0, unsigned off1) {
    for (unsigned spins = 0; spins < SPIN_CAP; ++spins) {
        const u64 a = ld_flag(k.ws + off0), b = ld_flag(k.ws + off1);
        if (__all(a == k.tag && b == k.tag)) break;
        __builtin_amdgcn_s_sleep(1);
    }
}
DI void poll_all(const Ctx& k, unsigned arr) {
    for (unsigned spins = 0; spins < SPIN_CAP; ++spins) {
        const unsigned o = arr + k.lane * FLAG_STRIDE;
        const u64 a = ld_flag(k.ws + o), b = ld_flag(k.ws + o + 64 * FLAG_STRIDE), c = ld_flag(k.ws + o + 128 * FLAG_STRIDE), d = ld_flag(k.ws + o + 192 * FLAG_STRIDE);
        if (__all(a == k.tag && b == k.tag && c == k.tag && d == k.tag)) break;
        __builtin_amdgcn_s_sleep(1);
    }
}

constexpr int SU_LDS = 131072;
DI void startup_load(const Ctx& k) { glds4(k.ws + WS_EPOCH + (k.lane & 1) * 4, k.lds0 + SU_LDS + k.w * 256); }
DI void startup_tag(Ctx& k) {
    const u32x2 e = *(const LAS u32x2*)(k.lds + SU_LDS + k.w * 256);
    k.tag = (((u64)(unsigned)rfl(e.y) << 32) | (unsigned)rfl(e.x)) + 1;
}
DI void ln1_stats(const Ctx& k, int j) {
    u32x4 v[2][10];
#pragma unroll
    for (int p = 0; p < 2; ++p)
#pragma unroll
        for (int i = 0; i < 10; ++i) v[p][i] = *(const u32x4*)(k.x + (size_t)(128 * j + 16 * k.w + 8 * p + (k.lane >> 3)) * D + ((k.lane & 7) + 8 * i) * 8);
#pragma unroll
    for (int p = 0; p < 2; ++p) {
        float s = 0.f, q = 0.f;
#pragma unroll
        for (int i = 0; i < 10; ++i)
#pragma unroll
            for (int e = 0; e < 4; ++e) { const float a = bflo(v[p][i][e]), b = bfhi(v[p][i][e]); s += a + b; q = fmaf(a, a, fmaf(b, b, q)); }
#pragma unroll
        for (int o = 4; o >= 1; o >>= 1) { s += __shfl_xor(s, o); q += __shfl_xor(q, o); }
        const float mean = s * (1.0f / D), var = fmaxf(q * (1.0f / D) - mean * mean, 0.f);
        const float val = (k.lane & 1) ? 1.0f / sqrtf(var + LN_EPS) : mean;
        const int row = 128 * j + 16 * k.w + 8 * p + (k.lane >> 3);
        if ((k.lane & 7) < 2) st64(k.rs, WS_ST1 + row * 16 + (k.lane & 1) * 8, (u32x2){__float_as_uint(val), (unsigned)k.tag});
    }
}
DI void colsum_qkv(const Ctx& k, int cb) {
    float a[8] = {0, 0, 0, 0, 0, 0, 0, 0};
    u32x4 v[20];
#pragma unroll
    for (int i = 0; i < 20; ++i) v[i] = *(const u32x4*)(k.wqkv + (size_t)(k.w * 80 + i * 4 + (k.lane >> 4)) * NQKV + cb * 128 + (k.lane & 15) * 8);
#pragma unroll
    for (int i = 0; i < 20; ++i)
#pragma unroll
        for (int j = 0; j < 4; ++j) { a[2 * j] += bflo(v[i][j]); a[2 * j + 1] += bfhi(v[i][j]); }
    LAS float* red = (LAS float*)k.lds;
#pragma unroll
    for (int j = 0; j < 8; ++j) { a[j] += __shfl_xor(a[j], 16); a[j] += __shfl_xor(a[j], 32); }
    if (k.lane < 16) {
#pragma unroll
        for (int j = 0; j < 8; ++j) red[k.w * 128 + k.lane * 8 + j] = a[j];
    }
    BAR_L();
    if (k.tid < 128) {
        float t = 0.f;
#pragma unroll
        for (int i = 0; i < 8; ++i) t += red[i * 128 + k.tid];
        st64(k.rs, WS_CSQ + (cb * 128 + k.tid) * 8, (u32x2){__float_as_uint(t), (unsigned)k.tag});
    }
}

constexpr int PT_LDS = 131072;
DI int pt_off(int i) { return PT_LDS + (i < 3 ? i * 8192 : 24576 + (i - 3) * 4096); }
DI void pretile_load(const Ctx& k) {
    const int wv = k.w - 1;
#pragma unroll
    for (int i = 0; i < 5; ++i) {
        const int u = k.c + 256 * i;
        if (u < 540) {
            const int s = u % 20, fb = u / 20;
            const uint16_t* src = ((k.lane & 1) ? k.w3 : k.w1) + fb * 64 + (k.lane >> 1) * 2 + (size_t)32 * s * F;
            for (int r = wv; r < 32; r += 7) glds4(src + (size_t)r * F, k.lds0 + pt_off(i) + r * 256);
        } else {
            const int pc = (i & 1) ? wv - 3 : wv;
            if (pc >= 0 && pc < 4) {
                const bool is2 = u < 1080;
                const int uu = is2 ? u - 540 : u - 1080, s = uu / 10, nb = uu % 10, r = 8 * pc + (k.lane >> 3);
                glds16<false>((32 * s + r) * (D * 2) + nb * 128 + (k.lane & 7) * 16, is2 ? k.w2 : k.wo, k.lds0 + pt_off(i) + pc * 1024);
            }
        }
    }
}
struct PreTile { u32x4 v[5]; };
DI void pretile_read(const Ctx& k, PreTile& p) {
    const int g = k.lane >> 4, q = (k.lane >> 2) & 3, pp = k.lane & 3;
#pragma unroll
    for (int i = 0; i < 5; ++i) {
        const int u = k.c + 256 * i;
        const bool wide = u < 540;
        const int rs = wide ? 256 : 128, f = wide ? k.w : (k.w & 3);
        const lds_ptr a = k.lds + pt_off(i) + (8 * g + q) * rs + f * 32 + pp * 8;
        p.v[i] = __builtin_bit_cast(u32x4, cat_frag(lds_tr(a), lds_tr(a + 4 * rs)));
    }
}
DI void pretile_store(const Ctx& k, const PreTile& p) {
#pragma unroll
    for (int i = 0; i < 5; ++i) {
        const int u = k.c + 256 * i;
        if (u < 540) st128(k.rs, WS_W13 + (((u / 20) * 8 + k.w) * 20 + u % 20) * 1024 + k.lane * 16, p.v[i]);
        else if ((k.w >> 2) == (i & 1)) {
            const bool is2 = u < 1080;
            const int uu = is2 ? u - 540 : u - 1080, s = uu / 10, cfg = (uu % 10) * 4 + (k.w & 3), ct = cfg / 5, cf = cfg % 5;
            st128(k.rs, (is2 ? WS_W2 + ((ct * 54 + s) * 5 + cf) * 1024 : WS_WO + ((ct * 20 + s) * 5 + cf) * 1024) + k.lane * 16, p.v[i]);
        }
    }
}

constexpr int P1_SLOT = 32768;
struct P1Frag { bf16x8 a[4], b[2]; };
template <bool ISV> DI void p1_mma(f32x4 (&acc)[4][2], const P1Frag& f, int m, int n) { if (ISV) mfma16_acc(acc[m][n], f.a[m], f.b[n]); else mfma16_acc(acc[m][n], f.b[n], f.a[m]); }
template <bool ISV>
DI void p1_body(Ctx& k, int rb, int cb) {
    const int lane = k.lane, w = k.w, th = w >> 2, cg = w & 3, t = lane & 15, g = lane >> 4;
    unsigned voffA[2], voffW[2];
#pragma unroll
    for (int i = 0; i < 2; ++i) {
        const int ra = 8 * (2 * w + i) + (lane >> 3), ca = (lane & 7) ^ ((ra >> 1) & 7);
        voffA[i] = ra * (D * 2) + ca * 16;
        const int rw = 4 * (2 * w + i) + (lane >> 4), fw = (rw & 3) | (((rw >> 3) & 1) << 2), cw = (lane & 15) ^ (fw << 1);
        voffW[i] = rw * (NQKV * 2) + cw * 16;
    }
    const char* gA = (const char*)k.x + (size_t)rb * 128 * D * 2;
    const char* gW = (const char*)k.wqkv + cb * 256;
    auto stage = [&](int kt) {
        const unsigned sl = k.lds0 + (kt & 3) * P1_SLOT + w * 2048;
#pragma unroll
        for (int i = 0; i < 2; ++i) glds16<false>(voffA[i], gA + kt * 128, sl + i * 1024);
#pragma unroll
        for (int i = 0; i < 2; ++i) glds16<false>(voffW[i], gW + (size_t)kt * 64 * NQKV * 2, sl + 16384 + i * 1024);
    };
    stage(0); stage(1); stage(2); stage(3);
    int aoff[2], woff[2];
    aoff[0] = (th * 64 + t) * 128 + ((g ^ (t >> 1)) << 4); aoff[1] = aoff[0] ^ 64;
    const int q4 = (lane >> 2) & 3, p = lane & 3, fw = q4 | ((g & 1) << 2);
#pragma unroll
    for (int n = 0; n < 2; ++n) woff[n] = 16384 + (8 * g + q4) * 256 + (((2 * cg + n) ^ fw) << 5) + p * 8;
    f32x4 acc[4][2];
#pragma unroll
    for (int m = 0; m < 4; ++m) { acc[m][0] = (f32x4){0, 0, 0, 0}; acc[m][1] = (f32x4){0, 0, 0, 0}; PIN(acc[m][0]); PIN(acc[m][1]); }
    mfma_fence();
    P1Frag f0, f1;
#define P1_RA(F_, sl, ks, m) F_.a[m] = as_frag(lds_r128(sl + aoff[ks] + (m) * 2048))
#define P1_RB(F_, sl, ks, n) F_.b[n] = cat_frag(lds_tr(sl + woff[n] + (ks) * 8192), lds_tr(sl + woff[n] + (ks) * 8192 + 1024))
#define P1_STEP(RD, FN, sl, ks, FC) do { \
        if (RD) P1_RB(FN, sl, ks, 0); p1_mma<ISV>(acc, FC, 0, 0); SBAR(); \
        if (RD) P1_RA(FN, sl, ks, 0); p1_mma<ISV>(acc, FC, 0, 1); SBAR(); \
        if (RD) P1_RA(FN, sl, ks, 1); p1_mma<ISV>(acc, FC, 1, 0); SBAR(); \
        if (RD) P1_RB(FN, sl, ks, 1); p1_mma<ISV>(acc, FC, 1, 1); SBAR(); \
        if (RD) P1_RA(FN, sl, ks, 2); p1_mma<ISV>(acc, FC, 2, 0); SBAR(); \
        if (RD) P1_RA(FN, sl, ks, 3); p1_mma<ISV>(acc, FC, 2, 1); SBAR(); \
        p1_mma<ISV>(acc, FC, 3, 0); p1_mma<ISV>(acc, FC, 3, 1); SBAR(); } while (0)
    WAIT_BAR(12);
    STAMP(1);
    startup_tag(k);
    { const lds_ptr sl = k.lds; P1_RB(f0, sl, 0, 0); P1_RB(f0, sl, 0, 1); P1_RA(f0, sl, 0, 0); P1_RA(f0, sl, 0, 1); P1_RA(f0, sl, 0, 2); P1_RA(f0, sl, 0, 3); SBAR(); }
    u32x2 gr = {0, 0};
    PRIO(1);
    const unsigned groff = k.tid < 256 ? WS_ST1 + (rb * 128) * 16 + k.tid * 8 : WS_CSQ + (cb * 128 + (k.tid & 127)) * 8;
#pragma unroll
    for (int kt = 0; kt < 10; ++kt) {
        const lds_ptr sl = k.lds + (kt & 3) * P1_SLOT, sn = k.lds + ((kt + 1) & 3) * P1_SLOT;
        P1_STEP(true, f1, sl, 1, f0);
        if (kt < 9) {
            if (kt < 7) WAIT_BAR(8); else if (kt == 7) WAIT_BAR(4); else WAIT_BAR(0);
            if (kt == 6) gr = ld64(k.rs, groff);
            if (kt + 4 < 10) stage(kt + 4);
            SBAR();
        }
        P1_STEP(kt < 9, f0, sn, 0, f1);
    }
#undef P1_RA
#undef P1_RB
#undef P1_STEP
    PRIO(0);
    mfma_fence();
    STAMP(2);
    for (unsigned spins = 0; spins < SPIN_CAP; ++spins) {
        if (__all(gr.y == (unsigned)k.tag)) break;
        gr = ld64(k.rs, groff);
    }
    LAS float* sst = (LAS float*)k.lds;
    if (k.tid < 384) sst[k.tid] = __uint_as_float(gr.x);
    BAR_L();
    const LAS f32x2* st = (const LAS f32x2*)sst + th * 64;
    const LAS float* cs = sst + 256 + cg * 32;
    if (!ISV) {
        const float osc = cb < 5 ? QSCALE : 1.0f;
        const unsigned img = cb < 5 ? WS_Q : WS_K;
        const int cbs = cb % 5;
        unsigned dst[2]; f32x4 c4[2];
#pragma unroll
        for (int n = 0; n < 2; ++n) {
            const int cgi = cbs * 32 + cg * 8 + n * 4 + g, hd = cgi / 5, gi = cgi - 5 * hd;
            const int blk = gi == 4 ? 4 : ((gi & 1) << 1) | (gi >> 1);
            dst[n] = img + (rb * 4 + th * 2) * QK_TILE + hd * 1280 + blk * 256 + t * 8;
            c4[n] = *(const LAS f32x4*)(cs + n * 16 + 4 * g);
        }
#pragma unroll
        for (int m = 0; m < 4; ++m) {
            const f32x2 sm = st[m * 16 + t];
            const float r = sm.y * osc;
#pragma unroll
            for (int n = 0; n < 2; ++n) {
                const f32x4 y = (acc[m][n] - c4[n] * sm.x) * r;
                const unsigned o = dst[n] + (m >> 1) * QK_TILE + (m & 1) * 128;
                st64(k.rs, o, (u32x2){cvtpk(y[0], y[1]), cvtpk(y[2], y[3])});
            }
        }
    } else {
        const int pos = ((g & 1) << 1) | (g >> 1);
        const unsigned dst = WS_V + (rb * 8 + th * 4) * 20480 + ((cb - 10) * 128 + cg * 32 + t) * 32 + pos * 8;
        const float c1[2] = {cs[t], cs[16 + t]};
#pragma unroll
        for (int m = 0; m < 4; ++m) {
            f32x2 sm[4];
#pragma unroll
            for (int i = 0; i < 4; ++i) sm[i] = st[m * 16 + 4 * g + i];
#pragma unroll
            for (int n = 0; n < 2; ++n) {
                float y[4];
#pragma unroll
                for (int i = 0; i < 4; ++i) y[i] = (acc[m][n][i] - c1[n] * sm[i].x) * sm[i].y;
                st64(k.rs, dst + m * 20480 + n * 512, (u32x2){cvtpk(y[0], y[1]), cvtpk(y[2], y[3])});
            }
        }
    }
}

constexpr int AT_KT = 8192, AT_KB = 10240, AT_HALF = AT_KB + 10240, AT_BUF = 2 * AT_HALF;
constexpr int AT_ONES = AT_BUF, AT_ONES_BYTES = 10752, AT_KC = AT_ONES + AT_ONES_BYTES, AT_KC_BYTES = 2048, AT_FLAG = AT_KC + AT_KC_BYTES, WO_LDS = 61440, AT_RED = 0;
DI int at_slot(int i) { return i == 0 ? 0 : (i == 1 ? 61440 : 61440 + AT_BUF); }
struct AttnCtx {
    unsigned voff[5]; const char* gsrc; unsigned ldst, sstride;
    u32x4 qf0[2]; u32x2 qf1[2];
    int kofs, kt, vlane; bool vdata, hi;
};
DI void attn_stage(const AttnCtx& c, int sidx, int slot) {
#pragma unroll
    for (int i = 0; i < 5; ++i) glds16<true>(c.voff[i], c.gsrc + sidx * c.sstride, c.ldst + at_slot(slot) + i * 1024);
}
DI void attn_setup(const Ctx& k, AttnCtx& c, int h) {
    const int lane = k.lane, w = k.w, kh = w >> 2, r = lane & 31, hi = lane >> 5;
    const bool stV = (w >> 1) & 1;
#pragma unroll
    for (int i = 0; i < 5; ++i) {
        const int pc = 5 * (w & 1) + i;
        if (!stV) c.voff[i] = pc < 8 ? pc * QK_TILE + lane * 16 : ((pc - 8) * 4 + (lane >> 4)) * QK_TILE + 1024 + (lane & 15) * 16;
        else { const int ci = 64 * pc + lane, st = ci / 40, wi = ci % 40; c.voff[i] = st * 20480 + ((wi % 20) * 2 + wi / 20) * 16; }
    }
    c.gsrc = k.ws + (stV ? WS_V + h * 640 + kh * 64 * 20480 : WS_K + h * 1280 + kh * 32 * QK_TILE);
    c.sstride = stV ? 16 * 20480 : 8 * QK_TILE;
    c.ldst = k.lds0 + kh * AT_HALF + (stV ? AT_KB : 0) + (w & 1) * 5120;
    c.kofs = kh * AT_HALF + hi * 256 + r * 8;
    c.hi = hi;
    c.kt = hi ? AT_KC + r * 8 : kh * AT_HALF + AT_KT + r * 8;
    const int rr = r >= 28 ? r - 12 : r;
    c.vdata = rr < 20;
    c.vlane = c.vdata ? kh * AT_HALF + AT_KB + hi * 320 + rr * 16 : AT_ONES + 128;
    for (int i = k.tid; i < AT_ONES_BYTES / 4; i += 512) ((LAS unsigned*)(k.lds + AT_ONES))[i] = 0x3F803F80u;
    ((LAS unsigned*)(k.lds + AT_KC))[k.tid] = (k.tid & 1) ? 0u : 0x00003F80u;
    if (k.tid == 0) *(LAS unsigned*)(k.lds + AT_FLAG) = 0;
}
DI void attn_first_loads(const Ctx& k, AttnCtx& c, int h, int qb) {
    const int qg = k.w & 3, r = k.lane & 31, hi = k.lane >> 5;
    attn_stage(c, 0, 0);
#pragma unroll
    for (int i = 0; i < 2; ++i) {
        const unsigned qp = WS_Q + (qb * 8 + qg * 2 + i) * QK_TILE + h * 1280 + r * 8;
        const u32x2 a = ld64(k.rs, qp + hi * 256), b = ld64(k.rs, qp + hi * 256 + 512), e = ld64(k.rs, qp + 1024);
        c.qf0[i] = (u32x4){a.x, a.y, b.x, b.y};
        c.qf1[i] = hi ? (u32x2){0, 0} : e;
    }
    attn_stage(c, 1, 1);
}
DI bf16x8 kfrag0(lds_ptr p) { const u32x2 a = *(const LAS u32x2*)p, b = *(const LAS u32x2*)(p + 512); return as_frag((u32x4){a.x, a.y, b.x, b.y}); }
DI s16x4 kfrag1(lds_ptr p) { return __builtin_bit_cast(s16x4, *(const LAS u32x2*)p); }
DI s16x4 as_frag4(u32x2 v) { return __builtin_bit_cast(s16x4, v); }
DI f32x16 mfma32x8(s16x4 a, s16x4 b, f32x16 c) { return __builtin_amdgcn_mfma_f32_32x32x8bf16_1k(a, b, c, 0, 0, 0); }
DI void wo_prefetch(const Ctx& k, int ct) {
    for (int pc = k.w; pc < 100; pc += 8) glds16<true>(k.lane * 16, k.ws + WS_WO + ct * 102400 + pc * 1024, k.lds0 + WO_LDS + pc * 1024);
}
DI void attn_fast(const Ctx& k, AttnCtx& c, f32x16& oA, f32x16& oB, int ct3, int f4) {
    const lds_ptr lds = k.lds;
    const int hi = k.lane >> 5, kh = k.w >> 2;
    {
        const bf16x8 k0 = kfrag0(lds + c.kofs - kh * AT_HALF); const s16x4 k1 = kfrag1(lds + (hi ? c.kt : c.kt - kh * AT_HALF));
#pragma unroll
        for (int i = 0; i < 2; ++i) {
            f32x16 s = mfma32(k0, as_frag(c.qf0[i]), (f32x16){});
            s = mfma32x8(k1, as_frag4(c.qf1[i]), s);
            float m = s[0];
#pragma unroll
            for (int j = 1; j < 16; ++j) m = fmaxf(m, s[j]);
            m = swap32_max(m);
            if (hi) c.qf1[i].x = cvtpk(-m, 0.f) & 0xffffu;
        }
    }
    const bf16x8 qA0 = as_frag(c.qf0[0]), qB0 = as_frag(c.qf0[1]); const s16x4 qA1 = as_frag4(c.qf1[0]), qB1 = as_frag4(c.qf1[1]);
    f32x16 sA, sB, nA, nB;
    oA = (f32x16){}; oB = (f32x16){};
    bf16x8 kf0 = kfrag0(lds + c.kofs); s16x4 kf1 = kfrag1(lds + c.kt);
    sA = mfma32(kf0, qA0, (f32x16){}); sB = mfma32(kf0, qB0, (f32x16){});
    sA = mfma32x8(kf1, qA1, sA); sB = mfma32x8(kf1, qB1, sB);
    kf0 = kfrag0(lds + c.kofs + 1024); kf1 = kfrag1(lds + c.kt + 256);
    u32x4 pA0, pA1, pB0, pB1 = {0, 0, 0, 0};
    bf16x8 vf0, vf1 = {0, 0, 0, 0, 0, 0, 0, 0};
    unsigned warm = 0;
    u32x2 fa = {0, 0}, fb = fa, fc = fa, fd = fa;
#define GAP(MF, X, i, P, j) do { MF; SBAR(); X[i] = EX(X[i]); X[i + 1] = EX(X[i + 1]); X[i + 2] = EX(X[i + 2]); X[i + 3] = EX(X[i + 3]); \
        P[j] = cvtpk(X[i], X[i + 1]); P[j + 1] = cvtpk(X[i + 2], X[i + 3]); PIN(P); SBAR(); } while (0)
#define STEP(SA, SB, NA, NB, VP, KP, KT) do { \
        GAP(NA = mfma32(kf0, qA0, (f32x16){}), SA, 0, pA0, 0); vf0 = as_frag(lds_r128(VP)); SBAR(); \
        GAP(oB = mfma32(vf1, as_frag(pB1), oB), SA, 4, pA0, 2); vf1 = as_frag(lds_r128((VP) + 640)); SBAR(); \
        GAP(NB = mfma32(kf0, qB0, (f32x16){}), SA, 8, pA1, 0); \
        GAP(NA = mfma32x8(kf1, qA1, NA), SA, 12, pA1, 2); \
        GAP(NB = mfma32x8(kf1, qB1, NB), SB, 0, pB0, 0); kf0 = kfrag0(KP); kf1 = kfrag1(KT); SBAR(); \
        GAP(oA = mfma32(vf0, as_frag(pA0), oA), SB, 4, pB0, 2); \
        GAP(oA = mfma32(vf1, as_frag(pA1), oA), SB, 8, pB1, 0); \
        GAP(oB = mfma32(vf0, as_frag(pB0), oB), SB, 12, pB1, 2); \
    } while (0)
    int slot = 0;
    PRIO(1);
    for (int sidx = 0; sidx < 4; ++sidx) {
        const int nslot = slot == 2 ? 0 : slot + 1;
        const int sb = at_slot(slot), sn = at_slot(nslot);
        const lds_ptr vb = lds + (c.vdata ? c.vlane + sb : c.vlane);
        lds_ptr k0 = lds + c.kofs + sb, k0n = lds + c.kofs + sn, k1 = lds + (hi ? c.kt : c.kt + sb), k1n = lds + (hi ? c.kt : c.kt + sn);
        PIN(k0); PIN(k0n); PIN(k1); PIN(k1n);
        STEP(sA, sB, nA, nB, vb, k0 + 2 * 1024, k1 + 2 * 256);
        STEP(nA, nB, sA, sB, vb + 1280, k0 + 3 * 1024, k1 + 3 * 256);
        STEP(sA, sB, nA, nB, vb + 2 * 1280, k0 + 4 * 1024, k1 + 4 * 256);
        if (sidx == 1 && k.w == 0) {
            const unsigned o = WS_FP + k.lane * FLAG_STRIDE;
            fa = ld64(k.rs, o); fb = ld64(k.rs, o + 64 * FLAG_STRIDE); fc = ld64(k.rs, o + 128 * FLAG_STRIDE); fd = ld64(k.rs, o + 192 * FLAG_STRIDE);
        }
        STEP(nA, nB, sA, sB, vb + 3 * 1280, k0 + 5 * 1024, k1 + 5 * 256);
        STEP(sA, sB, nA, nB, vb + 4 * 1280, k0 + 6 * 1024, k1 + 6 * 256);
        STEP(nA, nB, sA, sB, vb + 5 * 1280, k0 + 7 * 1024, k1 + 7 * 256);
        if (sidx == 1 && k.w == 0) {
            const unsigned tl = (unsigned)k.tag, th = (unsigned)(k.tag >> 32);
            if (!__all(fa.x == tl && fa.y == th && fb.x == tl && fb.y == th && fc.x == tl && fc.y == th && fd.x == tl && fd.y == th)) poll_all(k, WS_FP);
        }
        if (sidx == 0) STAMP(20); else if (sidx == 1) STAMP(22); else if (sidx == 2) STAMP(24); else STAMP(26);
        WAIT_BAR(0);
        if (sidx == 0) STAMP(21); else if (sidx == 1) STAMP(23); else if (sidx == 2) STAMP(25); else STAMP(27);
        if (sidx < 2) attn_stage(c, sidx + 2, nslot == 2 ? 0 : nslot + 1);
        if (sidx == 0 && k.tid == 0) st_flag(k.ws + WS_FP + k.c * FLAG_STRIDE, k.tag);
        if (sidx == 1) {
            const unsigned o = WS_W13 + f4 * 20480 + (k.lb >> 4) * 71680 + (k.w * 64 + k.lane) * 128;
            warm = ld32(k.rs, o);
            if (k.w == 0 && k.lane < 48) warm ^= ld32(k.rs, o + 65536);
        }
        if (sidx == 3) wo_prefetch(k, ct3);
        STEP(sA, sB, nA, nB, vb + 6 * 1280, k0n, k1n);
        STEP(nA, nB, sA, sB, vb + 7 * 1280, k0n + 1024, k1n + 256);
        slot = nslot;
    }
    PRIO(0);
    oB = mfma32(vf1, as_frag(pB1), oB);
    asm volatile("" :: "v"(warm));
#undef GAP
#undef STEP
}
DI void attn_safe(const Ctx& k, AttnCtx& c, f32x16 (&o)[2], float (&mrun)[2]) {
    const lds_ptr lds = k.lds;
    attn_stage(c, 0, 0);
    if (k.lane >> 5) { c.qf1[0].x = 0; c.qf1[1].x = 0; }
    o[0] = (f32x16){}; o[1] = (f32x16){};
    mrun[0] = -INFINITY; mrun[1] = -INFINITY;
    for (int sidx = 0; sidx < 4; ++sidx) {
        const int sb = at_slot(sidx & 1);
        WAIT_BAR(0);
        if (sidx < 3) attn_stage(c, sidx + 1, (sidx + 1) & 1);
        const lds_ptr vb = lds + (c.vdata ? c.vlane + sb : c.vlane);
        for (int kt = 0; kt < 8; ++kt) {
            const bf16x8 k0 = kfrag0(lds + c.kofs + sb + kt * 1024); const s16x4 k1 = kfrag1(lds + (c.hi ? c.kt : c.kt + sb) + kt * 256);
            const bf16x8 v0 = as_frag(lds_r128(vb + kt * 1280)), v1 = as_frag(lds_r128(vb + kt * 1280 + 640));
#pragma unroll
            for (int i = 0; i < 2; ++i) {
                f32x16 s = mfma32(k0, as_frag(c.qf0[i]), (f32x16){});
                s = mfma32x8(k1, as_frag4(c.qf1[i]), s);
                float m = s[0];
#pragma unroll
                for (int j = 1; j < 16; ++j) m = fmaxf(m, s[j]);
                m = fmaxf(swap32_max(m), mrun[i]);
                const float alpha = EX(mrun[i] - m);
                mrun[i] = m;
                unsigned pk[8];
#pragma unroll
                for (int j = 0; j < 16; ++j) o[i][j] *= alpha;
#pragma unroll
                for (int j = 0; j < 8; ++j) pk[j] = cvtpk(EX(s[2 * j] - m), EX(s[2 * j + 1] - m));
                o[i] = mfma32(v0, as_frag((u32x4){pk[0], pk[1], pk[2], pk[3]}), o[i]);
                o[i] = mfma32(v1, as_frag((u32x4){pk[4], pk[5], pk[6], pk[7]}), o[i]);
            }
        }
    }
}
DI void p2_phase(const Ctx& k, AttnCtx& c, int h, int qb, int ct3, int f4) {
    const int lane = k.lane, qg = k.w & 3, kh = k.w >> 2, r = lane & 31, hi = lane >> 5;
    const lds_ptr lds = k.lds;
    attn_first_loads(k, c, h, qb);
    PreTile pt;
    pretile_read(k, pt);
    WAIT_BAR(5);
    STAMP(6);
    pretile_store(k, pt);
    f32x16 o[2];
    attn_fast(k, c, o[0], o[1], ct3, f4);
    STAMP(7);
    LAS unsigned* flag = (LAS unsigned*)(lds + AT_FLAG);
    if (__any(hi && !(o[0][8] <= 5.764607523034235e17f && o[1][8] <= 5.764607523034235e17f)) && lane == 0) *flag = 1;
    BAR_L();
    const lds_ptr red = lds + AT_RED + qg * 8192 + lane * 16;
    auto put = [&](const f32x16& v, int slot) {
#pragma unroll
        for (int u = 0; u < 3; ++u) *(LAS f32x4*)(red + slot * 4096 + u * 1024) = (f32x4){v[4 * u], v[4 * u + 1], v[4 * u + 2], v[4 * u + 3]};
    };
    auto get = [&](int slot, int u) { return *(const LAS f32x4*)(red + slot * 4096 + u * 1024); };
    f32x4 of[3];
    if (*flag == 0) {
        if (kh) put(o[0], 0); else put(o[1], 1);
        BAR_L();
#pragma unroll
        for (int u = 0; u < 3; ++u) {
            const f32x4 p = get(kh, u);
#pragma unroll
            for (int j = 0; j < 4; ++j) of[u][j] = p[j] + (kh ? o[1][4 * u + j] : o[0][4 * u + j]);
        }
    } else {
        float mref[2];
        WAIT_BAR(0);
        attn_safe(k, c, o, mref);
        BAR_L();
        if (kh) put(o[0], 0); else put(o[1], 1);
        *(LAS float*)(red + (kh ? 0 : 4096) + 3072) = kh ? mref[0] : mref[1];
        BAR_L();
        const float m0 = kh ? mref[1] : mref[0], m1 = *(const LAS float*)(red + kh * 4096 + 3072), m = fmaxf(m0, m1);
        const float a0 = EX(m0 - m), a1 = EX(m1 - m);
#pragma unroll
        for (int u = 0; u < 3; ++u) {
            const f32x4 p = get(kh, u);
#pragma unroll
            for (int j = 0; j < 4; ++j) of[u][j] = p[j] * a1 + (kh ? o[1][4 * u + j] : o[0][4 * u + j]) * a0;
        }
        BAR_L();
        wo_prefetch(k, ct3);
    }
    {
        auto lr = __builtin_amdgcn_permlane32_swap(__float_as_uint(of[2][0]), __float_as_uint(of[2][0]), false, false);
        const float inv = 1.0f / __uint_as_float(lr[1]);
        const int tok = qb * 256 + qg * 64 + kh * 32 + r;
        const unsigned base = WS_O + (tok >> 4) * OH_TILE + (tok & 15) * 16;
        unsigned pk[3][2];
#pragma unroll
        for (int u = 0; u < 3; ++u) { pk[u][0] = cvtpk(of[u][0] * inv, of[u][1] * inv); pk[u][1] = cvtpk(of[u][2] * inv, of[u][3] * inv); }
        auto oaddr = [&](int col) { return base + (col >> 5) * 1024 + ((col >> 3) & 3) * 256 + (col & 7) * 2; };
        if ((h & 1) == 0) {
            auto s0 = __builtin_amdgcn_permlane32_swap(pk[0][0], pk[1][0], false, false);
            auto s1 = __builtin_amdgcn_permlane32_swap(pk[0][1], pk[1][1], false, false);
            st128(k.rs, oaddr(h * 20 + 8 * hi), (u32x4){s0[0], s1[0], s0[1], s1[1]});
            if (!hi) st64(k.rs, oaddr(h * 20 + 16), (u32x2){pk[2][0], pk[2][1]});
        } else {
            auto s0 = __builtin_amdgcn_permlane32_swap(pk[0][0], pk[2][0], false, false);
            auto s1 = __builtin_amdgcn_permlane32_swap(pk[0][1], pk[2][1], false, false);
            st128(k.rs, oaddr(h * 20 + 4 + 8 * hi), hi ? (u32x4){pk[1][0], pk[1][1], s0[0], s1[0]} : (u32x4){s0[1], s1[1], pk[1][0], pk[1][1]});
            if (!hi) st64(k.rs, oaddr(h * 20), (u32x2){s0[0], s1[0]});
        }
    }
}

DI int own_cf(int kh, int u) { return kh ? (u < 2 ? 3 + u : 4) : u; }
constexpr int RED_LDS = 143360;
struct P3Pre { u32x2 xv[5]; };
DI void p3_pre(const Ctx& k, int rb64, int ct, P3Pre& p) {
    const int tt = k.w & 3, t = k.lane & 15, g = k.lane >> 4;
    if (k.w < 4) {
#pragma unroll
        for (int cf = 0; cf < 5; ++cf) p.xv[cf] = *(const u32x2*)(k.x + (size_t)(rb64 * 64 + tt * 16 + t) * D + ct * 80 + cf * 16 + 4 * g);
    }
}
DI void w13_prefetch(const Ctx& k, int f0, int f);
DI void p3_phase(const Ctx& k, int rb64, int ct, int f0, const P3Pre& pre, u32x2 (&hv)[3]) {
    const int lane = k.lane, tt = k.w & 3, kh = k.w >> 2, t = lane & 15, g = lane >> 4;
    const unsigned ap = WS_O + (rb64 * 4 + tt) * OH_TILE + kh * 10240 + lane * 16;
    const lds_ptr wl = k.lds + WO_LDS + kh * 51200 + lane * 16;
    constexpr int WIN = 5;
    u32x4 a[10];
#pragma unroll
    for (int i = 0; i < WIN; ++i) a[i] = ld128(k.rs, ap + i * 1024);
    bf16x8 wf[2][5];
#pragma unroll
    for (int cf = 0; cf < 5; ++cf) wf[0][cf] = as_frag(lds_r128(wl + cf * 1024));
    f32x4 acc[5];
#pragma unroll
    for (int cf = 0; cf < 5; ++cf) { acc[cf] = (f32x4){0, 0, 0, 0}; PIN(acc[cf]); }
    mfma_fence();
    PRIO(1);
#pragma unroll
    for (int i = 0; i < 10; ++i) {
        if (i + WIN < 10) { a[i + WIN] = ld128(k.rs, ap + (i + WIN) * 1024); SBAR(); }
#pragma unroll
        for (int cf = 0; cf < 5; ++cf) {
            if (i + 1 < 10) wf[(i + 1) & 1][cf] = as_frag(lds_r128(wl + (i + 1) * 5120 + cf * 1024));
            mfma16_acc(acc[cf], wf[i & 1][cf], as_frag(a[i]));
            SBAR();
        }
    }
    PRIO(0);
    mfma_fence();
    STAMP(10);
    BAR_L();
    const lds_ptr red = k.lds + RED_LDS + tt * 5120 + lane * 16;
    if (kh) {
#pragma unroll
        for (int cf = 0; cf < 5; ++cf) *(LAS f32x4*)(red + cf * 1024) = acc[cf];
        BAR_L();
        w13_prefetch(k, f0, k.w - 1);
    } else {
        BAR_L();
        const int tok = rb64 * 64 + tt * 16 + t;
        float s = 0.f, q = 0.f;
        u32x2 hq[5];
#pragma unroll
        for (int cf = 0; cf < 5; ++cf) {
            const f32x4 v = acc[cf] + *(const LAS f32x4*)(red + cf * 1024);
            hq[cf] = (u32x2){cvtpk(v[0] + bflo(pre.xv[cf].x), v[1] + bfhi(pre.xv[cf].x)), cvtpk(v[2] + bflo(pre.xv[cf].y), v[3] + bfhi(pre.xv[cf].y))};
            const int col = ct * 80 + cf * 16 + 4 * g;
            st64(k.rs, WS_H + (tok >> 4) * OH_TILE + (col >> 5) * 1024 + ((col >> 3) & 3) * 256 + t * 16 + (col & 7) * 2, hq[cf]);
            const float h0 = bflo(hq[cf].x), h1 = bfhi(hq[cf].x), h2 = bflo(hq[cf].y), h3 = bfhi(hq[cf].y);
            s += (h0 + h1) + (h2 + h3); q += (h0 * h0 + h1 * h1) + (h2 * h2 + h3 * h3);
        }
        s = sum_x32(sum_x16(s)); q = sum_x32(sum_x16(q));
        if (g == 0) st64(k.rs, WS_PST + (tok >> 4) * 2048 + (ct * 2) * 128 + t * 8, (u32x2){__float_as_uint(s), __float_as_uint(q)});
        if (g == 1) st64(k.rs, WS_PST + (tok >> 4) * 2048 + (ct * 2 + 1) * 128 + t * 8, (u32x2){0u, 0u});
        hv[0] = hq[0]; hv[1] = hq[1]; hv[2] = hq[2];
        const lds_ptr stash = k.lds + RED_LDS + tt * 5120 + lane * 8;
        *(LAS u32x2*)(stash + 3072) = hq[3]; *(LAS u32x2*)(stash + 4096) = hq[4];
    }
}
DI void pair_reduce(lds_ptr red0, int tt, int kh, int lane, const f32x4 (&acc)[5], f32x4 (&own)[3]) {
    const lds_ptr red = red0 + tt * 5120 + lane * 16;
    if (kh == 0) { *(LAS f32x4*)(red + 3072) = acc[3]; *(LAS f32x4*)(red + 4096) = acc[4]; }
    else { *(LAS f32x4*)(red) = acc[0]; *(LAS f32x4*)(red + 1024) = acc[1]; *(LAS f32x4*)(red + 2048) = acc[2]; }
    BAR_L();
    if (kh == 0) {
#pragma unroll
        for (int u = 0; u < 3; ++u) own[u] = acc[u] + *(const LAS f32x4*)(red + u * 1024);
    } else {
        own[0] = acc[3] + *(const LAS f32x4*)(red + 3072); own[1] = acc[4] + *(const LAS f32x4*)(red + 4096); own[2] = own[1];
    }
}
template <int N, int NP>
DI void w2_stage(const Ctx& k, int ct, int b, int s0, int w0) {
    const int nw = 8 - w0;
#pragma unroll
    for (int j = 0; j < NP; ++j) {
        int q = k.w - w0 + nw * j;
        if (q >= 10 * N) q -= nw;
        const int hf = q >= 5 * N, rem = q - hf * 5 * N, n = rem / 5;
        glds16<true>(k.lane * 16, k.ws + WS_W2 + (ct * 54 + hf * 27 + s0) * 5120 + rem * 1024, k.lds0 + n * 20480 + b * 10240 + hf * 5120 + (rem - 5 * n) * 1024);
    }
}
DI void p5_phase(const Ctx& k, int rb64, int ct, u32x2 (&hv)[3]) {
    const int lane = k.lane, tt = k.w & 3, kh = k.w >> 2, t = lane & 15, g = lane >> 4;
    const unsigned ap = WS_HH + (rb64 * 4 + tt) * HH_TILE + kh * 27 * 1024 + lane * 16;
    constexpr int WIN = 6;
    u32x4 a[27];
#pragma unroll
    for (int i = 0; i < WIN; ++i) ald128(a[i], k.rsw, ap, i * 1024);
    if (kh) {
        const lds_ptr stash = k.lds + RED_LDS + tt * 5120 + lane * 8;
        hv[0] = *(const LAS u32x2*)(stash + 3072); hv[1] = *(const LAS u32x2*)(stash + 4096); hv[2] = hv[1];
    }
    lds_ptr wb[3] = {k.lds + lane * 16 + kh * 5120, k.lds + lane * 16 + kh * 5120 + 61440, k.lds + lane * 16 + kh * 5120 + 122880};
    PIN(wb[0]); PIN(wb[1]); PIN(wb[2]);
    auto wread = [&](int n, int cf) {
        const int m = n % 7;
        return as_frag(lds_r128(wb[m / 3] + (m % 3) * 20480 + ((n / 7) & 1) * 10240 + cf * 1024));
    };
    bf16x8 wf[2][5];
#pragma unroll
    for (int cf = 0; cf < 5; ++cf) wf[0][cf] = wread(0, cf);
    f32x4 acc[5];
#pragma unroll
    for (int cf = 0; cf < 5; ++cf) { acc[cf] = (f32x4){0, 0, 0, 0}; PIN(acc[cf]); }
    mfma_fence();
    PRIO(1);
#define P5_STEP(i, NW) do { \
        if ((i) == 7) { BAR_L(); w2_stage<7, 9>(k, ct, 0, 14, 0); } \
        if ((i) == 13) { WAIT_BAR(6); w2_stage<6, 8>(k, ct, 1, 21, 0); } \
        if ((i) == 20) { WAIT_BAR(6); } \
        if ((i) + WIN < 27) ald128(a[(i) + WIN], k.rsw, ap, ((i) + WIN) * 1024); \
        ald_wait<NW>(a[i]); SBAR(); \
        _Pragma("unroll") for (int cf = 0; cf < 5; ++cf) { \
            if ((i) + 1 < 27) wf[((i) + 1) & 1][cf] = wread((i) + 1, cf); \
            mfma16_acc(acc[cf], wf[(i) & 1][cf], as_frag(a[i])); SBAR(); } } while (0)
    P5_STEP(0, 6); P5_STEP(1, 6); P5_STEP(2, 6); P5_STEP(3, 6); P5_STEP(4, 6); P5_STEP(5, 6); P5_STEP(6, 6);
    P5_STEP(7, 15); P5_STEP(8, 15); P5_STEP(9, 15); P5_STEP(10, 15); P5_STEP(11, 15); P5_STEP(12, 15);
    P5_STEP(13, 14); P5_STEP(14, 14); P5_STEP(15, 14); P5_STEP(16, 14); P5_STEP(17, 14); P5_STEP(18, 14); P5_STEP(19, 6);
    P5_STEP(20, 6); P5_STEP(21, 5); P5_STEP(22, 4); P5_STEP(23, 3); P5_STEP(24, 2); P5_STEP(25, 1); P5_STEP(26, 0);
#undef P5_STEP
    PRIO(0);
    mfma_fence();
    STAMP(16);
    f32x4 own[3];
    pair_reduce(k.lds + RED_LDS, tt, kh, lane, acc, own);
    const int tok = rb64 * 64 + tt * 16 + t;
#pragma unroll
    for (int u = 0; u < 3; ++u) {
        if (u < 2 || kh == 0) {
            const int col = ct * 80 + own_cf(kh, u) * 16 + 4 * g;
            *(u32x2*)(k.out + (size_t)tok * D + col) = (u32x2){cvtpk(own[u][0] + bflo(hv[u].x), own[u][1] + bfhi(hv[u].x)), cvtpk(own[u][2] + bflo(hv[u].y), own[u][3] + bfhi(hv[u].y))};
        }
    }
}

constexpr int P4_CS = 143360;
DI void w13_prefetch(const Ctx& k, int f0, int f) {
    const int p0 = f * 20;
#pragma unroll 4
    for (int j = 0; j < 20; ++j) glds16<true>(k.lane * 16, k.ws + WS_W13 + f0 * 20480 + (p0 + j) * 1024, k.lds0 + (p0 + j) * 1024);
}
DI void p4_colsum(const Ctx& k, int f) {
    const int t = k.lane & 15, g = k.lane >> 4;
    u32x4 wv[20];
#pragma unroll
    for (int s = 0; s < 20; ++s) wv[s] = lds_r128(k.lds + (f * 20 + s) * 1024 + k.lane * 16);
    f32x4 c0 = {0, 0, 0, 0}, c1 = {0, 0, 0, 0};
    const bf16x8 ones = {0x3F80, 0x3F80, 0x3F80, 0x3F80, 0x3F80, 0x3F80, 0x3F80, 0x3F80};
#pragma unroll
    for (int s = 0; s < 20; s += 2) { c0 = mfma16(as_frag(wv[s]), ones, c0); c1 = mfma16(as_frag(wv[s + 1]), ones, c1); }
    if (t == 0) *(LAS f32x4*)(k.lds + P4_CS + f * 64 + g * 16) = c0 + c1;
}
DI void p4_phase(const Ctx& k, int rb, int f0, int nf, int ct5) {
    const int lane = k.lane, w = k.w, t = lane & 15, g = lane >> 4;
    const lds_ptr lds = k.lds;
    const unsigned ap = WS_H + (rb * 16 + w * 2) * OH_TILE + lane * 16;
    u32x4 a0[20], a1[20];
#define P4_LD(s) do { a0[s] = ld128(k.rs, ap + (s) * 1024); a1[s] = ld128(k.rs, ap + OH_TILE + (s) * 1024); } while (0)
    u32x2 pv[2][4];
#pragma unroll
    for (int i = 0; i < 2; ++i)
#pragma unroll
        for (int j = 0; j < 4; ++j) pv[i][j] = ld64(k.rs, WS_PST + (rb * 16 + w * 2 + i) * 2048 + (4 * g + j) * 128 + t * 8);
    P4_LD(0); P4_LD(1); P4_LD(2);
    lds_ptr wb[3] = {lds + lane * 16, lds + lane * 16 + 61440, lds + lane * 16 + 122880};
    PIN(wb[1]); PIN(wb[2]);
    bf16x8 wf[2][7];
#pragma unroll
    for (int f = 0; f < 7; ++f) wf[0][f] = as_frag(lds_r128(wb[f / 3] + (f % 3) * 20480));
    f32x4 acc[2][7];
#pragma unroll
    for (int f = 0; f < 7; ++f) { acc[0][f] = (f32x4){0, 0, 0, 0}; acc[1][f] = (f32x4){0, 0, 0, 0}; PIN(acc[0][f]); PIN(acc[1][f]); }
    float mean[2], rstd[2], vpe[2];
    mfma_fence();
    STAMP(13);
    PRIO(1);
#pragma unroll
    for (int s = 0; s < 20; ++s) {
        if (s < 6) { P4_LD(2 * s + 3); P4_LD(2 * s + 4); } else if (s + 9 < 20) P4_LD(s + 9);
        SBAR();
        if (s == 10) { __builtin_amdgcn_s_barrier(); w2_stage<7, 9>(k, ct5, 0, 0, 0); SBAR(); }
        if (s == 4) {
#pragma unroll
            for (int i = 0; i < 2; ++i) {
                float sm = (__uint_as_float(pv[i][0].x) + __uint_as_float(pv[i][1].x)) + (__uint_as_float(pv[i][2].x) + __uint_as_float(pv[i][3].x));
                float q = (__uint_as_float(pv[i][0].y) + __uint_as_float(pv[i][1].y)) + (__uint_as_float(pv[i][2].y) + __uint_as_float(pv[i][3].y));
                sm = sum_x32(sum_x16(sm)); q = sum_x32(sum_x16(q));
                mean[i] = sm * (1.0f / D);
                vpe[i] = fmaxf(q * (1.0f / D) - mean[i] * mean[i], 0.f) + LN_EPS;
                rstd[i] = __builtin_amdgcn_rsqf(vpe[i]);
            }
            PIN(mean[0]); PIN(mean[1]); PIN(rstd[0]); PIN(rstd[1]); PIN(vpe[0]); PIN(vpe[1]);
            SBAR();
        }
#pragma unroll
        for (int f = 0; f < 7; ++f) {
            if (s + 1 < 20) wf[(s + 1) & 1][f] = as_frag(lds_r128(wb[f / 3] + (f % 3) * 20480 + (s + 1) * 1024));
            mfma16_acc(acc[0][f], wf[s & 1][f], as_frag(a0[s]));
            mfma16_acc(acc[1][f], wf[s & 1][f], as_frag(a1[s]));
            SBAR();
        }
    }
#undef P4_LD
    PRIO(0);
    mfma_fence();
    STAMP(14);
    f32x4 cs[7];
#pragma unroll
    for (int f = 0; f < 7; ++f) cs[f] = *(const LAS f32x4*)(lds + P4_CS + f * 64 + g * 16);
#pragma unroll
    for (int i = 0; i < 2; ++i) {
        const float ce = -LOG2E * rstd[i], ir2 = vpe[i], nm = -mean[i];
        const f32x2 ce2 = {ce, ce}, ir22 = {ir2, ir2}, nm2 = {nm, nm};
        unsigned d[8];
#pragma unroll
        for (int f = 0; f < 7; ++f) {
            const f32x2 ga = __builtin_elementwise_fma(nm2, __builtin_shufflevector(cs[f], cs[f], 0, 1), __builtin_shufflevector(acc[i][f], acc[i][f], 0, 1));
            const f32x2 ua = __builtin_elementwise_fma(nm2, __builtin_shufflevector(cs[f], cs[f], 2, 3), __builtin_shufflevector(acc[i][f], acc[i][f], 2, 3));
            const f32x2 t2 = ga * ce2;
            const f32x2 e2 = {EX(t2.x), EX(t2.y)};
            const f32x2 d2 = __builtin_elementwise_fma(e2, ir22, ir22);
            const f32x2 r2 = {__builtin_amdgcn_rcpf(d2.x), __builtin_amdgcn_rcpf(d2.y)};
            const f32x2 o2 = (ga * ua) * r2;
            d[f] = cvtpk(o2.x, o2.y);
        }
        d[7] = 0;
        const unsigned dst = WS_HH + (rb * 16 + w * 2 + i) * HH_TILE + f0 * 256 + lane * 16;
#pragma unroll
        for (int u = 0; u < 2; ++u) {
            auto a = __builtin_amdgcn_permlane32_swap(d[4 * u], d[4 * u + 2], false, false);
            auto b = __builtin_amdgcn_permlane32_swap(d[4 * u + 1], d[4 * u + 3], false, false);
            auto e = __builtin_amdgcn_permlane16_swap(a[0], b[0], false, false);
            auto f = __builtin_amdgcn_permlane16_swap(a[1], b[1], false, false);
            if (4 * u + g < nf) st128(k.rs, dst + u * 1024, (u32x4){e[0], e[1], f[0], f[1]});
        }
    }
}

constexpr int LDS_BYTES = 163840;
constexpr int CODE_LINES = 530;
__global__ __launch_bounds__(512, 2) void fused_block(const uint16_t* __restrict__ x, const uint16_t* __restrict__ wqkv, const uint16_t* __restrict__ wo,
                                                      const uint16_t* __restrict__ w1, const uint16_t* __restrict__ w3, const uint16_t* __restrict__ w2,
                                                      char* __restrict__ ws, uint16_t* __restrict__ out) {
    extern __shared__ __attribute__((aligned(16))) char smem[];
    Ctx k;
    k.x = x; k.wqkv = wqkv; k.wo = wo; k.w1 = w1; k.w3 = w3; k.w2 = w2; k.ws = ws; k.out = out;
    k.rs = __builtin_amdgcn_make_buffer_rsrc(ws, 0, WS_END + 20480, 0x00020000);
    k.lds = (lds_ptr)smem; k.lds0 = (unsigned)(uintptr_t)smem;
    k.c = blockIdx.x; k.xcd = k.c & 7; k.lb = k.c >> 3;
    k.tid = threadIdx.x; k.lane = k.tid & 63; k.w = rfl(k.tid >> 6);
    k.rsw = (u32x4){(unsigned)(uintptr_t)ws, (unsigned)((uintptr_t)ws >> 32) & 0xffffu, WS_END + 20480, 0x00020000u};
    k.tag = 0;
    k.sc = 0; k.sr = 0;
    STAMP(0);
    if (k.w == 7) {
        unsigned long long pc;
        asm volatile("s_getpc_b64 %0" : "=s"(pc));
        const int line = (k.lb & 15) * 64 + k.lane;
        glds4((const char*)(pc & ~127ull) + (line < CODE_LINES ? line : 0) * 128, k.lds0 + SU_LDS + 2048);
    }
    startup_load(k);
    const int rb1 = (k.xcd >> 2) * 8 + (k.lb & 7), cb1 = (k.xcd & 3) * 4 + (k.lb >> 3);
    if (cb1 < 15) {
        if (cb1 >= 10) p1_body<true>(k, rb1, cb1); else p1_body<false>(k, rb1, cb1);
        STAMP(3);
        WAIT_BAR(0);
        if (k.tid == 0) st_flag(ws + WS_F1 + (rb1 * 16 + cb1) * FLAG_STRIDE, k.tag);
    } else {
        WAIT_VM(0);
        startup_tag(k);
        ln1_stats(k, rb1);
        if (rb1 < 15) colsum_qkv(k, rb1);
        WAIT_BAR(0);
    }
    STAMP(4);
    if (k.w) pretile_load(k);
    const int h = k.xcd * 4 + (k.lb & 3), qb = k.lb >> 2;
    const int rb64 = k.xcd * 4 + (k.lb >> 3), ct = k.lb & 7;
    const int rb4 = (k.xcd >> 1) * 2 + (k.lb >> 4), cb4 = (k.xcd & 1) * 16 + (k.lb & 15);
    const int f0 = cb4 < 24 ? cb4 * 7 : 168 + (cb4 - 24) * 6, nf = cb4 < 24 ? 7 : 6;
    AttnCtx ac;
    attn_setup(k, ac, h);
    if (k.w == 0) {
        const int c0 = (20 * h) >> 7, c1 = (20 * h + 19) >> 7, j = k.lane & 15, sel = k.lane >> 4;
        const int cbk = (sel & 1) ? c1 : c0;
        const unsigned o0 = WS_F1 + (j * 16 + (sel < 2 ? 5 : 10) + cbk) * FLAG_STRIDE;
        const unsigned o1 = WS_F1 + ((2 * qb + (k.lane & 1)) * 16 + ((k.lane & 2) ? c1 : c0)) * FLAG_STRIDE;
        poll_flags2(k, o0, o1);
    }
    WAIT_BAR(0);
    STAMP(5);
    p2_phase(k, ac, h, qb, ct, f0);
    STAMP(8);
    WAIT_BAR(0);
    if (k.tid == 0) st_flag(ws + WS_F2 + (qb * 32 + h) * FLAG_STRIDE, k.tag);
    P3Pre pre;
    p3_pre(k, rb64, ct, pre);
    if (k.w == 0) poll_flags(k, WS_F2 + (k.xcd * 32 + (k.lane & 31)) * FLAG_STRIDE);
    else if (k.w <= 3) w13_prefetch(k, f0, k.w - 1);
    WAIT_BAR(0);
    if (k.c == 0 && k.tid == 0) st_flag(ws + WS_EPOCH, k.tag);
    STAMP(9);
    u32x2 hv[3];
    p3_phase(k, rb64, ct, f0, pre, hv);
    STAMP(11);
    if (k.w < 4) WAIT_VM(0);
    BAR_L();
    if (k.tid == 0) st_flag(ws + WS_F3 + (k.xcd * 32 + k.lb) * FLAG_STRIDE, k.tag);
    if (k.w == 0) poll_flags(k, WS_F3 + (rb4 * 32 + (k.lane & 31)) * FLAG_STRIDE);
    else {
        WAIT_VM(0);
        p4_colsum(k, k.w - 1);
    }
    WAIT_BAR(0);
    STAMP(12);
    p4_phase(k, rb4, f0, nf, ct);
    STAMP(15);
    WAIT_BAR(0);
    if (k.tid == 0) st_flag(ws + WS_F4 + (rb4 * 32 + cb4) * FLAG_STRIDE, k.tag);
    if (k.w == 0) poll_flags(k, WS_F4 + ((rb64 >> 2) * 32 + (k.lane & 31)) * FLAG_STRIDE);
    else {
        w2_stage<7, 10>(k, ct, 1, 7, 1);
        const int li = (k.w - 1) * 64 + k.lane;
        if (k.w <= 5) {
            const int ln = (k.lb >> 3) * 260 + (li < 260 ? li : li - 64), hf = ln >= 520;
            const unsigned warm = ld32(k.rs, WS_W2 + (ct * 54 + hf * 27 + 14) * 5120 + (ln - hf * 520) * 128);
            asm volatile("" :: "v"(warm));
        }
    }
    WAIT_BAR(0);
    STAMP(17);
    p5_phase(k, rb64, ct, hv);
    STAMP(18);
    STAMP_FLUSH();
}

extern "C" void kernel_launch(void* const* d_in, const int* in_sizes, int n_in, void* d_out, int out_size, void* d_ws, size_t ws_size, hipStream_t stream) {
    (void)hipFuncSetAttribute((const void*)fused_block, hipFuncAttributeMaxDynamicSharedMemorySize, LDS_BYTES);
    fused_block<<<256, 512, LDS_BYTES, stream>>>((const uint16_t*)d_in[0], (const uint16_t*)d_in[1], (const uint16_t*)d_in[2], (const uint16_t*)d_in[4],
                                                 (const uint16_t*)d_in[5], (const uint16_t*)d_in[6], (char*)d_ws, (uint16_t*)d_out);
}
```

```cpp
#include <hip/hip_runtime.h>
#include <hip/hip_bf16.h>
#include <stdint.h>

#define DI __device__ __forceinline__
#define LAS __attribute__((address_space(3)))
#define GAS __attribute__((address_space(1)))
typedef short bf16x8 __attribute__((ext_vector_type(8)));
typedef short s16x4 __attribute__((ext_vector_type(4)));
typedef float f32x2 __attribute__((ext_vector_type(2)));
typedef float f32x4 __attribute__((ext_vector_type(4)));
typedef float f32x16 __attribute__((ext_vector_type(16)));
typedef unsigned u32x2 __attribute__((ext_vector_type(2)));
typedef unsigned u32x4 __attribute__((ext_vector_type(4)));
typedef unsigned long long u64;
typedef __bf16 bf16x2_t __attribute__((ext_vector_type(2)));
typedef short v4i16_t __attribute__((ext_vector_type(4)));
typedef LAS char* lds_ptr;
typedef __amdgpu_buffer_rsrc_t rsrc_t;

constexpr int S = 2048, D = 640, H = 32, DH = 20, F = 1728, NQKV = 3 * D;
constexpr float LN_EPS = 1e-5f;
constexpr float LOG2E = 1.4426950408889634f;
constexpr float QSCALE = 0.22360679774997896f * LOG2E;

constexpr unsigned WS_EPOCH = 0;
constexpr unsigned FLAG_STRIDE = 64, FLAG_ARR = 256 * FLAG_STRIDE;
constexpr unsigned WS_F1 = 4096, WS_FP = WS_F1 + FLAG_ARR, WS_F2 = WS_FP + FLAG_ARR, WS_F3 = WS_F2 + FLAG_ARR, WS_F4 = WS_F3 + FLAG_ARR;
constexpr unsigned WS_ST1 = 131072;
constexpr unsigned WS_CSQ = WS_ST1 + 32768;
constexpr unsigned WS_PST = 262144;
constexpr unsigned WS_Q = 524288;
constexpr unsigned IMG = S * D * 2;
constexpr unsigned QK_TILE = 32 * 1280, QK_IMG = 64 * QK_TILE;
constexpr unsigned WS_K = WS_Q + QK_IMG;
constexpr unsigned WS_V = WS_K + QK_IMG;
constexpr unsigned OH_TILE = 21504, HH_TILE = 56320;
constexpr unsigned WS_O = WS_V + IMG;
constexpr unsigned WS_H = WS_O + 128 * OH_TILE;
constexpr unsigned WS_HH = WS_H + 128 * OH_TILE;
constexpr unsigned WS_WO = WS_HH + 128 * HH_TILE;
constexpr unsigned WS_W13 = WS_WO + D * D * 2;
constexpr unsigned WS_W2 = WS_W13 + D * F * 4;
constexpr unsigned WS_END = WS_W2 + F * D * 2;

DI unsigned cvtpk(float lo, float hi) { f32x2 v = {lo, hi}; bf16x2_t b = __builtin_convertvector(v, bf16x2_t); return __builtin_bit_cast(unsigned, b); }
DI float bflo(unsigned u) { return __uint_as_float(u << 16); }
DI float bfhi(unsigned u) { return __uint_as_float(u & 0xffff0000u); }
DI f32x4 mfma16(bf16x8 a, bf16x8 b, f32x4 c) { return __builtin_amdgcn_mfma_f32_16x16x32_bf16(a, b, c, 0, 0, 0); }
DI void mfma16_acc(f32x4& c, bf16x8 a, bf16x8 b) { asm volatile("v_mfma_f32_16x16x32_bf16 %0, %1, %2, %0" : "+v"(c) : "v"(a), "v"(b)); }
DI void mfma_fence() { __builtin_amdgcn_sched_barrier(0); asm volatile("s_nop 7\n\ts_nop 1" ::: "memory"); __builtin_amdgcn_sched_barrier(0); }
DI f32x16 mfma32(bf16x8 a, bf16x8 b, f32x16 c) { return __builtin_amdgcn_mfma_f32_32x32x16_bf16(a, b, c, 0, 0, 0); }
DI int rfl(int x) { return __builtin_amdgcn_readfirstlane(x); }
template <bool SC1>
DI void glds16(unsigned voff, const void* sbase, unsigned ldsdst) {
    unsigned keep;
    if (SC1) asm volatile("s_mov_b32 %0, m0\n\ts_mov_b32 m0, %3\n\ts_nop 0\n\tglobal_load_lds_dwordx4 %1, %2 sc1\n\ts_mov_b32 m0, %0" : "=&s"(keep) : "v"(voff), "s"(sbase), "s"(ldsdst) : "memory");
    else asm volatile("s_mov_b32 %0, m0\n\ts_mov_b32 m0, %3\n\ts_nop 0\n\tglobal_load_lds_dwordx4 %1, %2\n\ts_mov_b32 m0, %0" : "=&s"(keep) : "v"(voff), "s"(sbase), "s"(ldsdst) : "memory");
}
DI void glds4_sc1(const void* gptr, unsigned ldsdst) {
    unsigned keep;
    asm volatile("s_mov_b32 %0, m0\n\ts_mov_b32 m0, %2\n\ts_nop 0\n\tglobal_load_lds_dword %1, off sc1\n\ts_mov_b32 m0, %0" : "=&s"(keep) : "v"(gptr), "s"(ldsdst) : "memory");
}
DI void glds4(const void* gptr, unsigned ldsdst) {
    unsigned keep;
    asm volatile("s_mov_b32 %0, m0\n\ts_mov_b32 m0, %2\n\ts_nop 0\n\tglobal_load_lds_dword %1, off\n\ts_mov_b32 m0, %0" : "=&s"(keep) : "v"(gptr), "s"(ldsdst) : "memory");
}
#define WAIT_VM(n) asm volatile("s_waitcnt vmcnt(" #n ")" ::: "memory")
#define WAIT_BAR(n) asm volatile("s_waitcnt vmcnt(" #n ") lgkmcnt(0)\n\ts_barrier" ::: "memory")
#define BAR_L() asm volatile("s_waitcnt lgkmcnt(0)\n\ts_barrier" ::: "memory")
#define SBAR() __builtin_amdgcn_sched_barrier(0)
#define PIN(x) asm volatile("" : "+v"(x))
#define PRIO(n) __builtin_amdgcn_s_setprio(n)
#define EX(v) __builtin_amdgcn_exp2f(v)
DI u32x4 lds_r128(lds_ptr p) { return *(const LAS u32x4*)p; }
DI s16x4 lds_tr(lds_ptr p) { return __builtin_bit_cast(s16x4, __builtin_amdgcn_ds_read_tr16_b64_v4i16((LAS v4i16_t*)p)); }
DI bf16x8 as_frag(u32x4 v) { return __builtin_bit_cast(bf16x8, v); }
DI bf16x8 cat_frag(s16x4 a, s16x4 b) { return (bf16x8){a[0], a[1], a[2], a[3], b[0], b[1], b[2], b[3]}; }
DI float swap32_max(float m) { auto r = __builtin_amdgcn_permlane32_swap(__float_as_uint(m), __float_as_uint(m), false, false); return fmaxf(__uint_as_float(r[0]), __uint_as_float(r[1])); }
DI float sum_x16(float v) { auto r = __builtin_amdgcn_permlane16_swap(__float_as_uint(v), __float_as_uint(v), false, false); return __uint_as_float(r[0]) + __uint_as_float(r[1]); }
DI float sum_x32(float v) { auto r = __builtin_amdgcn_permlane32_swap(__float_as_uint(v), __float_as_uint(v), false, false); return __uint_as_float(r[0]) + __uint_as_float(r[1]); }
DI u32x4 ld128(rsrc_t r, unsigned off) { return __builtin_amdgcn_raw_buffer_load_b128(r, off, 0, 16); }
DI u32x2 ld64(rsrc_t r, unsigned off) { return __builtin_amdgcn_raw_buffer_load_b64(r, off, 0, 16); }
DI unsigned ld32(rsrc_t r, unsigned off) { return __builtin_amdgcn_raw_buffer_load_b32(r, off, 0, 16); }
DI void st128(rsrc_t r, unsigned off, u32x4 v) { __builtin_amdgcn_raw_buffer_store_b128(v, r, off, 0, 16); }
DI void st64(rsrc_t r, unsigned off, u32x2 v) { __builtin_amdgcn_raw_buffer_store_b64(v, r, off, 0, 16); }

DI void ald128(u32x4& v, u32x4 rsw, unsigned voff, unsigned soff) { asm volatile("buffer_load_dwordx4 %0, %1, %2, %3 offen sc1" : "=&v"(v) : "v"(voff), "s"(rsw), "s"(soff) : "memory"); }
template <int N> DI void ald_wait(u32x4& v) { asm volatile("s_waitcnt vmcnt(%1)" : "+v"(v) : "i"(N) : "memory"); }
struct Ctx {
    const uint16_t *x, *wqkv, *wo, *w1, *w3, *w2;
    char* ws; uint16_t* out;
    rsrc_t rs; u32x4 rsw; u64 tag; lds_ptr lds; unsigned lds0;
    int c, xcd, lb, tid, lane, w;
    mutable unsigned sc, sr;
};

#define STAMP(i)
#define STAMP_FLUSH()

DI u64 ld_flag(const char* p) { return __hip_atomic_load((const GAS u64*)p, __ATOMIC_RELAXED, __HIP_MEMORY_SCOPE_AGENT); }
DI void st_flag(char* p, u64 v) { __hip_atomic_store((GAS u64*)p, v, __ATOMIC_RELAXED, __HIP_MEMORY_SCOPE_AGENT); }
constexpr unsigned SPIN_CAP = 1u << 18;
DI void poll_flags(const Ctx& k, unsigned off) {
    for (unsigned spins = 0; spins < SPIN_CAP; ++spins) {
        if (__all(ld_flag(k.ws + off) == k.tag)) break;
        __builtin_amdgcn_s_sleep(1);
    }
}
DI void poll_flags2(const Ctx& k, unsigned off0, unsigned off1) {
    for (unsigned spins = 0; spins < SPIN_CAP; ++spins) {
        const u64 a = ld_flag(k.ws + off0), b = ld_flag(k.ws + off1);
        if (__all(a == k.tag && b == k.tag)) break;
        __builtin_amdgcn_s_sleep(1);
    }
}
DI void poll_all(const Ctx& k, unsigned arr) {
    for (unsigned spins = 0; spins < SPIN_CAP; ++spins) {
        const unsigned o = arr + k.lane * FLAG_STRIDE;
        const u64 a = ld_flag(k.ws + o), b = ld_flag(k.ws + o + 64 * FLAG_STRIDE), c = ld_flag(k.ws + o + 128 * FLAG_STRIDE), d = ld_flag(k.ws + o + 192 * FLAG_STRIDE);
        if (__all(a == k.tag && b == k.tag && c == k.tag && d == k.tag)) break;
        __builtin_amdgcn_s_sleep(1);
    }
}

constexpr int SU_LDS = 131072;
DI void startup_load(const Ctx& k) { glds4(k.ws + WS_EPOCH + (k.lane & 1) * 4, k.lds0 + SU_LDS + k.w * 256); }
DI void startup_tag(Ctx& k) {
    const u32x2 e = *(const LAS u32x2*)(k.lds + SU_LDS + k.w * 256);
    k.tag = (((u64)(unsigned)rfl(e.y) << 32) | (unsigned)rfl(e.x)) + 1;
}
DI void ln1_stats(const Ctx& k, int j) {
    u32x4 v[2][10];
#pragma unroll
    for (int p = 0; p < 2; ++p)
#pragma unroll
        for (int i = 0; i < 10; ++i) v[p][i] = *(const u32x4*)(k.x + (size_t)(128 * j + 16 * k.w + 8 * p + (k.lane >> 3)) * D + ((k.lane & 7) + 8 * i) * 8);
#pragma unroll
    for (int p = 0; p < 2; ++p) {
        float s = 0.f, q = 0.f;
#pragma unroll
        for (int i = 0; i < 10; ++i)
#pragma unroll
            for (int e = 0; e < 4; ++e) { const float a = bflo(v[p][i][e]), b = bfhi(v[p][i][e]); s += a + b; q = fmaf(a, a, fmaf(b, b, q)); }
#pragma unroll
        for (int o = 4; o >= 1; o >>= 1) { s += __shfl_xor(s, o); q += __shfl_xor(q, o); }
        const float mean = s * (1.0f / D), var = fmaxf(q * (1.0f / D) - mean * mean, 0.f);
        const float val = (k.lane & 1) ? 1.0f / sqrtf(var + LN_EPS) : mean;
        const int row = 128 * j + 16 * k.w + 8 * p + (k.lane >> 3);
        if ((k.lane & 7) < 2) st64(k.rs, WS_ST1 + row * 16 + (k.lane & 1) * 8, (u32x2){__float_as_uint(val), (unsigned)k.tag});
    }
}
DI void colsum_qkv(const Ctx& k, int cb) {
    float a[8] = {0, 0, 0, 0, 0, 0, 0, 0};
    u32x4 v[20];
#pragma unroll
    for (int i = 0; i < 20; ++i) v[i] = *(const u32x4*)(k.wqkv + (size_t)(k.w * 80 + i * 4 + (k.lane >> 4)) * NQKV + cb * 128 + (k.lane & 15) * 8);
#pragma unroll
    for (int i = 0; i < 20; ++i)
#pragma unroll
        for (int j = 0; j < 4; ++j) { a[2 * j] += bflo(v[i][j]); a[2 * j + 1] += bfhi(v[i][j]); }
    LAS float* red = (LAS float*)k.lds;
#pragma unroll
    for (int j = 0; j < 8; ++j) { a[j] += __shfl_xor(a[j], 16); a[j] += __shfl_xor(a[j], 32); }
    if (k.lane < 16) {
#pragma unroll
        for (int j = 0; j < 8; ++j) red[k.w * 128 + k.lane * 8 + j] = a[j];
    }
    BAR_L();
    if (k.tid < 128) {
        float t = 0.f;
#pragma unroll
        for (int i = 0; i < 8; ++i) t += red[i * 128 + k.tid];
        st64(k.rs, WS_CSQ + (cb * 128 + k.tid) * 8, (u32x2){__float_as_uint(t), (unsigned)k.tag});
    }
}

constexpr int PT_LDS = 131072;
DI int pt_off(int i) { return PT_LDS + (i < 3 ? i * 8192 : 24576 + (i - 3) * 4096); }
DI void pretile_load(const Ctx& k) {
    const int wv = k.w - 1;
#pragma unroll
    for (int i = 0; i < 5; ++i) {
        const int u = k.c + 256 * i;
        if (u < 540) {
            const int s = u % 20, fb = u / 20;
            const uint16_t* src = ((k.lane & 1) ? k.w3 : k.w1) + fb * 64 + (k.lane >> 1) * 2 + (size_t)32 * s * F;
            for (int r = wv; r < 32; r += 7) glds4(src + (size_t)r * F, k.lds0 + pt_off(i) + r * 256);
        } else {
            const int pc = (i & 1) ? wv - 3 : wv;
            if (pc >= 0 && pc < 4) {
                const bool is2 = u < 1080;
                const int uu = is2 ? u - 540 : u - 1080, s = uu / 10, nb = uu % 10, r = 8 * pc + (k.lane >> 3);
                glds16<false>((32 * s + r) * (D * 2) + nb * 128 + (k.lane & 7) * 16, is2 ? k.w2 : k.wo, k.lds0 + pt_off(i) + pc * 1024);
            }
        }
    }
}
struct PreTile { u32x4 v[5]; };
DI void pretile_read(const Ctx& k, PreTile& p) {
    const int g = k.lane >> 4, q = (k.lane >> 2) & 3, pp = k.lane & 3;
#pragma unroll
    for (int i = 0; i < 5; ++i) {
        const int u = k.c + 256 * i;
        const bool wide = u < 540;
        const int rs = wide ? 256 : 128, f = wide ? k.w : (k.w & 3);
        const lds_ptr a = k.lds + pt_off(i) + (8 * g + q) * rs + f * 32 + pp * 8;
        p.v[i] = __builtin_bit_cast(u32x4, cat_frag(lds_tr(a), lds_tr(a + 4 * rs)));
    }
}
DI void pretile_store(const Ctx& k, const PreTile& p) {
#pragma unroll
    for (int i = 0; i < 5; ++i) {
        const int u = k.c + 256 * i;
        if (u < 540) st128(k.rs, WS_W13 + (((u / 20) * 8 + k.w) * 20 + u % 20) * 1024 + k.lane * 16, p.v[i]);
        else if ((k.w >> 2) == (i & 1)) {
            const bool is2 = u < 1080;
            const int uu = is2 ? u - 540 : u - 1080, s = uu / 10, cfg = (uu % 10) * 4 + (k.w & 3), ct = cfg / 5, cf = cfg % 5;
            st128(k.rs, (is2 ? WS_W2 + ((ct * 54 + s) * 5 + cf) * 1024 : WS_WO + ((ct * 20 + s) * 5 + cf) * 1024) + k.lane * 16, p.v[i]);
        }
    }
}

constexpr int P1_SLOT = 32768;
struct P1Frag { bf16x8 a[4], b[2]; };
template <bool ISV> DI void p1_mma(f32x4 (&acc)[4][2], const P1Frag& f, int m, int n) { if (ISV) mfma16_acc(acc[m][n], f.a[m], f.b[n]); else mfma16_acc(acc[m][n], f.b[n], f.a[m]); }
template <bool ISV>
DI void p1_body(Ctx& k, int rb, int cb) {
    const int lane = k.lane, w = k.w, th = w >> 2, cg = w & 3, t = lane & 15, g = lane >> 4;
    unsigned voffA[2], voffW[2];
#pragma unroll
    for (int i = 0; i < 2; ++i) {
        const int ra = 8 * (2 * w + i) + (lane >> 3), ca = (lane & 7) ^ ((ra >> 1) & 7);
        voffA[i] = ra * (D * 2) + ca * 16;
        const int rw = 4 * (2 * w + i) + (lane >> 4), fw = (rw & 3) | (((rw >> 3) & 1) << 2), cw = (lane & 15) ^ (fw << 1);
        voffW[i] = rw * (NQKV * 2) + cw * 16;
    }
    const char* gA = (const char*)k.x + (size_t)rb * 128 * D * 2;
    const char* gW = (const char*)k.wqkv + cb * 256;
    auto stage = [&](int kt) {
        const unsigned sl = k.lds0 + (kt & 3) * P1_SLOT + w * 2048;
#pragma unroll
        for (int i = 0; i < 2; ++i) glds16<false>(voffA[i], gA + kt * 128, sl + i * 1024);
#pragma unroll
        for (int i = 0; i < 2; ++i) glds16<false>(voffW[i], gW + (size_t)kt * 64 * NQKV * 2, sl + 16384 + i * 1024);
    };
    stage(0); stage(1); stage(2); stage(3);
    int aoff[2], woff[2];
    aoff[0] = (th * 64 + t) * 128 + ((g ^ (t >> 1)) << 4); aoff[1] = aoff[0] ^ 64;
    const int q4 = (lane >> 2) & 3, p = lane & 3, fw = q4 | ((g & 1) << 2);
#pragma unroll
    for (int n = 0; n < 2; ++n) woff[n] = 16384 + (8 * g + q4) * 256 + (((2 * cg + n) ^ fw) << 5) + p * 8;
    f32x4 acc[4][2];
#pragma unroll
    for (int m = 0; m < 4; ++m) { acc[m][0] = (f32x4){0, 0, 0, 0}; acc[m][1] = (f32x4){0, 0, 0, 0}; PIN(acc[m][0]); PIN(acc[m][1]); }
    mfma_fence();
    P1Frag f0, f1;
#define P1_RA(F_, sl, ks, m) F_.a[m] = as_frag(lds_r128(sl + aoff[ks] + (m) * 2048))
#define P1_RB(F_, sl, ks, n) F_.b[n] = cat_frag(lds_tr(sl + woff[n] + (ks) * 8192), lds_tr(sl + woff[n] + (ks) * 8192 + 1024))
#define P1_STEP(RD, FN, sl, ks, FC) do { \
        if (RD) P1_RB(FN, sl, ks, 0); p1_mma<ISV>(acc, FC, 0, 0); SBAR(); \
        if (RD) P1_RA(FN, sl, ks, 0); p1_mma<ISV>(acc, FC, 0, 1); SBAR(); \
        if (RD) P1_RA(FN, sl, ks, 1); p1_mma<ISV>(acc, FC, 1, 0); SBAR(); \
        if (RD) P1_RB(FN, sl, ks, 1); p1_mma<ISV>(acc, FC, 1, 1); SBAR(); \
        if (RD) P1_RA(FN, sl, ks, 2); p1_mma<ISV>(acc, FC, 2, 0); SBAR(); \
        if (RD) P1_RA(FN, sl, ks, 3); p1_mma<ISV>(acc, FC, 2, 1); SBAR(); \
        p1_mma<ISV>(acc, FC, 3, 0); p1_mma<ISV>(acc, FC, 3, 1); SBAR(); } while (0)
    WAIT_BAR(12);
    STAMP(1);
    startup_tag(k);
    { const lds_ptr sl = k.lds; P1_RB(f0, sl, 0, 0); P1_RB(f0, sl, 0, 1); P1_RA(f0, sl, 0, 0); P1_RA(f0, sl, 0, 1); P1_RA(f0, sl, 0, 2); P1_RA(f0, sl, 0, 3); SBAR(); }
    u32x2 gr = {0, 0};
    PRIO(1);
    const unsigned groff = k.tid < 256 ? WS_ST1 + (rb * 128) * 16 + k.tid * 8 : WS_CSQ + (cb * 128 + (k.tid & 127)) * 8;
#pragma unroll
    for (int kt = 0; kt < 10; ++kt) {
        const lds_ptr sl = k.lds + (kt & 3) * P1_SLOT, sn = k.lds + ((kt + 1) & 3) * P1_SLOT;
        P1_STEP(true, f1, sl, 1, f0);
        if (kt < 9) {
            if (kt < 7) WAIT_BAR(8); else if (kt == 7) WAIT_BAR(4); else WAIT_BAR(0);
            if (kt == 6) gr = ld64(k.rs, groff);
            if (kt + 4 < 10) stage(kt + 4);
            SBAR();
        }
        P1_STEP(kt < 9, f0, sn, 0, f1);
    }
#undef P1_RA
#undef P1_RB
#undef P1_STEP
    PRIO(0);
    mfma_fence();
    STAMP(2);
    for (unsigned spins = 0; spins < SPIN_CAP; ++spins) {
        if (__all(gr.y == (unsigned)k.tag)) break;
        gr = ld64(k.rs, groff);
    }
    LAS float* sst = (LAS float*)k.lds;
    if (k.tid < 384) sst[k.tid] = __uint_as_float(gr.x);
    BAR_L();
    const LAS f32x2* st = (const LAS f32x2*)sst + th * 64;
    const LAS float* cs = sst + 256 + cg * 32;
    if (!ISV) {
        const float osc = cb < 5 ? QSCALE : 1.0f;
        const unsigned img = cb < 5 ? WS_Q : WS_K;
        const int cbs = cb % 5;
        unsigned dst[2]; f32x4 c4[2];
#pragma unroll
        for (int n = 0; n < 2; ++n) {
            const int cgi = cbs * 32 + cg * 8 + n * 4 + g, hd = cgi / 5, gi = cgi - 5 * hd;
            const int blk = gi == 4 ? 4 : ((gi & 1) << 1) | (gi >> 1);
            dst[n] = img + (rb * 4 + th * 2) * QK_TILE + hd * 1280 + blk * 256 + t * 8;
            c4[n] = *(const LAS f32x4*)(cs + n * 16 + 4 * g);
        }
#pragma unroll
        for (int m = 0; m < 4; ++m) {
            const f32x2 sm = st[m * 16 + t];
            const float r = sm.y * osc;
#pragma unroll
            for (int n = 0; n < 2; ++n) {
                const f32x4 y = (acc[m][n] - c4[n] * sm.x) * r;
                const unsigned o = dst[n] + (m >> 1) * QK_TILE + (m & 1) * 128;
                st64(k.rs, o, (u32x2){cvtpk(y[0], y[1]), cvtpk(y[2], y[3])});
            }
        }
    } else {
        const int pos = ((g & 1) << 1) | (g >> 1);
        const unsigned dst = WS_V + (rb * 8 + th * 4) * 20480 + ((cb - 10) * 128 + cg * 32 + t) * 32 + pos * 8;
        const float c1[2] = {cs[t], cs[16 + t]};
#pragma unroll
        for (int m = 0; m < 4; ++m) {
            f32x2 sm[4];
#pragma unroll
            for (int i = 0; i < 4; ++i) sm[i] = st[m * 16 + 4 * g + i];
#pragma unroll
            for (int n = 0; n < 2; ++n) {
                float y[4];
#pragma unroll
                for (int i = 0; i < 4; ++i) y[i] = (acc[m][n][i] - c1[n] * sm[i].x) * sm[i].y;
                st64(k.rs, dst + m * 20480 + n * 512, (u32x2){cvtpk(y[0], y[1]), cvtpk(y[2], y[3])});
            }
        }
    }
}

constexpr int AT_KT = 8192, AT_KB = 10240, AT_HALF = AT_KB + 10240, AT_BUF = 2 * AT_HALF;
constexpr int AT_ONES = AT_BUF, AT_ONES_BYTES = 10752, AT_KC = AT_ONES + AT_ONES_BYTES, AT_KC_BYTES = 2048, AT_FLAG = AT_KC + AT_KC_BYTES, WO_LDS = 61440, AT_RED = 0;
DI int at_slot(int i) { return i == 0 ? 0 : (i == 1 ? 61440 : 61440 + AT_BUF); }
struct AttnCtx {
    unsigned voff[5]; const char* gsrc; unsigned ldst, sstride;
    u32x4 qf0[2]; u32x2 qf1[2];
    int kofs, kt, vlane; bool vdata, hi;
};
DI void attn_stage(const AttnCtx& c, int sidx, int slot) {
#pragma unroll
    for (int i = 0; i < 5; ++i) glds16<true>(c.voff[i], c.gsrc + sidx * c.sstride, c.ldst + at_slot(slot) + i * 1024);
}
DI void attn_setup(const Ctx& k, AttnCtx& c, int h) {
    const int lane = k.lane, w = k.w, kh = w >> 2, r = lane & 31, hi = lane >> 5;
    const bool stV = (w >> 1) & 1;
#pragma unroll
    for (int i = 0; i < 5; ++i) {
        const int pc = 5 * (w & 1) + i;
        if (!stV) c.voff[i] = pc < 8 ? pc * QK_TILE + lane * 16 : ((pc - 8) * 4 + (lane >> 4)) * QK_TILE + 1024 + (lane & 15) * 16;
        else { const int ci = 64 * pc + lane, st = ci / 40, wi = ci % 40; c.voff[i] = st * 20480 + ((wi % 20) * 2 + wi / 20) * 16; }
    }
    c.gsrc = k.ws + (stV ? WS_V + h * 640 + kh * 64 * 20480 : WS_K + h * 1280 + kh * 32 * QK_TILE);
    c.sstride = stV ? 16 * 20480 : 8 * QK_TILE;
    c.ldst = k.lds0 + kh * AT_HALF + (stV ? AT_KB : 0) + (w & 1) * 5120;
    c.kofs = kh * AT_HALF + hi * 256 + r * 8;
    c.hi = hi;
    c.kt = hi ? AT_KC + r * 8 : kh * AT_HALF + AT_KT + r * 8;
    const int rr = r >= 28 ? r - 12 : r;
    c.vdata = rr < 20;
    c.vlane = c.vdata ? kh * AT_HALF + AT_KB + hi * 320 + rr * 16 : AT_ONES + 128;
    for (int i = k.tid; i < AT_ONES_BYTES / 4; i += 512) ((LAS unsigned*)(k.lds + AT_ONES))[i] = 0x3F803F80u;
    ((LAS unsigned*)(k.lds + AT_KC))[k.tid] = (k.tid & 1) ? 0u : 0x00003F80u;
    if (k.tid == 0) *(LAS unsigned*)(k.lds + AT_FLAG) = 0;
}
DI void attn_first_loads(const Ctx& k, AttnCtx& c, int h, int qb) {
    const int qg = k.w & 3, r = k.lane & 31, hi = k.lane >> 5;
    attn_stage(c, 0, 0);
#pragma unroll
    for (int i = 0; i < 2; ++i) {
        const unsigned qp = WS_Q + (qb * 8 + qg * 2 + i) * QK_TILE + h * 1280 + r * 8;
        const u32x2 a = ld64(k.rs, qp + hi * 256), b = ld64(k.rs, qp + hi * 256 + 512), e = ld64(k.rs, qp + 1024);
        c.qf0[i] = (u32x4){a.x, a.y, b.x, b.y};
        c.qf1[i] = hi ? (u32x2){0, 0} : e;
    }
    attn_stage(c, 1, 1);
}
DI bf16x8 kfrag0(lds_ptr p) { const u32x2 a = *(const LAS u32x2*)p, b = *(const LAS u32x2*)(p + 512); return as_frag((u32x4){a.x, a.y, b.x, b.y}); }
DI s16x4 kfrag1(lds_ptr p) { return __builtin_bit_cast(s16x4, *(const LAS u32x2*)p); }
DI s16x4 as_frag4(u32x2 v) { return __builtin_bit_cast(s16x4, v); }
DI f32x16 mfma32x8(s16x4 a, s16x4 b, f32x16 c) { return __builtin_amdgcn_mfma_f32_32x32x8bf16_1k(a, b, c, 0, 0, 0); }
DI void wo_prefetch(const Ctx& k, int ct) {
    for (int pc = k.w; pc < 100; pc += 8) glds16<true>(k.lane * 16, k.ws + WS_WO + ct * 102400 + pc * 1024, k.lds0 + WO_LDS + pc * 1024);
}
DI void attn_fast(const Ctx& k, AttnCtx& c, f32x16& oA, f32x16& oB, int ct3, int f4) {
    const lds_ptr lds = k.lds;
    const int hi = k.lane >> 5, kh = k.w >> 2;
    {
        const bf16x8 k0 = kfrag0(lds + c.kofs - kh * AT_HALF); const s16x4 k1 = kfrag1(lds + (hi ? c.kt : c.kt - kh * AT_HALF));
#pragma unroll
        for (int i = 0; i < 2; ++i) {
            f32x16 s = mfma32(k0, as_frag(c.qf0[i]), (f32x16){});
            s = mfma32x8(k1, as_frag4(c.qf1[i]), s);
            float m = s[0];
#pragma unroll
            for (int j = 1; j < 16; ++j) m = fmaxf(m, s[j]);
            m = swap32_max(m);
            if (hi) c.qf1[i].x = cvtpk(-m, 0.f) & 0xffffu;
        }
    }
    const bf16x8 qA0 = as_frag(c.qf0[0]), qB0 = as_frag(c.qf0[1]); const s16x4 qA1 = as_frag4(c.qf1[0]), qB1 = as_frag4(c.qf1[1]);
    f32x16 sA, sB, nA, nB;
    oA = (f32x16){}; oB = (f32x16){};
    bf16x8 kf0 = kfrag0(lds + c.kofs); s16x4 kf1 = kfrag1(lds + c.kt);
    sA = mfma32(kf0, qA0, (f32x16){}); sB = mfma32(kf0, qB0, (f32x16){});
    sA = mfma32x8(kf1, qA1, sA); sB = mfma32x8(kf1, qB1, sB);
    kf0 = kfrag0(lds + c.kofs + 1024); kf1 = kfrag1(lds + c.kt + 256);
    u32x4 pA0, pA1, pB0, pB1 = {0, 0, 0, 0};
    bf16x8 vf0, vf1 = {0, 0, 0, 0, 0, 0, 0, 0};
    unsigned warm = 0;
    u32x2 fa = {0, 0}, fb = fa, fc = fa, fd = fa;
#define GAP(MF, X, i, P, j) do { MF; SBAR(); X[i] = EX(X[i]); X[i + 1] = EX(X[i + 1]); X[i + 2] = EX(X[i + 2]); X[i + 3] = EX(X[i + 3]); \
        P[j] = cvtpk(X[i], X[i + 1]); P[j + 1] = cvtpk(X[i + 2], X[i + 3]); PIN(P); SBAR(); } while (0)
#define STEP(SA, SB, NA, NB, VP, KP, KT) do { \
        GAP(NA = mfma32(kf0, qA0, (f32x16){}), SA, 0, pA0, 0); vf0 = as_frag(lds_r128(VP)); SBAR(); \
        GAP(oB = mfma32(vf1, as_frag(pB1), oB), SA, 4, pA0, 2); vf1 = as_frag(lds_r128((VP) + 640)); SBAR(); \
        GAP(NB = mfma32(kf0, qB0, (f32x16){}), SA, 8, pA1, 0); \
        GAP(NA = mfma32x8(kf1, qA1, NA), SA, 12, pA1, 2); \
        GAP(NB = mfma32x8(kf1, qB1, NB), SB, 0, pB0, 0); kf0 = kfrag0(KP); kf1 = kfrag1(KT); SBAR(); \
        GAP(oA = mfma32(vf0, as_frag(pA0), oA), SB, 4, pB0, 2); \
        GAP(oA = mfma32(vf1, as_frag(pA1), oA), SB, 8, pB1, 0); \
        GAP(oB = mfma32(vf0, as_frag(pB0), oB), SB, 12, pB1, 2); \
    } while (0)
    int slot = 0;
    PRIO(1);
    for (int sidx = 0; sidx < 4; ++sidx) {
        const int nslot = slot == 2 ? 0 : slot + 1;
        const int sb = at_slot(slot), sn = at_slot(nslot);
        const lds_ptr vb = lds + (c.vdata ? c.vlane + sb : c.vlane);
        lds_ptr k0 = lds + c.kofs + sb, k0n = lds + c.kofs + sn, k1 = lds + (hi ? c.kt : c.kt + sb), k1n = lds + (hi ? c.kt : c.kt + sn);
        PIN(k0); PIN(k0n); PIN(k1); PIN(k1n);
        STEP(sA, sB, nA, nB, vb, k0 + 2 * 1024, k1 + 2 * 256);
        STEP(nA, nB, sA, sB, vb + 1280, k0 + 3 * 1024, k1 + 3 * 256);
        STEP(sA, sB, nA, nB, vb + 2 * 1280, k0 + 4 * 1024, k1 + 4 * 256);
        if (sidx == 1 && k.w == 0) {
            const unsigned o = WS_FP + k.lane * FLAG_STRIDE;
            fa = ld64(k.rs, o); fb = ld64(k.rs, o + 64 * FLAG_STRIDE); fc = ld64(k.rs, o + 128 * FLAG_STRIDE); fd = ld64(k.rs, o + 192 * FLAG_STRIDE);
        }
        STEP(nA, nB, sA, sB, vb + 3 * 1280, k0 + 5 * 1024, k1 + 5 * 256);
        STEP(sA, sB, nA, nB, vb + 4 * 1280, k0 + 6 * 1024, k1 + 6 * 256);
        STEP(nA, nB, sA, sB, vb + 5 * 1280, k0 + 7 * 1024, k1 + 7 * 256);
        if (sidx == 1 && k.w == 0) {
            const unsigned tl = (unsigned)k.tag, th = (unsigned)(k.tag >> 32);
            if (!__all(fa.x == tl && fa.y == th && fb.x == tl && fb.y == th && fc.x == tl && fc.y == th && fd.x == tl && fd.y == th)) poll_all(k, WS_FP);
        }
        if (sidx == 0) STAMP(20); else if (sidx == 1) STAMP(22); else if (sidx == 2) STAMP(24); else STAMP(26);
        WAIT_BAR(0);
        if (sidx == 0) STAMP(21); else if (sidx == 1) STAMP(23); else if (sidx == 2) STAMP(25); else STAMP(27);
        if (sidx < 2) attn_stage(c, sidx + 2, nslot == 2 ? 0 : nslot + 1);
        if (sidx == 0 && k.tid == 0) st_flag(k.ws + WS_FP + k.c * FLAG_STRIDE, k.tag);
        if (sidx == 1) {
            const unsigned o = WS_W13 + f4 * 20480 + (k.lb >> 4) * 71680 + (k.w * 64 + k.lane) * 128;
            warm = ld32(k.rs, o);
            if (k.w == 0 && k.lane < 48) warm ^= ld32(k.rs, o + 65536);
        }
        if (sidx == 3) wo_prefetch(k, ct3);
        STEP(sA, sB, nA, nB, vb + 6 * 1280, k0n, k1n);
        STEP(nA, nB, sA, sB, vb + 7 * 1280, k0n + 1024, k1n + 256);
        slot = nslot;
    }
    PRIO(0);
    oB = mfma32(vf1, as_frag(pB1), oB);
    asm volatile("" :: "v"(warm));
#undef GAP
#undef STEP
}
DI void attn_safe(const Ctx& k, AttnCtx& c, f32x16 (&o)[2], float (&mrun)[2]) {
    const lds_ptr lds = k.lds;
    attn_stage(c, 0, 0);
    if (k.lane >> 5) { c.qf1[0].x = 0; c.qf1[1].x = 0; }
    o[0] = (f32x16){}; o[1] = (f32x16){};
    mrun[0] = -INFINITY; mrun[1] = -INFINITY;
    for (int sidx = 0; sidx < 4; ++sidx) {
        const int sb = at_slot(sidx & 1);
        WAIT_BAR(0);
        if (sidx < 3) attn_stage(c, sidx + 1, (sidx + 1) & 1);
        const lds_ptr vb = lds + (c.vdata ? c.vlane + sb : c.vlane);
        for (int kt = 0; kt < 8; ++kt) {
            const bf16x8 k0 = kfrag0(lds + c.kofs + sb + kt * 1024); const s16x4 k1 = kfrag1(lds + (c.hi ? c.kt : c.kt + sb) + kt * 256);
            const bf16x8 v0 = as_frag(lds_r128(vb + kt * 1280)), v1 = as_frag(lds_r128(vb + kt * 1280 + 640));
#pragma unroll
            for (int i = 0; i < 2; ++i) {
                f32x16 s = mfma32(k0, as_frag(c.qf0[i]), (f32x16){});
                s = mfma32x8(k1, as_frag4(c.qf1[i]), s);
                float m = s[0];
#pragma unroll
                for (int j = 1; j < 16; ++j) m = fmaxf(m, s[j]);
                m = fmaxf(swap32_max(m), mrun[i]);
                const float alpha = EX(mrun[i] - m);
                mrun[i] = m;
                unsigned pk[8];
#pragma unroll
                for (int j = 0; j < 16; ++j) o[i][j] *= alpha;
#pragma unroll
                for (int j = 0; j < 8; ++j) pk[j] = cvtpk(EX(s[2 * j] - m), EX(s[2 * j + 1] - m));
                o[i] = mfma32(v0, as_frag((u32x4){pk[0], pk[1], pk[2], pk[3]}), o[i]);
                o[i] = mfma32(v1, as_frag((u32x4){pk[4], pk[5], pk[6], pk[7]}), o[i]);
            }
        }
    }
}
DI void p2_phase(const Ctx& k, AttnCtx& c, int h, int qb, int ct3, int f4) {
    const int lane = k.lane, qg = k.w & 3, kh = k.w >> 2, r = lane & 31, hi = lane >> 5;
    const lds_ptr lds = k.lds;
    attn_first_loads(k, c, h, qb);
    PreTile pt;
    pretile_read(k, pt);
    WAIT_BAR(5);
    STAMP(6);
    pretile_store(k, pt);
    f32x16 o[2];
    attn_fast(k, c, o[0], o[1], ct3, f4);
    STAMP(7);
    LAS unsigned* flag = (LAS unsigned*)(lds + AT_FLAG);
    if (__any(hi && !(o[0][8] <= 5.764607523034235e17f && o[1][8] <= 5.764607523034235e17f)) && lane == 0) *flag = 1;
    BAR_L();
    const lds_ptr red = lds + AT_RED + qg * 8192 + lane * 16;
    auto put = [&](const f32x16& v, int slot) {
#pragma unroll
        for (int u = 0; u < 3; ++u) *(LAS f32x4*)(red + slot * 4096 + u * 1024) = (f32x4){v[4 * u], v[4 * u + 1], v[4 * u + 2], v[4 * u + 3]};
    };
    auto get = [&](int slot, int u) { return *(const LAS f32x4*)(red + slot * 4096 + u * 1024); };
    f32x4 of[3];
    if (*flag == 0) {
        if (kh) put(o[0], 0); else put(o[1], 1);
        BAR_L();
#pragma unroll
        for (int u = 0; u < 3; ++u) {
            const f32x4 p = get(kh, u);
#pragma unroll
            for (int j = 0; j < 4; ++j) of[u][j] = p[j] + (kh ? o[1][4 * u + j] : o[0][4 * u + j]);
        }
    } else {
        float mref[2];
        WAIT_BAR(0);
        attn_safe(k, c, o, mref);
        BAR_L();
        if (kh) put(o[0], 0); else put(o[1], 1);
        *(LAS float*)(red + (kh ? 0 : 4096) + 3072) = kh ? mref[0] : mref[1];
        BAR_L();
        const float m0 = kh ? mref[1] : mref[0], m1 = *(const LAS float*)(red + kh * 4096 + 3072), m = fmaxf(m0, m1);
        const float a0 = EX(m0 - m), a1 = EX(m1 - m);
#pragma unroll
        for (int u = 0; u < 3; ++u) {
            const f32x4 p = get(kh, u);
#pragma unroll
            for (int j = 0; j < 4; ++j) of[u][j] = p[j] * a1 + (kh ? o[1][4 * u + j] : o[0][4 * u + j]) * a0;
        }
        BAR_L();
        wo_prefetch(k, ct3);
    }
    {
        auto lr = __builtin_amdgcn_permlane32_swap(__float_as_uint(of[2][0]), __float_as_uint(of[2][0]), false, false);
        const float inv = 1.0f / __uint_as_float(lr[1]);
        const int tok = qb * 256 + qg * 64 + kh * 32 + r;
        const unsigned base = WS_O + (tok >> 4) * OH_TILE + (tok & 15) * 16;
        unsigned pk[3][2];
#pragma unroll
        for (int u = 0; u < 3; ++u) { pk[u][0] = cvtpk(of[u][0] * inv, of[u][1] * inv); pk[u][1] = cvtpk(of[u][2] * inv, of[u][3] * inv); }
        auto oaddr = [&](int col) { return base + (col >> 5) * 1024 + ((col >> 3) & 3) * 256 + (col & 7) * 2; };
        if ((h & 1) == 0) {
            auto s0 = __builtin_amdgcn_permlane32_swap(pk[0][0], pk[1][0], false, false);
            auto s1 = __builtin_amdgcn_permlane32_swap(pk[0][1], pk[1][1], false, false);
            st128(k.rs, oaddr(h * 20 + 8 * hi), (u32x4){s0[0], s1[0], s0[1], s1[1]});
            if (!hi) st64(k.rs, oaddr(h * 20 + 16), (u32x2){pk[2][0], pk[2][1]});
        } else {
            auto s0 = __builtin_amdgcn_permlane32_swap(pk[0][0], pk[2][0], false, false);
            auto s1 = __builtin_amdgcn_permlane32_swap(pk[0][1], pk[2][1], false, false);
            st128(k.rs, oaddr(h * 20 + 4 + 8 * hi), hi ? (u32x4){pk[1][0], pk[1][1], s0[0], s1[0]} : (u32x4){s0[1], s1[1], pk[1][0], pk[1][1]});
            if (!hi) st64(k.rs, oaddr(h * 20), (u32x2){s0[0], s1[0]});
        }
    }
}

DI int own_cf(int kh, int u) { return kh ? (u < 2 ? 3 + u : 4) : u; }
constexpr int RED_LDS = 143360;
struct P3Pre { u32x2 xv[5]; };
DI void p3_pre(const Ctx& k, int rb64, int ct, P3Pre& p) {
    const int tt = k.w & 3, t = k.lane & 15, g = k.lane >> 4;
    if (k.w < 4) {
#pragma unroll
        for (int cf = 0; cf < 5; ++cf) p.xv[cf] = *(const u32x2*)(k.x + (size_t)(rb64 * 64 + tt * 16 + t) * D + ct * 80 + cf * 16 + 4 * g);
    }
}
DI void w13_prefetch(const Ctx& k, int f0, int f);
DI void p3_phase(const Ctx& k, int rb64, int ct, int f0, const P3Pre& pre, u32x2 (&hv)[3]) {
    const int lane = k.lane, tt = k.w & 3, kh = k.w >> 2, t = lane & 15, g = lane >> 4;
    const unsigned ap = WS_O + (rb64 * 4 + tt) * OH_TILE + kh * 10240 + lane * 16;
    const lds_ptr wl = k.lds + WO_LDS + kh * 51200 + lane * 16;
    constexpr int WIN = 5;
    u32x4 a[10];
#pragma unroll
    for (int i = 0; i < WIN; ++i) a[i] = ld128(k.rs, ap + i * 1024);
    bf16x8 wf[2][5];
#pragma unroll
    for (int cf = 0; cf < 5; ++cf) wf[0][cf] = as_frag(lds_r128(wl + cf * 1024));
    f32x4 acc[5];
#pragma unroll
    for (int cf = 0; cf < 5; ++cf) { acc[cf] = (f32x4){0, 0, 0, 0}; PIN(acc[cf]); }
    mfma_fence();
    PRIO(1);
#pragma unroll
    for (int i = 0; i < 10; ++i) {
        if (i + WIN < 10) { a[i + WIN] = ld128(k.rs, ap + (i + WIN) * 1024); SBAR(); }
#pragma unroll
        for (int cf = 0; cf < 5; ++cf) {
            if (i + 1 < 10) wf[(i + 1) & 1][cf] = as_frag(lds_r128(wl + (i + 1) * 5120 + cf * 1024));
            mfma16_acc(acc[cf], wf[i & 1][cf], as_frag(a[i]));
            SBAR();
        }
    }
    PRIO(0);
    mfma_fence();
    STAMP(10);
    BAR_L();
    const lds_ptr red = k.lds + RED_LDS + tt * 5120 + lane * 16;
    if (kh) {
#pragma unroll
        for (int cf = 0; cf < 5; ++cf) *(LAS f32x4*)(red + cf * 1024) = acc[cf];
        BAR_L();
        w13_prefetch(k, f0, k.w - 1);
    } else {
        BAR_L();
        const int tok = rb64 * 64 + tt * 16 + t;
        float s = 0.f, q = 0.f;
        u32x2 hq[5];
#pragma unroll
        for (int cf = 0; cf < 5; ++cf) {
            const f32x4 v = acc[cf] + *(const LAS f32x4*)(red + cf * 1024);
            hq[cf] = (u32x2){cvtpk(v[0] + bflo(pre.xv[cf].x), v[1] + bfhi(pre.xv[cf].x)), cvtpk(v[2] + bflo(pre.xv[cf].y), v[3] + bfhi(pre.xv[cf].y))};
            const int col = ct * 80 + cf * 16 + 4 * g;
            st64(k.rs, WS_H + (tok >> 4) * OH_TILE + (col >> 5) * 1024 + ((col >> 3) & 3) * 256 + t * 16 + (col & 7) * 2, hq[cf]);
            const float h0 = bflo(hq[cf].x), h1 = bfhi(hq[cf].x), h2 = bflo(hq[cf].y), h3 = bfhi(hq[cf].y);
            s += (h0 + h1) + (h2 + h3); q += (h0 * h0 + h1 * h1) + (h2 * h2 + h3 * h3);
        }
        s = sum_x32(sum_x16(s)); q = sum_x32(sum_x16(q));
        if (g == 0) st64(k.rs, WS_PST + (tok >> 4) * 2048 + (ct * 2) * 128 + t * 8, (u32x2){__float_as_uint(s), __float_as_uint(q)});
        if (g == 1) st64(k.rs, WS_PST + (tok >> 4) * 2048 + (ct * 2 + 1) * 128 + t * 8, (u32x2){0u, 0u});
        hv[0] = hq[0]; hv[1] = hq[1]; hv[2] = hq[2];
        const lds_ptr stash = k.lds + RED_LDS + tt * 5120 + lane * 8;
        *(LAS u32x2*)(stash + 3072) = hq[3]; *(LAS u32x2*)(stash + 4096) = hq[4];
    }
}
DI void pair_reduce(lds_ptr red0, int tt, int kh, int lane, const f32x4 (&acc)[5], f32x4 (&own)[3]) {
    const lds_ptr red = red0 + tt * 5120 + lane * 16;
    if (kh == 0) { *(LAS f32x4*)(red + 3072) = acc[3]; *(LAS f32x4*)(red + 4096) = acc[4]; }
    else { *(LAS f32x4*)(red) = acc[0]; *(LAS f32x4*)(red + 1024) = acc[1]; *(LAS f32x4*)(red + 2048) = acc[2]; }
    BAR_L();
    if (kh == 0) {
#pragma unroll
        for (int u = 0; u < 3; ++u) own[u] = acc[u] + *(const LAS f32x4*)(red + u * 1024);
    } else {
        own[0] = acc[3] + *(const LAS f32x4*)(red + 3072); own[1] = acc[4] + *(const LAS f32x4*)(red + 4096); own[2] = own[1];
    }
}
template <int N, int NP>
DI void w2_stage(const Ctx& k, int ct, int b, int s0, int w0) {
    const int nw = 8 - w0;
#pragma unroll
    for (int j = 0; j < NP; ++j) {
        int q = k.w - w0 + nw * j;
        if (q >= 10 * N) q -= nw;
        const int hf = q >= 5 * N, rem = q - hf * 5 * N, n = rem / 5;
        glds16<true>(k.lane * 16, k.ws + WS_W2 + (ct * 54 + hf * 27 + s0) * 5120 + rem * 1024, k.lds0 + n * 20480 + b * 10240 + hf * 5120 + (rem - 5 * n) * 1024);
    }
}
DI void p5_phase(const Ctx& k, int rb64, int ct, u32x2 (&hv)[3]) {
    const int lane = k.lane, tt = k.w & 3, kh = k.w >> 2, t = lane & 15, g = lane >> 4;
    const unsigned ap = WS_HH + (rb64 * 4 + tt) * HH_TILE + kh * 27 * 1024 + lane * 16;
    constexpr int WIN = 6;
    u32x4 a[27];
#pragma unroll
    for (int i = 0; i < WIN; ++i) ald128(a[i], k.rsw, ap, i * 1024);
    if (kh) {
        const lds_ptr stash = k.lds + RED_LDS + tt * 5120 + lane * 8;
        hv[0] = *(const LAS u32x2*)(stash + 3072); hv[1] = *(const LAS u32x2*)(stash + 4096); hv[2] = hv[1];
    }
    lds_ptr wb[3] = {k.lds + lane * 16 + kh * 5120, k.lds + lane * 16 + kh * 5120 + 61440, k.lds + lane * 16 + kh * 5120 + 122880};
    PIN(wb[0]); PIN(wb[1]); PIN(wb[2]);
    auto wread = [&](int n, int cf) {
        const int m = n % 7;
        return as_frag(lds_r128(wb[m / 3] + (m % 3) * 20480 + ((n / 7) & 1) * 10240 + cf * 1024));
    };
    bf16x8 wf[2][5];
#pragma unroll
    for (int cf = 0; cf < 5; ++cf) wf[0][cf] = wread(0, cf);
    f32x4 acc[5];
#pragma unroll
    for (int cf = 0; cf < 5; ++cf) { acc[cf] = (f32x4){0, 0, 0, 0}; PIN(acc[cf]); }
    mfma_fence();
    PRIO(1);
#define P5_STEP(i, NW) do { \
        if ((i) == 7) { BAR_L(); w2_stage<7, 9>(k, ct, 0, 14, 0); } \
        if ((i) == 13) { WAIT_BAR(6); w2_stage<6, 8>(k, ct, 1, 21, 0); } \
        if ((i) == 20) { WAIT_BAR(6); } \
        if ((i) + WIN < 27) ald128(a[(i) + WIN], k.rsw, ap, ((i) + WIN) * 1024); \
        ald_wait<NW>(a[i]); SBAR(); \
        _Pragma("unroll") for (int cf = 0; cf < 5; ++cf) { \
            if ((i) + 1 < 27) wf[((i) + 1) & 1][cf] = wread((i) + 1, cf); \
            mfma16_acc(acc[cf], wf[(i) & 1][cf], as_frag(a[i])); SBAR(); } } while (0)
    P5_STEP(0, 6); P5_STEP(1, 6); P5_STEP(2, 6); P5_STEP(3, 6); P5_STEP(4, 6); P5_STEP(5, 6); P5_STEP(6, 6);
    P5_STEP(7, 15); P5_STEP(8, 15); P5_STEP(9, 15); P5_STEP(10, 15); P5_STEP(11, 15); P5_STEP(12, 15);
    P5_STEP(13, 14); P5_STEP(14, 14); P5_STEP(15, 14); P5_STEP(16, 14); P5_STEP(17, 14); P5_STEP(18, 14); P5_STEP(19, 6);
    P5_STEP(20, 6); P5_STEP(21, 5); P5_STEP(22, 4); P5_STEP(23, 3); P5_STEP(24, 2); P5_STEP(25, 1); P5_STEP(26, 0);
#undef P5_STEP
    PRIO(0);
    mfma_fence();
    STAMP(16);
    f32x4 own[3];
    pair_reduce(k.lds + RED_LDS, tt, kh, lane, acc, own);
    const int tok = rb64 * 64 + tt * 16 + t;
#pragma unroll
    for (int u = 0; u < 3; ++u) {
        if (u < 2 || kh == 0) {
            const int col = ct * 80 + own_cf(kh, u) * 16 + 4 * g;
            *(u32x2*)(k.out + (size_t)tok * D + col) = (u32x2){cvtpk(own[u][0] + bflo(hv[u].x), own[u][1] + bfhi(hv[u].x)), cvtpk(own[u][2] + bflo(hv[u].y), own[u][3] + bfhi(hv[u].y))};
        }
    }
}

constexpr int P4_CS = 143360;
DI void w13_prefetch(const Ctx& k, int f0, int f) {
    const int p0 = f * 20;
#pragma unroll 4
    for (int j = 0; j < 20; ++j) glds16<true>(k.lane * 16, k.ws + WS_W13 + f0 * 20480 + (p0 + j) * 1024, k.lds0 + (p0 + j) * 1024);
}
DI void p4_colsum(const Ctx& k, int f) {
    const int t = k.lane & 15, g = k.lane >> 4;
    u32x4 wv[20];
#pragma unroll
    for (int s = 0; s < 20; ++s) wv[s] = lds_r128(k.lds + (f * 20 + s) * 1024 + k.lane * 16);
    f32x4 c0 = {0, 0, 0, 0}, c1 = {0, 0, 0, 0};
    const bf16x8 ones = {0x3F80, 0x3F80, 0x3F80, 0x3F80, 0x3F80, 0x3F80, 0x3F80, 0x3F80};
#pragma unroll
    for (int s = 0; s < 20; s += 2) { c0 = mfma16(as_frag(wv[s]), ones, c0); c1 = mfma16(as_frag(wv[s + 1]), ones, c1); }
    if (t == 0) *(LAS f32x4*)(k.lds + P4_CS + f * 64 + g * 16) = c0 + c1;
}
DI void p4_phase(const Ctx& k, int rb, int f0, int nf, int ct5) {
    const int lane = k.lane, w = k.w, t = lane & 15, g = lane >> 4;
    const lds_ptr lds = k.lds;
    const unsigned ap = WS_H + (rb * 16 + w * 2) * OH_TILE + lane * 16;
    u32x4 a0[20], a1[20];
#define P4_LD(s) do { a0[s] = ld128(k.rs, ap + (s) * 1024); a1[s] = ld128(k.rs, ap + OH_TILE + (s) * 1024); } while (0)
    u32x2 pv[2][4];
#pragma unroll
    for (int i = 0; i < 2; ++i)
#pragma unroll
        for (int j = 0; j < 4; ++j) pv[i][j] = ld64(k.rs, WS_PST + (rb * 16 + w * 2 + i) * 2048 + (4 * g + j) * 128 + t * 8);
    P4_LD(0); P4_LD(1); P4_LD(2);
    lds_ptr wb[3] = {lds + lane * 16, lds + lane * 16 + 61440, lds + lane * 16 + 122880};
    PIN(wb[1]); PIN(wb[2]);
    bf16x8 wf[2][7];
#pragma unroll
    for (int f = 0; f < 7; ++f) wf[0][f] = as_frag(lds_r128(wb[f / 3] + (f % 3) * 20480));
    f32x4 acc[2][7];
#pragma unroll
    for (int f = 0; f < 7; ++f) { acc[0][f] = (f32x4){0, 0, 0, 0}; acc[1][f] = (f32x4){0, 0, 0, 0}; PIN(acc[0][f]); PIN(acc[1][f]); }
    float mean[2], rstd[2], vpe[2];
    mfma_fence();
    STAMP(13);
    PRIO(1);
#pragma unroll
    for (int s = 0; s < 20; ++s) {
        if (s < 6) { P4_LD(2 * s + 3); P4_LD(2 * s + 4); } else if (s + 9 < 20) P4_LD(s + 9);
        SBAR();
        if (s == 10) { __builtin_amdgcn_s_barrier(); w2_stage<7, 9>(k, ct5, 0, 0, 0); SBAR(); }
        if (s == 4) {
#pragma unroll
            for (int i = 0; i < 2; ++i) {
                float sm = (__uint_as_float(pv[i][0].x) + __uint_as_float(pv[i][1].x)) + (__uint_as_float(pv[i][2].x) + __uint_as_float(pv[i][3].x));
                float q = (__uint_as_float(pv[i][0].y) + __uint_as_float(pv[i][1].y)) + (__uint_as_float(pv[i][2].y) + __uint_as_float(pv[i][3].y));
                sm = sum_x32(sum_x16(sm)); q = sum_x32(sum_x16(q));
                mean[i] = sm * (1.0f / D);
                vpe[i] = fmaxf(q * (1.0f / D) - mean[i] * mean[i], 0.f) + LN_EPS;
                rstd[i] = __builtin_amdgcn_rsqf(vpe[i]);
            }
            PIN(mean[0]); PIN(mean[1]); PIN(rstd[0]); PIN(rstd[1]); PIN(vpe[0]); PIN(vpe[1]);
            SBAR();
        }
#pragma unroll
        for (int f = 0; f < 7; ++f) {
            if (s + 1 < 20) wf[(s + 1) & 1][f] = as_frag(lds_r128(wb[f / 3] + (f % 3) * 20480 + (s + 1) * 1024));
            mfma16_acc(acc[0][f], wf[s & 1][f], as_frag(a0[s]));
            mfma16_acc(acc[1][f], wf[s & 1][f], as_frag(a1[s]));
            SBAR();
        }
    }
#undef P4_LD
    PRIO(0);
    mfma_fence();
    STAMP(14);
    f32x4 cs[7];
#pragma unroll
    for (int f = 0; f < 7; ++f) cs[f] = *(const LAS f32x4*)(lds + P4_CS + f * 64 + g * 16);
#pragma unroll
    for (int i = 0; i < 2; ++i) {
        const float ce = -LOG2E * rstd[i], ir2 = vpe[i], nm = -mean[i];
        const f32x2 ce2 = {ce, ce}, ir22 = {ir2, ir2}, nm2 = {nm, nm};
        unsigned d[8];
#pragma unroll
        for (int f = 0; f < 7; ++f) {
            const f32x2 ga = __builtin_elementwise_fma(nm2, __builtin_shufflevector(cs[f], cs[f], 0, 1), __builtin_shufflevector(acc[i][f], acc[i][f], 0, 1));
            const f32x2 ua = __builtin_elementwise_fma(nm2, __builtin_shufflevector(cs[f], cs[f], 2, 3), __builtin_shufflevector(acc[i][f], acc[i][f], 2, 3));
            const f32x2 t2 = ga * ce2;
            const f32x2 e2 = {EX(t2.x), EX(t2.y)};
            const f32x2 d2 = __builtin_elementwise_fma(e2, ir22, ir22);
            const f32x2 r2 = {__builtin_amdgcn_rcpf(d2.x), __builtin_amdgcn_rcpf(d2.y)};
            const f32x2 o2 = (ga * ua) * r2;
            d[f] = cvtpk(o2.x, o2.y);
        }
        d[7] = 0;
        const unsigned dst = WS_HH + (rb * 16 + w * 2 + i) * HH_TILE + f0 * 256 + lane * 16;
#pragma unroll
        for (int u = 0; u < 2; ++u) {
            auto a = __builtin_amdgcn_permlane32_swap(d[4 * u], d[4 * u + 2], false, false);
            auto b = __builtin_amdgcn_permlane32_swap(d[4 * u + 1], d[4 * u + 3], false, false);
            auto e = __builtin_amdgcn_permlane16_swap(a[0], b[0], false, false);
            auto f = __builtin_amdgcn_permlane16_swap(a[1], b[1], false, false);
            if (4 * u + g < nf) st128(k.rs, dst + u * 1024, (u32x4){e[0], e[1], f[0], f[1]});
        }
    }
}

constexpr int LDS_BYTES = 163840;
__global__ __launch_bounds__(512, 2) void fused_block(const uint16_t* __restrict__ x, const uint16_t* __restrict__ wqkv, const uint16_t* __restrict__ wo,
                                                      const uint16_t* __restrict__ w1, const uint16_t* __restrict__ w3, const uint16_t* __restrict__ w2,
                                                      char* __restrict__ ws, uint16_t* __restrict__ out) {
    extern __shared__ __attribute__((aligned(16))) char smem[];
    Ctx k;
    k.x = x; k.wqkv = wqkv; k.wo = wo; k.w1 = w1; k.w3 = w3; k.w2 = w2; k.ws = ws; k.out = out;
    k.rs = __builtin_amdgcn_make_buffer_rsrc(ws, 0, WS_END + 20480, 0x00020000);
    k.lds = (lds_ptr)smem; k.lds0 = (unsigned)(uintptr_t)smem;
    k.c = blockIdx.x; k.xcd = k.c & 7; k.lb = k.c >> 3;
    k.tid = threadIdx.x; k.lane = k.tid & 63; k.w = rfl(k.tid >> 6);
    k.rsw = (u32x4){(unsigned)(uintptr_t)ws, (unsigned)((uintptr_t)ws >> 32) & 0xffffu, WS_END + 20480, 0x00020000u};
    k.tag = 0;
    k.sc = 0; k.sr = 0;
    STAMP(0);
    if (k.w == 7) {
        const int i = k.lb * 64 + k.lane;
        const char* p = ws;
        constexpr int NWS = WS_END / 65536 + 1;
        if (i < NWS) p = ws + (size_t)i * 65536;
        else if (i < NWS + 40) p = (const char*)out + (size_t)(i - NWS) * 65536;
        else if (i < NWS + 80) p = (const char*)x + (size_t)(i - NWS - 40) * 65536;
        else if (i < NWS + 117) p = (const char*)wqkv + (size_t)(i - NWS - 80) * 65536;
        else if (i < NWS + 129) p = (const char*)wo + (size_t)(i - NWS - 117) * 65536;
        else if (i < NWS + 162) p = (const char*)w1 + (size_t)(i - NWS - 129) * 65536;
        else if (i < NWS + 195) p = (const char*)w3 + (size_t)(i - NWS - 162) * 65536;
        else if (i < NWS + 228) p = (const char*)w2 + (size_t)(i - NWS - 195) * 65536;
        glds4_sc1(p, k.lds0 + SU_LDS + 2048);
    }
    startup_load(k);
    const int rb1 = (k.xcd >> 2) * 8 + (k.lb & 7), cb1 = (k.xcd & 3) * 4 + (k.lb >> 3);
    if (cb1 < 15) {
        if (cb1 >= 10) p1_body<true>(k, rb1, cb1); else p1_body<false>(k, rb1, cb1);
        STAMP(3);
        WAIT_BAR(0);
        if (k.tid == 0) st_flag(ws + WS_F1 + (rb1 * 16 + cb1) * FLAG_STRIDE, k.tag);
    } else {
        WAIT_VM(0);
        startup_tag(k);
        ln1_stats(k, rb1);
        if (rb1 < 15) colsum_qkv(k, rb1);
        WAIT_BAR(0);
    }
    STAMP(4);
    if (k.w) pretile_load(k);
    const int h = k.xcd * 4 + (k.lb & 3), qb = k.lb >> 2;
    const int rb64 = k.xcd * 4 + (k.lb >> 3), ct = k.lb & 7;
    const int rb4 = (k.xcd >> 1) * 2 + (k.lb >> 4), cb4 = (k.xcd & 1) * 16 + (k.lb & 15);
    const int f0 = cb4 < 24 ? cb4 * 7 : 168 + (cb4 - 24) * 6, nf = cb4 < 24 ? 7 : 6;
    AttnCtx ac;
    attn_setup(k, ac, h);
    if (k.w == 0) {
        const int c0 = (20 * h) >> 7, c1 = (20 * h + 19) >> 7, j = k.lane & 15, sel = k.lane >> 4;
        const int cbk = (sel & 1) ? c1 : c0;
        const unsigned o0 = WS_F1 + (j * 16 + (sel < 2 ? 5 : 10) + cbk) * FLAG_STRIDE;
        const unsigned o1 = WS_F1 + ((2 * qb + (k.lane & 1)) * 16 + ((k.lane & 2) ? c1 : c0)) * FLAG_STRIDE;
        poll_flags2(k, o0, o1);
    }
    WAIT_BAR(0);
    STAMP(5);
    p2_phase(k, ac, h, qb, ct, f0);
    STAMP(8);
    WAIT_BAR(0);
    if (k.tid == 0) st_flag(ws + WS_F2 + (qb * 32 + h) * FLAG_STRIDE, k.tag);
    P3Pre pre;
    p3_pre(k, rb64, ct, pre);
    if (k.w == 0) poll_flags(k, WS_F2 + (k.xcd * 32 + (k.lane & 31)) * FLAG_STRIDE);
    else if (k.w <= 3) w13_prefetch(k, f0, k.w - 1);
    WAIT_BAR(0);
    if (k.c == 0 && k.tid == 0) st_flag(ws + WS_EPOCH, k.tag);
    STAMP(9);
    u32x2 hv[3];
    p3_phase(k, rb64, ct, f0, pre, hv);
    STAMP(11);
    if (k.w < 4) WAIT_VM(0);
    BAR_L();
    if (k.tid == 0) st_flag(ws + WS_F3 + (k.xcd * 32 + k.lb) * FLAG_STRIDE, k.tag);
    if (k.w == 0) poll_flags(k, WS_F3 + (rb4 * 32 + (k.lane & 31)) * FLAG_STRIDE);
    else {
        WAIT_VM(0);
        p4_colsum(k, k.w - 1);
    }
    WAIT_BAR(0);
    STAMP(12);
    p4_phase(k, rb4, f0, nf, ct);
    STAMP(15);
    WAIT_BAR(0);
    if (k.tid == 0) st_flag(ws + WS_F4 + (rb4 * 32 + cb4) * FLAG_STRIDE, k.tag);
    if (k.w == 0) poll_flags(k, WS_F4 + ((rb64 >> 2) * 32 + (k.lane & 31)) * FLAG_STRIDE);
    else {
        w2_stage<7, 10>(k, ct, 1, 7, 1);
        const int li = (k.w - 1) * 64 + k.lane;
        if (k.w <= 5) {
            const int ln = (k.lb >> 3) * 260 + (li < 260 ? li : li - 64), hf = ln >= 520;
            const unsigned warm = ld32(k.rs, WS_W2 + (ct * 54 + hf * 27 + 14) * 5120 + (ln - hf * 520) * 128);
            asm volatile("" :: "v"(warm));
        }
    }
    WAIT_BAR(0);
    STAMP(17);
    p5_phase(k, rb64, ct, hv);
    STAMP(18);
    STAMP_FLUSH();
}

extern "C" void kernel_launch(void* const* d_in, const int* in_sizes, int n_in, void* d_out, int out_size, void* d_ws, size_t ws_size, hipStream_t stream) {
    (void)hipFuncSetAttribute((const void*)fused_block, hipFuncAttributeMaxDynamicSharedMemorySize, LDS_BYTES);
    fused_block<<<256, 512, LDS_BYTES, stream>>>((const uint16_t*)d_in[0], (const uint16_t*)d_in[1], (const uint16_t*)d_in[2], (const uint16_t*)d_in[4],
                                                 (const uint16_t*)d_in[5], (const uint16_t*)d_in[6], (char*)d_ws, (uint16_t*)d_out);
}
```

```cpp
#include <hip/hip_runtime.h>
#include <hip/hip_bf16.h>
#include <stdint.h>

#define DI __device__ __forceinline__
#define LAS __attribute__((address_space(3)))
#define GAS __attribute__((address_space(1)))
typedef short bf16x8 __attribute__((ext_vector_type(8)));
typedef short s16x4 __attribute__((ext_vector_type(4)));
typedef float f32x2 __attribute__((ext_vector_type(2)));
typedef float f32x4 __attribute__((ext_vector_type(4)));
typedef float f32x16 __attribute__((ext_vector_type(16)));
typedef unsigned u32x2 __attribute__((ext_vector_type(2)));
typedef unsigned u32x4 __attribute__((ext_vector_type(4)));
typedef unsigned long long u64;
typedef __bf16 bf16x2_t __attribute__((ext_vector_type(2)));
typedef short v4i16_t __attribute__((ext_vector_type(4)));
typedef LAS char* lds_ptr;
typedef __amdgpu_buffer_rsrc_t rsrc_t;

constexpr int S = 2048, D = 640, H = 32, DH = 20, F = 1728, NQKV = 3 * D;
constexpr float LN_EPS = 1e-5f;
constexpr float LOG2E = 1.4426950408889634f;
constexpr float QSCALE = 0.22360679774997896f * LOG2E;

constexpr unsigned WS_EPOCH = 0;
constexpr unsigned FLAG_STRIDE = 64, FLAG_ARR = 256 * FLAG_STRIDE;
constexpr unsigned WS_F1 = 4096, WS_FP = WS_F1 + FLAG_ARR, WS_F2 = WS_FP + FLAG_ARR, WS_F3 = WS_F2 + FLAG_ARR, WS_F4 = WS_F3 + FLAG_ARR;
constexpr unsigned WS_ST1 = 131072;
constexpr unsigned WS_CSQ = WS_ST1 + 32768;
constexpr unsigned WS_PST = 262144;
constexpr unsigned WS_Q = 524288;
constexpr unsigned IMG = S * D * 2;
constexpr unsigned QK_TILE = 32 * 1280, QK_IMG = 64 * QK_TILE;
constexpr unsigned WS_K = WS_Q + QK_IMG;
constexpr unsigned WS_V = WS_K + QK_IMG;
constexpr unsigned OH_TILE = 21504, HH_TILE = 56320;
constexpr unsigned WS_O = WS_V + IMG;
constexpr unsigned WS_H = WS_O + 128 * OH_TILE;
constexpr unsigned WS_HH = WS_H + 128 * OH_TILE;
constexpr unsigned WS_WO = WS_HH + 128 * HH_TILE;
constexpr unsigned WS_W13 = WS_WO + D * D * 2;
constexpr unsigned WS_W2 = WS_W13 + D * F * 4;
constexpr unsigned WS_END = WS_W2 + F * D * 2;

DI unsigned cvtpk(float lo, float hi) { f32x2 v = {lo, hi}; bf16x2_t b = __builtin_convertvector(v, bf16x2_t); return __builtin_bit_cast(unsigned, b); }
DI float bflo(unsigned u) { return __uint_as_float(u << 16); }
DI float bfhi(unsigned u) { return __uint_as_float(u & 0xffff0000u); }
DI f32x4 mfma16(bf16x8 a, bf16x8 b, f32x4 c) { return __builtin_amdgcn_mfma_f32_16x16x32_bf16(a, b, c, 0, 0, 0); }
DI void mfma16_acc(f32x4& c, bf16x8 a, bf16x8 b) { asm volatile("v_mfma_f32_16x16x32_bf16 %0, %1, %2, %0" : "+v"(c) : "v"(a), "v"(b)); }
DI void mfma_fence() { __builtin_amdgcn_sched_barrier(0); asm volatile("s_nop 7\n\ts_nop 1" ::: "memory"); __builtin_amdgcn_sched_barrier(0); }
DI f32x16 mfma32(bf16x8 a, bf16x8 b, f32x16 c) { return __builtin_amdgcn_mfma_f32_32x32x16_bf16(a, b, c, 0, 0, 0); }
DI int rfl(int x) { return __builtin_amdgcn_readfirstlane(x); }
template <bool SC1>
DI void glds16(unsigned voff, const void* sbase, unsigned ldsdst) {
    unsigned keep;
    if (SC1) asm volatile("s_mov_b32 %0, m0\n\ts_mov_b32 m0, %3\n\ts_nop 0\n\tglobal_load_lds_dwordx4 %1, %2 sc1\n\ts_mov_b32 m0, %0" : "=&s"(keep) : "v"(voff), "s"(sbase), "s"(ldsdst) : "memory");
    else asm volatile("s_mov_b32 %0, m0\n\ts_mov_b32 m0, %3\n\ts_nop 0\n\tglobal_load_lds_dwordx4 %1, %2\n\ts_mov_b32 m0, %0" : "=&s"(keep) : "v"(voff), "s"(sbase), "s"(ldsdst) : "memory");
}
DI void glds4(const void* gptr, unsigned ldsdst) {
    unsigned keep;
    asm volatile("s_mov_b32 %0, m0\n\ts_mov_b32 m0, %2\n\ts_nop 0\n\tglobal_load_lds_dword %1, off\n\ts_mov_b32 m0, %0" : "=&s"(keep) : "v"(gptr), "s"(ldsdst) : "memory");
}
#define WAIT_VM(n) asm volatile("s_waitcnt vmcnt(" #n ")" ::: "memory")
#define WAIT_BAR(n) asm volatile("s_waitcnt vmcnt(" #n ") lgkmcnt(0)\n\ts_barrier" ::: "memory")
#define BAR_L() asm volatile("s_waitcnt lgkmcnt(0)\n\ts_barrier" ::: "memory")
#define SBAR() __builtin_amdgcn_sched_barrier(0)
#define PIN(x) asm volatile("" : "+v"(x))
#define PRIO(n) __builtin_amdgcn_s_setprio(n)
#define EX(v) __builtin_amdgcn_exp2f(v)
DI u32x4 lds_r128(lds_ptr p) { return *(const LAS u32x4*)p; }
DI s16x4 lds_tr(lds_ptr p) { return __builtin_bit_cast(s16x4, __builtin_amdgcn_ds_read_tr16_b64_v4i16((LAS v4i16_t*)p)); }
DI bf16x8 as_frag(u32x4 v) { return __builtin_bit_cast(bf16x8, v); }
DI bf16x8 cat_frag(s16x4 a, s16x4 b) { return (bf16x8){a[0], a[1], a[2], a[3], b[0], b[1], b[2], b[3]}; }
DI float swap32_max(float m) { auto r = __builtin_amdgcn_permlane32_swap(__float_as_uint(m), __float_as_uint(m), false, false); return fmaxf(__uint_as_float(r[0]), __uint_as_float(r[1])); }
DI float sum_x16(float v) { auto r = __builtin_amdgcn_permlane16_swap(__float_as_uint(v), __float_as_uint(v), false, false); return __uint_as_float(r[0]) + __uint_as_float(r[1]); }
DI float sum_x32(float v) { auto r = __builtin_amdgcn_permlane32_swap(__float_as_uint(v), __float_as_uint(v), false, false); return __uint_as_float(r[0]) + __uint_as_float(r[1]); }
DI u32x4 ld128(rsrc_t r, unsigned off) { return __builtin_amdgcn_raw_buffer_load_b128(r, off, 0, 16); }
DI u32x2 ld64(rsrc_t r, unsigned off) { return __builtin_amdgcn_raw_buffer_load_b64(r, off, 0, 16); }
DI unsigned ld32(rsrc_t r, unsigned off) { return __builtin_amdgcn_raw_buffer_load_b32(r, off, 0, 16); }
DI void st128(rsrc_t r, unsigned off, u32x4 v) { __builtin_amdgcn_raw_buffer_store_b128(v, r, off, 0, 16); }
DI void st64(rsrc_t r, unsigned off, u32x2 v) { __builtin_amdgcn_raw_buffer_store_b64(v, r, off, 0, 16); }

DI void ald128(u32x4& v, u32x4 rsw, unsigned voff, unsigned soff) { asm volatile("buffer_load_dwordx4 %0, %1, %2, %3 offen sc1" : "=&v"(v) : "v"(voff), "s"(rsw), "s"(soff) : "memory"); }
template <int N> DI void ald_wait(u32x4& v) { asm volatile("s_waitcnt vmcnt(%1)" : "+v"(v) : "i"(N) : "memory"); }
struct Ctx {
    const uint16_t *x, *wqkv, *wo, *w1, *w3, *w2;
    char* ws; uint16_t* out;
    rsrc_t rs; u32x4 rsw; u64 tag; lds_ptr lds; unsigned lds0;
    int c, xcd, lb, tid, lane, w;
    mutable unsigned sc, sr;
};

#define STAMP(i)
#define STAMP_FLUSH()

DI u64 ld_flag(const char* p) { return __hip_atomic_load((const GAS u64*)p, __ATOMIC_RELAXED, __HIP_MEMORY_SCOPE_AGENT); }
DI void st_flag(char* p, u64 v) { __hip_atomic_store((GAS u64*)p, v, __ATOMIC_RELAXED, __HIP_MEMORY_SCOPE_AGENT); }
constexpr unsigned SPIN_CAP = 1u << 18;
DI void poll_flags(const Ctx& k, unsigned off) {
    for (unsigned spins = 0; spins < SPIN_CAP; ++spins) {
        if (__all(ld_flag(k.ws + off) == k.tag)) break;
        __builtin_amdgcn_s_sleep(1);
    }
}
DI void poll_flags2(const Ctx& k, unsigned off0, unsigned off1) {
    for (unsigned spins = 0; spins < SPIN_CAP; ++spins) {
        const u64 a = ld_flag(k.ws + off0), b = ld_flag(k.ws + off1);
        if (__all(a == k.tag && b == k.tag)) break;
        __builtin_amdgcn_s_sleep(1);
    }
}
DI void poll_all(const Ctx& k, unsigned arr) {
    for (unsigned spins = 0; spins < SPIN_CAP; ++spins) {
        const unsigned o = arr + k.lane * FLAG_STRIDE;
        const u64 a = ld_flag(k.ws + o), b = ld_flag(k.ws + o + 64 * FLAG_STRIDE), c = ld_flag(k.ws + o + 128 * FLAG_STRIDE), d = ld_flag(k.ws + o + 192 * FLAG_STRIDE);
        if (__all(a == k.tag && b == k.tag && c == k.tag && d == k.tag)) break;
        __builtin_amdgcn_s_sleep(1);
    }
}

constexpr int SU_LDS = 131072;
DI void startup_load(const Ctx& k) { glds4(k.ws + WS_EPOCH + (k.lane & 1) * 4, k.lds0 + SU_LDS + k.w * 256); }
DI void startup_tag(Ctx& k) {
    const u32x2 e = *(const LAS u32x2*)(k.lds + SU_LDS + k.w * 256);
    k.tag = (((u64)(unsigned)rfl(e.y) << 32) | (unsigned)rfl(e.x)) + 1;
}
DI void ln1_stats(const Ctx& k, int j) {
    u32x4 v[2][10];
#pragma unroll
    for (int p = 0; p < 2; ++p)
#pragma unroll
        for (int i = 0; i < 10; ++i) v[p][i] = *(const u32x4*)(k.x + (size_t)(128 * j + 16 * k.w + 8 * p + (k.lane >> 3)) * D + ((k.lane & 7) + 8 * i) * 8);
#pragma unroll
    for (int p = 0; p < 2; ++p) {
        float s = 0.f, q = 0.f;
#pragma unroll
        for (int i = 0; i < 10; ++i)
#pragma unroll
            for (int e = 0; e < 4; ++e) { const float a = bflo(v[p][i][e]), b = bfhi(v[p][i][e]); s += a + b; q = fmaf(a, a, fmaf(b, b, q)); }
#pragma unroll
        for (int o = 4; o >= 1; o >>= 1) { s += __shfl_xor(s, o); q += __shfl_xor(q, o); }
        const float mean = s * (1.0f / D), var = fmaxf(q * (1.0f / D) - mean * mean, 0.f);
        const float val = (k.lane & 1) ? 1.0f / sqrtf(var + LN_EPS) : mean;
        const int row = 128 * j + 16 * k.w + 8 * p + (k.lane >> 3);
        if ((k.lane & 7) < 2) st64(k.rs, WS_ST1 + row * 16 + (k.lane & 1) * 8, (u32x2){__float_as_uint(val), (unsigned)k.tag});
    }
}
DI void colsum_qkv(const Ctx& k, int cb) {
    float a[8] = {0, 0, 0, 0, 0, 0, 0, 0};
    u32x4 v[20];
#pragma unroll
    for (int i = 0; i < 20; ++i) v[i] = *(const u32x4*)(k.wqkv + (size_t)(k.w * 80 + i * 4 + (k.lane >> 4)) * NQKV + cb * 128 + (k.lane & 15) * 8);
#pragma unroll
    for (int i = 0; i < 20; ++i)
#pragma unroll
        for (int j = 0; j < 4; ++j) { a[2 * j] += bflo(v[i][j]); a[2 * j + 1] += bfhi(v[i][j]); }
    LAS float* red = (LAS float*)k.lds;
#pragma unroll
    for (int j = 0; j < 8; ++j) { a[j] += __shfl_xor(a[j], 16); a[j] += __shfl_xor(a[j], 32); }
    if (k.lane < 16) {
#pragma unroll
        for (int j = 0; j < 8; ++j) red[k.w * 128 + k.lane * 8 + j] = a[j];
    }
    BAR_L();
    if (k.tid < 128) {
        float t = 0.f;
#pragma unroll
        for (int i = 0; i < 8; ++i) t += red[i * 128 + k.tid];
        st64(k.rs, WS_CSQ + (cb * 128 + k.tid) * 8, (u32x2){__float_as_uint(t), (unsigned)k.tag});
    }
}

constexpr int PT_LDS = 131072;
DI int pt_off(int i) { return PT_LDS + (i < 3 ? i * 8192 : 24576 + (i - 3) * 4096); }
DI void pretile_load(const Ctx& k) {
    const int wv = k.w - 1;
#pragma unroll
    for (int i = 0; i < 5; ++i) {
        const int u = k.c + 256 * i;
        if (u < 540) {
            const int s = u % 20, fb = u / 20;
            const uint16_t* src = ((k.lane & 1) ? k.w3 : k.w1) + fb * 64 + (k.lane >> 1) * 2 + (size_t)32 * s * F;
            for (int r = wv; r < 32; r += 7) glds4(src + (size_t)r * F, k.lds0 + pt_off(i) + r * 256);
        } else {
            const int pc = (i & 1) ? wv - 3 : wv;
            if (pc >= 0 && pc < 4) {
                const bool is2 = u < 1080;
                const int uu = is2 ? u - 540 : u - 1080, s = uu / 10, nb = uu % 10, r = 8 * pc + (k.lane >> 3);
                glds16<false>((32 * s + r) * (D * 2) + nb * 128 + (k.lane & 7) * 16, is2 ? k.w2 : k.wo, k.lds0 + pt_off(i) + pc * 1024);
            }
        }
    }
}
struct PreTile { u32x4 v[5]; };
DI void pretile_read(const Ctx& k, PreTile& p) {
    const int g = k.lane >> 4, q = (k.lane >> 2) & 3, pp = k.lane & 3;
#pragma unroll
    for (int i = 0; i < 5; ++i) {
        const int u = k.c + 256 * i;
        const bool wide = u < 540;
        const int rs = wide ? 256 : 128, f = wide ? k.w : (k.w & 3);
        const lds_ptr a = k.lds + pt_off(i) + (8 * g + q) * rs + f * 32 + pp * 8;
        p.v[i] = __builtin_bit_cast(u32x4, cat_frag(lds_tr(a), lds_tr(a + 4 * rs)));
    }
}
DI void pretile_store(const Ctx& k, const PreTile& p) {
#pragma unroll
    for (int i = 0; i < 5; ++i) {
        const int u = k.c + 256 * i;
        if (u < 540) st128(k.rs, WS_W13 + (((u / 20) * 8 + k.w) * 20 + u % 20) * 1024 + k.lane * 16, p.v[i]);
        else if ((k.w >> 2) == (i & 1)) {
            const bool is2 = u < 1080;
            const int uu = is2 ? u - 540 : u - 1080, s = uu / 10, cfg = (uu % 10) * 4 + (k.w & 3), ct = cfg / 5, cf = cfg % 5;
            st128(k.rs, (is2 ? WS_W2 + ((ct * 54 + s) * 5 + cf) * 1024 : WS_WO + ((ct * 20 + s) * 5 + cf) * 1024) + k.lane * 16, p.v[i]);
        }
    }
}

constexpr int P1_SLOT = 32768;
struct P1Frag { bf16x8 a[4], b[2]; };
template <bool ISV> DI void p1_mma(f32x4 (&acc)[4][2], const P1Frag& f, int m, int n) { if (ISV) mfma16_acc(acc[m][n], f.a[m], f.b[n]); else mfma16_acc(acc[m][n], f.b[n], f.a[m]); }
template <bool ISV>
DI void p1_body(Ctx& k, int rb, int cb) {
    const int lane = k.lane, w = k.w, th = w >> 2, cg = w & 3, t = lane & 15, g = lane >> 4;
    unsigned voffA[2], voffW[2];
#pragma unroll
    for (int i = 0; i < 2; ++i) {
        const int ra = 8 * (2 * w + i) + (lane >> 3), ca = (lane & 7) ^ ((ra >> 1) & 7);
        voffA[i] = ra * (D * 2) + ca * 16;
        const int rw = 4 * (2 * w + i) + (lane >> 4), fw = (rw & 3) | (((rw >> 3) & 1) << 2), cw = (lane & 15) ^ (fw << 1);
        voffW[i] = rw * (NQKV * 2) + cw * 16;
    }
    const char* gA = (const char*)k.x + (size_t)rb * 128 * D * 2;
    const char* gW = (const char*)k.wqkv + cb * 256;
    auto stage = [&](int kt) {
        const unsigned sl = k.lds0 + (kt & 3) * P1_SLOT + w * 2048;
#pragma unroll
        for (int i = 0; i < 2; ++i) glds16<false>(voffA[i], gA + kt * 128, sl + i * 1024);
#pragma unroll
        for (int i = 0; i < 2; ++i) glds16<false>(voffW[i], gW + (size_t)kt * 64 * NQKV * 2, sl + 16384 + i * 1024);
    };
    stage(0); stage(1); stage(2); stage(3);
    int aoff[2], woff[2];
    aoff[0] = (th * 64 + t) * 128 + ((g ^ (t >> 1)) << 4); aoff[1] = aoff[0] ^ 64;
    const int q4 = (lane >> 2) & 3, p = lane & 3, fw = q4 | ((g & 1) << 2);
#pragma unroll
    for (int n = 0; n < 2; ++n) woff[n] = 16384 + (8 * g + q4) * 256 + (((2 * cg + n) ^ fw) << 5) + p * 8;
    f32x4 acc[4][2];
#pragma unroll
    for (int m = 0; m < 4; ++m) { acc[m][0] = (f32x4){0, 0, 0, 0}; acc[m][1] = (f32x4){0, 0, 0, 0}; PIN(acc[m][0]); PIN(acc[m][1]); }
    mfma_fence();
    P1Frag f0, f1;
#define P1_RA(F_, sl, ks, m) F_.a[m] = as_frag(lds_r128(sl + aoff[ks] + (m) * 2048))
#define P1_RB(F_, sl, ks, n) F_.b[n] = cat_frag(lds_tr(sl + woff[n] + (ks) * 8192), lds_tr(sl + woff[n] + (ks) * 8192 + 1024))
#define P1_STEP(RD, FN, sl, ks, FC) do { \
        if (RD) P1_RB(FN, sl, ks, 0); p1_mma<ISV>(acc, FC, 0, 0); SBAR(); \
        if (RD) P1_RA(FN, sl, ks, 0); p1_mma<ISV>(acc, FC, 0, 1); SBAR(); \
        if (RD) P1_RA(FN, sl, ks, 1); p1_mma<ISV>(acc, FC, 1, 0); SBAR(); \
        if (RD) P1_RB(FN, sl, ks, 1); p1_mma<ISV>(acc, FC, 1, 1); SBAR(); \
        if (RD) P1_RA(FN, sl, ks, 2); p1_mma<ISV>(acc, FC, 2, 0); SBAR(); \
        if (RD) P1_RA(FN, sl, ks, 3); p1_mma<ISV>(acc, FC, 2, 1); SBAR(); \
        p1_mma<ISV>(acc, FC, 3, 0); p1_mma<ISV>(acc, FC, 3, 1); SBAR(); } while (0)
    WAIT_BAR(12);
    STAMP(1);
    startup_tag(k);
    { const lds_ptr sl = k.lds; P1_RB(f0, sl, 0, 0); P1_RB(f0, sl, 0, 1); P1_RA(f0, sl, 0, 0); P1_RA(f0, sl, 0, 1); P1_RA(f0, sl, 0, 2); P1_RA(f0, sl, 0, 3); SBAR(); }
    u32x2 gr = {0, 0};
    PRIO(1);
    const unsigned groff = k.tid < 256 ? WS_ST1 + (rb * 128) * 16 + k.tid * 8 : WS_CSQ + (cb * 128 + (k.tid & 127)) * 8;
#pragma unroll
    for (int kt = 0; kt < 10; ++kt) {
        const lds_ptr sl = k.lds + (kt & 3) * P1_SLOT, sn = k.lds + ((kt + 1) & 3) * P1_SLOT;
        P1_STEP(true, f1, sl, 1, f0);
        if (kt < 9) {
            if (kt < 7) WAIT_BAR(8); else if (kt == 7) WAIT_BAR(4); else WAIT_BAR(0);
            if (kt == 6) gr = ld64(k.rs, groff);
            if (kt + 4 < 10) stage(kt + 4);
            SBAR();
        }
        P1_STEP(kt < 9, f0, sn, 0, f1);
    }
#undef P1_RA
#undef P1_RB
#undef P1_STEP
    PRIO(0);
    mfma_fence();
    STAMP(2);
    for (unsigned spins = 0; spins < SPIN_CAP; ++spins) {
        if (__all(gr.y == (unsigned)k.tag)) break;
        gr = ld64(k.rs, groff);
    }
    LAS float* sst = (LAS float*)k.lds;
    if (k.tid < 384) sst[k.tid] = __uint_as_float(gr.x);
    BAR_L();
    const LAS f32x2* st = (const LAS f32x2*)sst + th * 64;
    const LAS float* cs = sst + 256 + cg * 32;
    if (!ISV) {
        const float osc = cb < 5 ? QSCALE : 1.0f;
        const unsigned img = cb < 5 ? WS_Q : WS_K;
        const int cbs = cb % 5;
        unsigned dst[2]; f32x4 c4[2];
#pragma unroll
        for (int n = 0; n < 2; ++n) {
            const int cgi = cbs * 32 + cg * 8 + n * 4 + g, hd = cgi / 5, gi = cgi - 5 * hd;
            const int blk = gi == 4 ? 4 : ((gi & 1) << 1) | (gi >> 1);
            dst[n] = img + (rb * 4 + th * 2) * QK_TILE + hd * 1280 + blk * 256 + t * 8;
            c4[n] = *(const LAS f32x4*)(cs + n * 16 + 4 * g);
        }
#pragma unroll
        for (int m = 0; m < 4; ++m) {
            const f32x2 sm = st[m * 16 + t];
            const float r = sm.y * osc;
#pragma unroll
            for (int n = 0; n < 2; ++n) {
                const f32x4 y = (acc[m][n] - c4[n] * sm.x) * r;
                const unsigned o = dst[n] + (m >> 1) * QK_TILE + (m & 1) * 128;
                st64(k.rs, o, (u32x2){cvtpk(y[0], y[1]), cvtpk(y[2], y[3])});
            }
        }
    } else {
        const int pos = ((g & 1) << 1) | (g >> 1);
        const unsigned dst = WS_V + (rb * 8 + th * 4) * 20480 + ((cb - 10) * 128 + cg * 32 + t) * 32 + pos * 8;
        const float c1[2] = {cs[t], cs[16 + t]};
#pragma unroll
        for (int m = 0; m < 4; ++m) {
            f32x2 sm[4];
#pragma unroll
            for (int i = 0; i < 4; ++i) sm[i] = st[m * 16 + 4 * g + i];
#pragma unroll
            for (int n = 0; n < 2; ++n) {
                float y[4];
#pragma unroll
                for (int i = 0; i < 4; ++i) y[i] = (acc[m][n][i] - c1[n] * sm[i].x) * sm[i].y;
                st64(k.rs, dst + m * 20480 + n * 512, (u32x2){cvtpk(y[0], y[1]), cvtpk(y[2], y[3])});
            }
        }
    }
}

constexpr int AT_KT = 8192, AT_KB = 10240, AT_HALF = AT_KB + 10240, AT_BUF = 2 * AT_HALF;
constexpr int AT_ONES = AT_BUF, AT_ONES_BYTES = 10752, AT_KC = AT_ONES + AT_ONES_BYTES, AT_KC_BYTES = 2048, AT_FLAG = AT_KC + AT_KC_BYTES, WO_LDS = 61440, AT_RED = 0;
DI int at_slot(int i) { return i == 0 ? 0 : (i == 1 ? 61440 : 61440 + AT_BUF); }
struct AttnCtx {
    unsigned voff[5]; const char* gsrc; unsigned ldst, sstride;
    u32x4 qf0[2]; u32x2 qf1[2];
    int kofs, kt, vlane; bool vdata, hi;
};
DI void attn_stage(const AttnCtx& c, int sidx, int slot) {
#pragma unroll
    for (int i = 0; i < 5; ++i) glds16<true>(c.voff[i], c.gsrc + sidx * c.sstride, c.ldst + at_slot(slot) + i * 1024);
}
DI void attn_setup(const Ctx& k, AttnCtx& c, int h) {
    const int lane = k.lane, w = k.w, kh = w >> 2, r = lane & 31, hi = lane >> 5;
    const bool stV = (w >> 1) & 1;
#pragma unroll
    for (int i = 0; i < 5; ++i) {
        const int pc = 5 * (w & 1) + i;
        if (!stV) c.voff[i] = pc < 8 ? pc * QK_TILE + lane * 16 : ((pc - 8) * 4 + (lane >> 4)) * QK_TILE + 1024 + (lane & 15) * 16;
        else { const int ci = 64 * pc + lane, st = ci / 40, wi = ci % 40; c.voff[i] = st * 20480 + ((wi % 20) * 2 + wi / 20) * 16; }
    }
    c.gsrc = k.ws + (stV ? WS_V + h * 640 + kh * 64 * 20480 : WS_K + h * 1280 + kh * 32 * QK_TILE);
    c.sstride = stV ? 16 * 20480 : 8 * QK_TILE;
    c.ldst = k.lds0 + kh * AT_HALF + (stV ? AT_KB : 0) + (w & 1) * 5120;
    c.kofs = kh * AT_HALF + hi * 256 + r * 8;
    c.hi = hi;
    c.kt = hi ? AT_KC + r * 8 : kh * AT_HALF + AT_KT + r * 8;
    const int rr = r >= 28 ? r - 12 : r;
    c.vdata = rr < 20;
    c.vlane = c.vdata ? kh * AT_HALF + AT_KB + hi * 320 + rr * 16 : AT_ONES + 128;
    for (int i = k.tid; i < AT_ONES_BYTES / 4; i += 512) ((LAS unsigned*)(k.lds + AT_ONES))[i] = 0x3F803F80u;
    ((LAS unsigned*)(k.lds + AT_KC))[k.tid] = (k.tid & 1) ? 0u : 0x00003F80u;
    if (k.tid == 0) *(LAS unsigned*)(k.lds + AT_FLAG) = 0;
}
DI void attn_first_loads(const Ctx& k, AttnCtx& c, int h, int qb) {
    const int qg = k.w & 3, r = k.lane & 31, hi = k.lane >> 5;
    attn_stage(c, 0, 0);
#pragma unroll
    for (int i = 0; i < 2; ++i) {
        const unsigned qp = WS_Q + (qb * 8 + qg * 2 + i) * QK_TILE + h * 1280 + r * 8;
        const u32x2 a = ld64(k.rs, qp + hi * 256), b = ld64(k.rs, qp + hi * 256 + 512), e = ld64(k.rs, qp + 1024);
        c.qf0[i] = (u32x4){a.x, a.y, b.x, b.y};
        c.qf1[i] = hi ? (u32x2){0, 0} : e;
    }
    attn_stage(c, 1, 1);
}
DI bf16x8 kfrag0(lds_ptr p) { const u32x2 a = *(const LAS u32x2*)p, b = *(const LAS u32x2*)(p + 512); return as_frag((u32x4){a.x, a.y, b.x, b.y}); }
DI s16x4 kfrag1(lds_ptr p) { return __builtin_bit_cast(s16x4, *(const LAS u32x2*)p); }
DI s16x4 as_frag4(u32x2 v) { return __builtin_bit_cast(s16x4, v); }
DI f32x16 mfma32x8(s16x4 a, s16x4 b, f32x16 c) { return __builtin_amdgcn_mfma_f32_32x32x8bf16_1k(a, b, c, 0, 0, 0); }
DI void wo_prefetch(const Ctx& k, int ct) {
    for (int pc = k.w; pc < 100; pc += 8) glds16<true>(k.lane * 16, k.ws + WS_WO + ct * 102400 + pc * 1024, k.lds0 + WO_LDS + pc * 1024);
}
DI void attn_fast(const Ctx& k, AttnCtx& c, f32x16& oA, f32x16& oB, int ct3, int f4) {
    const lds_ptr lds = k.lds;
    const int hi = k.lane >> 5, kh = k.w >> 2;
    {
        const bf16x8 k0 = kfrag0(lds + c.kofs - kh * AT_HALF); const s16x4 k1 = kfrag1(lds + (hi ? c.kt : c.kt - kh * AT_HALF));
#pragma unroll
        for (int i = 0; i < 2; ++i) {
            f32x16 s = mfma32(k0, as_frag(c.qf0[i]), (f32x16){});
            s = mfma32x8(k1, as_frag4(c.qf1[i]), s);
            float m = s[0];
#pragma unroll
            for (int j = 1; j < 16; ++j) m = fmaxf(m, s[j]);
            m = swap32_max(m);
            if (hi) c.qf1[i].x = cvtpk(-m, 0.f) & 0xffffu;
        }
    }
    const bf16x8 qA0 = as_frag(c.qf0[0]), qB0 = as_frag(c.qf0[1]); const s16x4 qA1 = as_frag4(c.qf1[0]), qB1 = as_frag4(c.qf1[1]);
    f32x16 sA, sB, nA, nB;
    oA = (f32x16){}; oB = (f32x16){};
    bf16x8 kf0 = kfrag0(lds + c.kofs); s16x4 kf1 = kfrag1(lds + c.kt);
    sA = mfma32(kf0, qA0, (f32x16){}); sB = mfma32(kf0, qB0, (f32x16){});
    sA = mfma32x8(kf1, qA1, sA); sB = mfma32x8(kf1, qB1, sB);
    kf0 = kfrag0(lds + c.kofs + 1024); kf1 = kfrag1(lds + c.kt + 256);
    u32x4 pA0, pA1, pB0, pB1 = {0, 0, 0, 0};
    bf16x8 vf0, vf1 = {0, 0, 0, 0, 0, 0, 0, 0};
    unsigned warm = 0;
    u32x2 fa = {0, 0}, fb = fa, fc = fa, fd = fa;
#define GAP(MF, X, i, P, j) do { MF; SBAR(); X[i] = EX(X[i]); X[i + 1] = EX(X[i + 1]); X[i + 2] = EX(X[i + 2]); X[i + 3] = EX(X[i + 3]); \
        P[j] = cvtpk(X[i], X[i + 1]); P[j + 1] = cvtpk(X[i + 2], X[i + 3]); PIN(P); SBAR(); } while (0)
#define STEP(SA, SB, NA, NB, VP, KP, KT) do { \
        GAP(NA = mfma32(kf0, qA0, (f32x16){}), SA, 0, pA0, 0); vf0 = as_frag(lds_r128(VP)); SBAR(); \
        GAP(oB = mfma32(vf1, as_frag(pB1), oB), SA, 4, pA0, 2); vf1 = as_frag(lds_r128((VP) + 640)); SBAR(); \
        GAP(NB = mfma32(kf0, qB0, (f32x16){}), SA, 8, pA1, 0); \
        GAP(NA = mfma32x8(kf1, qA1, NA), SA, 12, pA1, 2); \
        GAP(NB = mfma32x8(kf1, qB1, NB), SB, 0, pB0, 0); kf0 = kfrag0(KP); kf1 = kfrag1(KT); SBAR(); \
        GAP(oA = mfma32(vf0, as_frag(pA0), oA), SB, 4, pB0, 2); \
        GAP(oA = mfma32(vf1, as_frag(pA1), oA), SB, 8, pB1, 0); \
        GAP(oB = mfma32(vf0, as_frag(pB0), oB), SB, 12, pB1, 2); \
    } while (0)
    int slot = 0;
    PRIO(1);
    for (int sidx = 0; sidx < 4; ++sidx) {
        const int nslot = slot == 2 ? 0 : slot + 1;
        const int sb = at_slot(slot), sn = at_slot(nslot);
        const lds_ptr vb = lds + (c.vdata ? c.vlane + sb : c.vlane);
        lds_ptr k0 = lds + c.kofs + sb, k0n = lds + c.kofs + sn, k1 = lds + (hi ? c.kt : c.kt + sb), k1n = lds + (hi ? c.kt : c.kt + sn);
        PIN(k0); PIN(k0n); PIN(k1); PIN(k1n);
        STEP(sA, sB, nA, nB, vb, k0 + 2 * 1024, k1 + 2 * 256);
        STEP(nA, nB, sA, sB, vb + 1280, k0 + 3 * 1024, k1 + 3 * 256);
        STEP(sA, sB, nA, nB, vb + 2 * 1280, k0 + 4 * 1024, k1 + 4 * 256);
        if (sidx == 1 && k.w == 0) {
            const unsigned o = WS_FP + k.lane * FLAG_STRIDE;
            fa = ld64(k.rs, o); fb = ld64(k.rs, o + 64 * FLAG_STRIDE); fc = ld64(k.rs, o + 128 * FLAG_STRIDE); fd = ld64(k.rs, o + 192 * FLAG_STRIDE);
        }
        STEP(nA, nB, sA, sB, vb + 3 * 1280, k0 + 5 * 1024, k1 + 5 * 256);
        STEP(sA, sB, nA, nB, vb + 4 * 1280, k0 + 6 * 1024, k1 + 6 * 256);
        STEP(nA, nB, sA, sB, vb + 5 * 1280, k0 + 7 * 1024, k1 + 7 * 256);
        if (sidx == 1 && k.w == 0) {
            const unsigned tl = (unsigned)k.tag, th = (unsigned)(k.tag >> 32);
            if (!__all(fa.x == tl && fa.y == th && fb.x == tl && fb.y == th && fc.x == tl && fc.y == th && fd.x == tl && fd.y == th)) poll_all(k, WS_FP);
        }
        if (sidx == 0) STAMP(20); else if (sidx == 1) STAMP(22); else if (sidx == 2) STAMP(24); else STAMP(26);
        WAIT_BAR(0);
        if (sidx == 0) STAMP(21); else if (sidx == 1) STAMP(23); else if (sidx == 2) STAMP(25); else STAMP(27);
        if (sidx < 2) attn_stage(c, sidx + 2, nslot == 2 ? 0 : nslot + 1);
        if (sidx == 0 && k.tid == 0) st_flag(k.ws + WS_FP + k.c * FLAG_STRIDE, k.tag);
        if (sidx == 1) {
            const unsigned o = WS_W13 + f4 * 20480 + (k.lb >> 4) * 71680 + (k.w * 64 + k.lane) * 128;
            warm = ld32(k.rs, o);
            if (k.w == 0 && k.lane < 48) warm ^= ld32(k.rs, o + 65536);
        }
        if (sidx == 3) wo_prefetch(k, ct3);
        STEP(sA, sB, nA, nB, vb + 6 * 1280, k0n, k1n);
        STEP(nA, nB, sA, sB, vb + 7 * 1280, k0n + 1024, k1n + 256);
        slot = nslot;
    }
    PRIO(0);
    oB = mfma32(vf1, as_frag(pB1), oB);
    asm volatile("" :: "v"(warm));
#undef GAP
#undef STEP
}
DI void attn_safe(const Ctx& k, AttnCtx& c, f32x16 (&o)[2], float (&mrun)[2]) {
    const lds_ptr lds = k.lds;
    attn_stage(c, 0, 0);
    if (k.lane >> 5) { c.qf1[0].x = 0; c.qf1[1].x = 0; }
    o[0] = (f32x16){}; o[1] = (f32x16){};
    mrun[0] = -INFINITY; mrun[1] = -INFINITY;
    for (int sidx = 0; sidx < 4; ++sidx) {
        const int sb = at_slot(sidx & 1);
        WAIT_BAR(0);
        if (sidx < 3) attn_stage(c, sidx + 1, (sidx + 1) & 1);
        const lds_ptr vb = lds + (c.vdata ? c.vlane + sb : c.vlane);
        for (int kt = 0; kt < 8; ++kt) {
            const bf16x8 k0 = kfrag0(lds + c.kofs + sb + kt * 1024); const s16x4 k1 = kfrag1(lds + (c.hi ? c.kt : c.kt + sb) + kt * 256);
            const bf16x8 v0 = as_frag(lds_r128(vb + kt * 1280)), v1 = as_frag(lds_r128(vb + kt * 1280 + 640));
#pragma unroll
            for (int i = 0; i < 2; ++i) {
                f32x16 s = mfma32(k0, as_frag(c.qf0[i]), (f32x16){});
                s = mfma32x8(k1, as_frag4(c.qf1[i]), s);
                float m = s[0];
#pragma unroll
                for (int j = 1; j < 16; ++j) m = fmaxf(m, s[j]);
                m = fmaxf(swap32_max(m), mrun[i]);
                const float alpha = EX(mrun[i] - m);
                mrun[i] = m;
                unsigned pk[8];
#pragma unroll
                for (int j = 0; j < 16; ++j) o[i][j] *= alpha;
#pragma unroll
                for (int j = 0; j < 8; ++j) pk[j] = cvtpk(EX(s[2 * j] - m), EX(s[2 * j + 1] - m));
                o[i] = mfma32(v0, as_frag((u32x4){pk[0], pk[1], pk[2], pk[3]}), o[i]);
                o[i] = mfma32(v1, as_frag((u32x4){pk[4], pk[5], pk[6], pk[7]}), o[i]);
            }
        }
    }
}
DI void p2_phase(const Ctx& k, AttnCtx& c, int h, int qb, int ct3, int f4) {
    const int lane = k.lane, qg = k.w & 3, kh = k.w >> 2, r = lane & 31, hi = lane >> 5;
    const lds_ptr lds = k.lds;
    attn_first_loads(k, c, h, qb);
    PreTile pt;
    pretile_read(k, pt);
    WAIT_BAR(5);
    STAMP(6);
    pretile_store(k, pt);
    f32x16 o[2];
    attn_fast(k, c, o[0], o[1], ct3, f4);
    STAMP(7);
    LAS unsigned* flag = (LAS unsigned*)(lds + AT_FLAG);
    if (__any(hi && !(o[0][8] <= 5.764607523034235e17f && o[1][8] <= 5.764607523034235e17f)) && lane == 0) *flag = 1;
    BAR_L();
    const lds_ptr red = lds + AT_RED + qg * 8192 + lane * 16;
    auto put = [&](const f32x16& v, int slot) {
#pragma unroll
        for (int u = 0; u < 3; ++u) *(LAS f32x4*)(red + slot * 4096 + u * 1024) = (f32x4){v[4 * u], v[4 * u + 1], v[4 * u + 2], v[4 * u + 3]};
    };
    auto get = [&](int slot, int u) { return *(const LAS f32x4*)(red + slot * 4096 + u * 1024); };
    f32x4 of[3];
    if (*flag == 0) {
        if (kh) put(o[0], 0); else put(o[1], 1);
        BAR_L();
#pragma unroll
        for (int u = 0; u < 3; ++u) {
            const f32x4 p = get(kh, u);
#pragma unroll
            for (int j = 0; j < 4; ++j) of[u][j] = p[j] + (kh ? o[1][4 * u + j] : o[0][4 * u + j]);
        }
    } else {
        float mref[2];
        WAIT_BAR(0);
        attn_safe(k, c, o, mref);
        BAR_L();
        if (kh) put(o[0], 0); else put(o[1], 1);
        *(LAS float*)(red + (kh ? 0 : 4096) + 3072) = kh ? mref[0] : mref[1];
        BAR_L();
        const float m0 = kh ? mref[1] : mref[0], m1 = *(const LAS float*)(red + kh * 4096 + 3072), m = fmaxf(m0, m1);
        const float a0 = EX(m0 - m), a1 = EX(m1 - m);
#pragma unroll
        for (int u = 0; u < 3; ++u) {
            const f32x4 p = get(kh, u);
#pragma unroll
            for (int j = 0; j < 4; ++j) of[u][j] = p[j] * a1 + (kh ? o[1][4 * u + j] : o[0][4 * u + j]) * a0;
        }
        BAR_L();
        wo_prefetch(k, ct3);
    }
    {
        auto lr = __builtin_amdgcn_permlane32_swap(__float_as_uint(of[2][0]), __float_as_uint(of[2][0]), false, false);
        const float inv = 1.0f / __uint_as_float(lr[1]);
        const int tok = qb * 256 + qg * 64 + kh * 32 + r;
        const unsigned base = WS_O + (tok >> 4) * OH_TILE + (tok & 15) * 16;
        unsigned pk[3][2];
#pragma unroll
        for (int u = 0; u < 3; ++u) { pk[u][0] = cvtpk(of[u][0] * inv, of[u][1] * inv); pk[u][1] = cvtpk(of[u][2] * inv, of[u][3] * inv); }
        auto oaddr = [&](int col) { return base + (col >> 5) * 1024 + ((col >> 3) & 3) * 256 + (col & 7) * 2; };
        if ((h & 1) == 0) {
            auto s0 = __builtin_amdgcn_permlane32_swap(pk[0][0], pk[1][0], false, false);
            auto s1 = __builtin_amdgcn_permlane32_swap(pk[0][1], pk[1][1], false, false);
            st128(k.rs, oaddr(h * 20 + 8 * hi), (u32x4){s0[0], s1[0], s0[1], s1[1]});
            if (!hi) st64(k.rs, oaddr(h * 20 + 16), (u32x2){pk[2][0], pk[2][1]});
        } else {
            auto s0 = __builtin_amdgcn_permlane32_swap(pk[0][0], pk[2][0], false, false);
            auto s1 = __builtin_amdgcn_permlane32_swap(pk[0][1], pk[2][1], false, false);
            st128(k.rs, oaddr(h * 20 + 4 + 8 * hi), hi ? (u32x4){pk[1][0], pk[1][1], s0[0], s1[0]} : (u32x4){s0[1], s1[1], pk[1][0], pk[1][1]});
            if (!hi) st64(k.rs, oaddr(h * 20), (u32x2){s0[0], s1[0]});
        }
    }
}

DI int own_cf(int kh, int u) { return kh ? (u < 2 ? 3 + u : 4) : u; }
constexpr int RED_LDS = 143360;
struct P3Pre { u32x2 xv[5]; };
DI void p3_pre(const Ctx& k, int rb64, int ct, P3Pre& p) {
    const int tt = k.w & 3, t = k.lane & 15, g = k.lane >> 4;
    if (k.w < 4) {
#pragma unroll
        for (int cf = 0; cf < 5; ++cf) p.xv[cf] = *(const u32x2*)(k.x + (size_t)(rb64 * 64 + tt * 16 + t) * D + ct * 80 + cf * 16 + 4 * g);
    }
}
DI void w13_prefetch(const Ctx& k, int f0, int f);
DI void p3_phase(const Ctx& k, int rb64, int ct, int f0, const P3Pre& pre, u32x2 (&hv)[3]) {
    const int lane = k.lane, tt = k.w & 3, kh = k.w >> 2, t = lane & 15, g = lane >> 4;
    const unsigned ap = WS_O + (rb64 * 4 + tt) * OH_TILE + kh * 10240 + lane * 16;
    const lds_ptr wl = k.lds + WO_LDS + kh * 51200 + lane * 16;
    constexpr int WIN = 5;
    u32x4 a[10];
#pragma unroll
    for (int i = 0; i < WIN; ++i) a[i] = ld128(k.rs, ap + i * 1024);
    bf16x8 wf[2][5];
#pragma unroll
    for (int cf = 0; cf < 5; ++cf) wf[0][cf] = as_frag(lds_r128(wl + cf * 1024));
    f32x4 acc[5];
#pragma unroll
    for (int cf = 0; cf < 5; ++cf) { acc[cf] = (f32x4){0, 0, 0, 0}; PIN(acc[cf]); }
    mfma_fence();
    PRIO(1);
#pragma unroll
    for (int i = 0; i < 10; ++i) {
        if (i + WIN < 10) { a[i + WIN] = ld128(k.rs, ap + (i + WIN) * 1024); SBAR(); }
#pragma unroll
        for (int cf = 0; cf < 5; ++cf) {
            if (i + 1 < 10) wf[(i + 1) & 1][cf] = as_frag(lds_r128(wl + (i + 1) * 5120 + cf * 1024));
            mfma16_acc(acc[cf], wf[i & 1][cf], as_frag(a[i]));
            SBAR();
        }
    }
    PRIO(0);
    mfma_fence();
    STAMP(10);
    BAR_L();
    const lds_ptr red = k.lds + RED_LDS + tt * 5120 + lane * 16;
    if (kh) {
#pragma unroll
        for (int cf = 0; cf < 5; ++cf) *(LAS f32x4*)(red + cf * 1024) = acc[cf];
        BAR_L();
        w13_prefetch(k, f0, k.w - 1);
    } else {
        BAR_L();
        const int tok = rb64 * 64 + tt * 16 + t;
        float s = 0.f, q = 0.f;
        u32x2 hq[5];
#pragma unroll
        for (int cf = 0; cf < 5; ++cf) {
            const f32x4 v = acc[cf] + *(const LAS f32x4*)(red + cf * 1024);
            hq[cf] = (u32x2){cvtpk(v[0] + bflo(pre.xv[cf].x), v[1] + bfhi(pre.xv[cf].x)), cvtpk(v[2] + bflo(pre.xv[cf].y), v[3] + bfhi(pre.xv[cf].y))};
            const int col = ct * 80 + cf * 16 + 4 * g;
            st64(k.rs, WS_H + (tok >> 4) * OH_TILE + (col >> 5) * 1024 + ((col >> 3) & 3) * 256 + t * 16 + (col & 7) * 2, hq[cf]);
            const float h0 = bflo(hq[cf].x), h1 = bfhi(hq[cf].x), h2 = bflo(hq[cf].y), h3 = bfhi(hq[cf].y);
            s += (h0 + h1) + (h2 + h3); q += (h0 * h0 + h1 * h1) + (h2 * h2 + h3 * h3);
        }
        s = sum_x32(sum_x16(s)); q = sum_x32(sum_x16(q));
        if (g == 0) st64(k.rs, WS_PST + (tok >> 4) * 2048 + (ct * 2) * 128 + t * 8, (u32x2){__float_as_uint(s), __float_as_uint(q)});
        if (g == 1) st64(k.rs, WS_PST + (tok >> 4) * 2048 + (ct * 2 + 1) * 128 + t * 8, (u32x2){0u, 0u});
        hv[0] = hq[0]; hv[1] = hq[1]; hv[2] = hq[2];
        const lds_ptr stash = k.lds + RED_LDS + tt * 5120 + lane * 8;
        *(LAS u32x2*)(stash + 3072) = hq[3]; *(LAS u32x2*)(stash + 4096) = hq[4];
    }
}
DI void pair_reduce(lds_ptr red0, int tt, int kh, int lane, const f32x4 (&acc)[5], f32x4 (&own)[3]) {
    const lds_ptr red = red0 + tt * 5120 + lane * 16;
    if (kh == 0) { *(LAS f32x4*)(red + 3072) = acc[3]; *(LAS f32x4*)(red + 4096) = acc[4]; }
    else { *(LAS f32x4*)(red) = acc[0]; *(LAS f32x4*)(red + 1024) = acc[1]; *(LAS f32x4*)(red + 2048) = acc[2]; }
    BAR_L();
    if (kh == 0) {
#pragma unroll
        for (int u = 0; u < 3; ++u) own[u] = acc[u] + *(const LAS f32x4*)(red + u * 1024);
    } else {
        own[0] = acc[3] + *(const LAS f32x4*)(red + 3072); own[1] = acc[4] + *(const LAS f32x4*)(red + 4096); own[2] = own[1];
    }
}
template <int N, int NP>
DI void w2_stage(const Ctx& k, int ct, int b, int s0, int w0) {
    const int nw = 8 - w0;
#pragma unroll
    for (int j = 0; j < NP; ++j) {
        int q = k.w - w0 + nw * j;
        if (q >= 10 * N) q -= nw;
        const int hf = q >= 5 * N, rem = q - hf * 5 * N, n = rem / 5;
        glds16<true>(k.lane * 16, k.ws + WS_W2 + (ct * 54 + hf * 27 + s0) * 5120 + rem * 1024, k.lds0 + n * 20480 + b * 10240 + hf * 5120 + (rem - 5 * n) * 1024);
    }
}
DI void p5_phase(const Ctx& k, int rb64, int ct, u32x2 (&hv)[3]) {
    const int lane = k.lane, tt = k.w & 3, kh = k.w >> 2, t = lane & 15, g = lane >> 4;
    const unsigned ap = WS_HH + (rb64 * 4 + tt) * HH_TILE + kh * 27 * 1024 + lane * 16;
    constexpr int WIN = 6;
    u32x4 a[27];
#pragma unroll
    for (int i = 0; i < WIN; ++i) ald128(a[i], k.rsw, ap, i * 1024);
    if (kh) {
        const lds_ptr stash = k.lds + RED_LDS + tt * 5120 + lane * 8;
        hv[0] = *(const LAS u32x2*)(stash + 3072); hv[1] = *(const LAS u32x2*)(stash + 4096); hv[2] = hv[1];
    }
    lds_ptr wb[3] = {k.lds + lane * 16 + kh * 5120, k.lds + lane * 16 + kh * 5120 + 61440, k.lds + lane * 16 + kh * 5120 + 122880};
    PIN(wb[0]); PIN(wb[1]); PIN(wb[2]);
    auto wread = [&](int n, int cf) {
        const int m = n % 7;
        return as_frag(lds_r128(wb[m / 3] + (m % 3) * 20480 + ((n / 7) & 1) * 10240 + cf * 1024));
    };
    bf16x8 wf[2][5];
#pragma unroll
    for (int cf = 0; cf < 5; ++cf) wf[0][cf] = wread(0, cf);
    f32x4 acc[5];
#pragma unroll
    for (int cf = 0; cf < 5; ++cf) { acc[cf] = (f32x4){0, 0, 0, 0}; PIN(acc[cf]); }
    mfma_fence();
    PRIO(1);
#define P5_STEP(i, NW) do { \
        if ((i) == 7) { BAR_L(); w2_stage<7, 9>(k, ct, 0, 14, 0); } \
        if ((i) == 13) { WAIT_BAR(6); w2_stage<6, 8>(k, ct, 1, 21, 0); } \
        if ((i) == 20) { WAIT_BAR(6); } \
        if ((i) + WIN < 27) ald128(a[(i) + WIN], k.rsw, ap, ((i) + WIN) * 1024); \
        ald_wait<NW>(a[i]); SBAR(); \
        _Pragma("unroll") for (int cf = 0; cf < 5; ++cf) { \
            if ((i) + 1 < 27) wf[((i) + 1) & 1][cf] = wread((i) + 1, cf); \
            mfma16_acc(acc[cf], wf[(i) & 1][cf], as_frag(a[i])); SBAR(); } } while (0)
    P5_STEP(0, 6); P5_STEP(1, 6); P5_STEP(2, 6); P5_STEP(3, 6); P5_STEP(4, 6); P5_STEP(5, 6); P5_STEP(6, 6);
    P5_STEP(7, 15); P5_STEP(8, 15); P5_STEP(9, 15); P5_STEP(10, 15); P5_STEP(11, 15); P5_STEP(12, 15);
    P5_STEP(13, 14); P5_STEP(14, 14); P5_STEP(15, 14); P5_STEP(16, 14); P5_STEP(17, 14); P5_STEP(18, 14); P5_STEP(19, 6);
    P5_STEP(20, 6); P5_STEP(21, 5); P5_STEP(22, 4); P5_STEP(23, 3); P5_STEP(24, 2); P5_STEP(25, 1); P5_STEP(26, 0);
#undef P5_STEP
    PRIO(0);
    mfma_fence();
    STAMP(16);
    f32x4 own[3];
    pair_reduce(k.lds + RED_LDS, tt, kh, lane, acc, own);
    const int tok = rb64 * 64 + tt * 16 + t;
#pragma unroll
    for (int u = 0; u < 3; ++u) {
        if (u < 2 || kh == 0) {
            const int col = ct * 80 + own_cf(kh, u) * 16 + 4 * g;
            *(u32x2*)(k.out + (size_t)tok * D + col) = (u32x2){cvtpk(own[u][0] + bflo(hv[u].x), own[u][1] + bfhi(hv[u].x)), cvtpk(own[u][2] + bflo(hv[u].y), own[u][3] + bfhi(hv[u].y))};
        }
    }
}

constexpr int P4_CS = 143360;
DI void w13_prefetch(const Ctx& k, int f0, int f) {
    const int p0 = f * 20;
#pragma unroll 4
    for (int j = 0; j < 20; ++j) glds16<true>(k.lane * 16, k.ws + WS_W13 + f0 * 20480 + (p0 + j) * 1024, k.lds0 + (p0 + j) * 1024);
}
DI void p4_colsum(const Ctx& k, int f) {
    const int t = k.lane & 15, g = k.lane >> 4;
    u32x4 wv[20];
#pragma unroll
    for (int s = 0; s < 20; ++s) wv[s] = lds_r128(k.lds + (f * 20 + s) * 1024 + k.lane * 16);
    f32x4 c0 = {0, 0, 0, 0}, c1 = {0, 0, 0, 0};
    const bf16x8 ones = {0x3F80, 0x3F80, 0x3F80, 0x3F80, 0x3F80, 0x3F80, 0x3F80, 0x3F80};
#pragma unroll
    for (int s = 0; s < 20; s += 2) { c0 = mfma16(as_frag(wv[s]), ones, c0); c1 = mfma16(as_frag(wv[s + 1]), ones, c1); }
    if (t == 0) *(LAS f32x4*)(k.lds + P4_CS + f * 64 + g * 16) = c0 + c1;
}
DI void p4_phase(const Ctx& k, int rb, int f0, int nf, int ct5) {
    const int lane = k.lane, w = k.w, t = lane & 15, g = lane >> 4;
    const lds_ptr lds = k.lds;
    const unsigned ap = WS_H + (rb * 16 + w * 2) * OH_TILE + lane * 16;
    u32x4 a0[20], a1[20];
#define P4_LD(s) do { a0[s] = ld128(k.rs, ap + (s) * 1024); a1[s] = ld128(k.rs, ap + OH_TILE + (s) * 1024); } while (0)
    u32x2 pv[2][4];
#pragma unroll
    for (int i = 0; i < 2; ++i)
#pragma unroll
        for (int j = 0; j < 4; ++j) pv[i][j] = ld64(k.rs, WS_PST + (rb * 16 + w * 2 + i) * 2048 + (4 * g + j) * 128 + t * 8);
    P4_LD(0); P4_LD(1); P4_LD(2);
    lds_ptr wb[3] = {lds + lane * 16, lds + lane * 16 + 61440, lds + lane * 16 + 122880};
    PIN(wb[1]); PIN(wb[2]);
    bf16x8 wf[2][7];
#pragma unroll
    for (int f = 0; f < 7; ++f) wf[0][f] = as_frag(lds_r128(wb[f / 3] + (f % 3) * 20480));
    f32x4 acc[2][7];
#pragma unroll
    for (int f = 0; f < 7; ++f) { acc[0][f] = (f32x4){0, 0, 0, 0}; acc[1][f] = (f32x4){0, 0, 0, 0}; PIN(acc[0][f]); PIN(acc[1][f]); }
    float mean[2], rstd[2], vpe[2];
    mfma_fence();
    STAMP(13);
    PRIO(1);
#pragma unroll
    for (int s = 0; s < 20; ++s) {
        if (s < 6) { P4_LD(2 * s + 3); P4_LD(2 * s + 4); } else if (s + 9 < 20) P4_LD(s + 9);
        SBAR();
        if (s == 10) { __builtin_amdgcn_s_barrier(); w2_stage<7, 9>(k, ct5, 0, 0, 0); SBAR(); }
        if (s == 4) {
#pragma unroll
            for (int i = 0; i < 2; ++i) {
                float sm = (__uint_as_float(pv[i][0].x) + __uint_as_float(pv[i][1].x)) + (__uint_as_float(pv[i][2].x) + __uint_as_float(pv[i][3].x));
                float q = (__uint_as_float(pv[i][0].y) + __uint_as_float(pv[i][1].y)) + (__uint_as_float(pv[i][2].y) + __uint_as_float(pv[i][3].y));
                sm = sum_x32(sum_x16(sm)); q = sum_x32(sum_x16(q));
                mean[i] = sm * (1.0f / D);
                vpe[i] = fmaxf(q * (1.0f / D) - mean[i] * mean[i], 0.f) + LN_EPS;
                rstd[i] = __builtin_amdgcn_rsqf(vpe[i]);
            }
            PIN(mean[0]); PIN(mean[1]); PIN(rstd[0]); PIN(rstd[1]); PIN(vpe[0]); PIN(vpe[1]);
            SBAR();
        }
#pragma unroll
        for (int f = 0; f < 7; ++f) {
            if (s + 1 < 20) wf[(s + 1) & 1][f] = as_frag(lds_r128(wb[f / 3] + (f % 3) * 20480 + (s + 1) * 1024));
            mfma16_acc(acc[0][f], wf[s & 1][f], as_frag(a0[s]));
            mfma16_acc(acc[1][f], wf[s & 1][f], as_frag(a1[s]));
            SBAR();
        }
    }
#undef P4_LD
    PRIO(0);
    mfma_fence();
    STAMP(14);
    f32x4 cs[7];
#pragma unroll
    for (int f = 0; f < 7; ++f) cs[f] = *(const LAS f32x4*)(lds + P4_CS + f * 64 + g * 16);
#pragma unroll
    for (int i = 0; i < 2; ++i) {
        const float ce = -LOG2E * rstd[i], ir2 = vpe[i], nm = -mean[i];
        const f32x2 ce2 = {ce, ce}, ir22 = {ir2, ir2}, nm2 = {nm, nm};
        unsigned d[8];
#pragma unroll
        for (int f = 0; f < 7; ++f) {
            const f32x2 ga = __builtin_elementwise_fma(nm2, __builtin_shufflevector(cs[f], cs[f], 0, 1), __builtin_shufflevector(acc[i][f], acc[i][f], 0, 1));
            const f32x2 ua = __builtin_elementwise_fma(nm2, __builtin_shufflevector(cs[f], cs[f], 2, 3), __builtin_shufflevector(acc[i][f], acc[i][f], 2, 3));
            const f32x2 t2 = ga * ce2;
            const f32x2 e2 = {EX(t2.x), EX(t2.y)};
            const f32x2 d2 = __builtin_elementwise_fma(e2, ir22, ir22);
            const f32x2 r2 = {__builtin_amdgcn_rcpf(d2.x), __builtin_amdgcn_rcpf(d2.y)};
            const f32x2 o2 = (ga * ua) * r2;
            d[f] = cvtpk(o2.x, o2.y);
        }
        d[7] = 0;
        const unsigned dst = WS_HH + (rb * 16 + w * 2 + i) * HH_TILE + f0 * 256 + lane * 16;
#pragma unroll
        for (int u = 0; u < 2; ++u) {
            auto a = __builtin_amdgcn_permlane32_swap(d[4 * u], d[4 * u + 2], false, false);
            auto b = __builtin_amdgcn_permlane32_swap(d[4 * u + 1], d[4 * u + 3], false, false);
            auto e = __builtin_amdgcn_permlane16_swap(a[0], b[0], false, false);
            auto f = __builtin_amdgcn_permlane16_swap(a[1], b[1], false, false);
            if (4 * u + g < nf) st128(k.rs, dst + u * 1024, (u32x4){e[0], e[1], f[0], f[1]});
        }
    }
}

constexpr int LDS_BYTES = 163840;
__global__ __launch_bounds__(512, 2) void fused_block(const uint16_t* __restrict__ x, const uint16_t* __restrict__ wqkv, const uint16_t* __restrict__ wo,
                                                      const uint16_t* __restrict__ w1, const uint16_t* __restrict__ w3, const uint16_t* __restrict__ w2,
                                                      char* __restrict__ ws, uint16_t* __restrict__ out) {
    extern __shared__ __attribute__((aligned(16))) char smem[];
    unsigned long long t_begin;
    asm volatile("s_memrealtime %0" : "=s"(t_begin));
    __builtin_amdgcn_sched_barrier(0);
    Ctx k;
    k.x = x; k.wqkv = wqkv; k.wo = wo; k.w1 = w1; k.w3 = w3; k.w2 = w2; k.ws = ws; k.out = out;
    k.rs = __builtin_amdgcn_make_buffer_rsrc(ws, 0, WS_END + 20480, 0x00020000);
    k.lds = (lds_ptr)smem; k.lds0 = (unsigned)(uintptr_t)smem;
    k.c = blockIdx.x; k.xcd = k.c & 7; k.lb = k.c >> 3;
    k.tid = threadIdx.x; k.lane = k.tid & 63; k.w = rfl(k.tid >> 6);
    k.rsw = (u32x4){(unsigned)(uintptr_t)ws, (unsigned)((uintptr_t)ws >> 32) & 0xffffu, WS_END + 20480, 0x00020000u};
    k.tag = 0;
    k.sc = 0; k.sr = 0;
    startup_load(k);
    const int rb1 = (k.xcd >> 2) * 8 + (k.lb & 7), cb1 = (k.xcd & 3) * 4 + (k.lb >> 3);
    if (cb1 < 15) {
        if (cb1 >= 10) p1_body<true>(k, rb1, cb1); else p1_body<false>(k, rb1, cb1);
        STAMP(3);
        WAIT_BAR(0);
        if (k.tid == 0) st_flag(ws + WS_F1 + (rb1 * 16 + cb1) * FLAG_STRIDE, k.tag);
    } else {
        WAIT_VM(0);
        startup_tag(k);
        ln1_stats(k, rb1);
        if (rb1 < 15) colsum_qkv(k, rb1);
        WAIT_BAR(0);
    }
    STAMP(4);
    if (k.w) pretile_load(k);
    const int h = k.xcd * 4 + (k.lb & 3), qb = k.lb >> 2;
    const int rb64 = k.xcd * 4 + (k.lb >> 3), ct = k.lb & 7;
    const int rb4 = (k.xcd >> 1) * 2 + (k.lb >> 4), cb4 = (k.xcd & 1) * 16 + (k.lb & 15);
    const int f0 = cb4 < 24 ? cb4 * 7 : 168 + (cb4 - 24) * 6, nf = cb4 < 24 ? 7 : 6;
    AttnCtx ac;
    attn_setup(k, ac, h);
    if (k.w == 0) {
        const int c0 = (20 * h) >> 7, c1 = (20 * h + 19) >> 7, j = k.lane & 15, sel = k.lane >> 4;
        const int cbk = (sel & 1) ? c1 : c0;
        const unsigned o0 = WS_F1 + (j * 16 + (sel < 2 ? 5 : 10) + cbk) * FLAG_STRIDE;
        const unsigned o1 = WS_F1 + ((2 * qb + (k.lane & 1)) * 16 + ((k.lane & 2) ? c1 : c0)) * FLAG_STRIDE;
        poll_flags2(k, o0, o1);
    }
    WAIT_BAR(0);
    STAMP(5);
    p2_phase(k, ac, h, qb, ct, f0);
    STAMP(8);
    WAIT_BAR(0);
    if (k.tid == 0) st_flag(ws + WS_F2 + (qb * 32 + h) * FLAG_STRIDE, k.tag);
    P3Pre pre;
    p3_pre(k, rb64, ct, pre);
    if (k.w == 0) poll_flags(k, WS_F2 + (k.xcd * 32 + (k.lane & 31)) * FLAG_STRIDE);
    else if (k.w <= 3) w13_prefetch(k, f0, k.w - 1);
    WAIT_BAR(0);
    if (k.c == 0 && k.tid == 0) st_flag(ws + WS_EPOCH, k.tag);
    STAMP(9);
    u32x2 hv[3];
    p3_phase(k, rb64, ct, f0, pre, hv);
    STAMP(11);
    if (k.w < 4) WAIT_VM(0);
    BAR_L();
    if (k.tid == 0) st_flag(ws + WS_F3 + (k.xcd * 32 + k.lb) * FLAG_STRIDE, k.tag);
    if (k.w == 0) poll_flags(k, WS_F3 + (rb4 * 32 + (k.lane & 31)) * FLAG_STRIDE);
    else {
        WAIT_VM(0);
        p4_colsum(k, k.w - 1);
    }
    WAIT_BAR(0);
    STAMP(12);
    p4_phase(k, rb4, f0, nf, ct);
    STAMP(15);
    WAIT_BAR(0);
    if (k.tid == 0) st_flag(ws + WS_F4 + (rb4 * 32 + cb4) * FLAG_STRIDE, k.tag);
    if (k.w == 0) poll_flags(k, WS_F4 + ((rb64 >> 2) * 32 + (k.lane & 31)) * FLAG_STRIDE);
    else {
        w2_stage<7, 10>(k, ct, 1, 7, 1);
        const int li = (k.w - 1) * 64 + k.lane;
        if (k.w <= 5) {
            const int ln = (k.lb >> 3) * 260 + (li < 260 ? li : li - 64), hf = ln >= 520;
            const unsigned warm = ld32(k.rs, WS_W2 + (ct * 54 + hf * 27 + 14) * 5120 + (ln - hf * 520) * 128);
            asm volatile("" :: "v"(warm));
        }
    }
    WAIT_BAR(0);
    STAMP(17);
    p5_phase(k, rb64, ct, hv);
    while (__builtin_amdgcn_s_memrealtime() - t_begin < 8000ull) __builtin_amdgcn_s_sleep(2);
}

extern "C" void kernel_launch(void* const* d_in, const int* in_sizes, int n_in, void* d_out, int out_size, void* d_ws, size_t ws_size, hipStream_t stream) {
    (void)hipFuncSetAttribute((const void*)fused_block, hipFuncAttributeMaxDynamicSharedMemorySize, LDS_BYTES);
    fused_block<<<256, 512, LDS_BYTES, stream>>>((const uint16_t*)d_in[0], (const uint16_t*)d_in[1], (const uint16_t*)d_in[2], (const uint16_t*)d_in[4],
                                                 (const uint16_t*)d_in[5], (const uint16_t*)d_in[6], (char*)d_ws, (uint16_t*)d_out);
}
```

```cpp
#include <hip/hip_runtime.h>
#include <hip/hip_bf16.h>
#include <stdint.h>

#define DI __device__ __forceinline__
#define LAS __attribute__((address_space(3)))
#define GAS __attribute__((address_space(1)))
typedef short bf16x8 __attribute__((ext_vector_type(8)));
typedef short s16x4 __attribute__((ext_vector_type(4)));
typedef float f32x2 __attribute__((ext_vector_type(2)));
typedef float f32x4 __attribute__((ext_vector_type(4)));
typedef float f32x16 __attribute__((ext_vector_type(16)));
typedef unsigned u32x2 __attribute__((ext_vector_type(2)));
typedef unsigned u32x4 __attribute__((ext_vector_type(4)));
typedef unsigned long long u64;
typedef __bf16 bf16x2_t __attribute__((ext_vector_type(2)));
typedef short v4i16_t __attribute__((ext_vector_type(4)));
typedef LAS char* lds_ptr;
typedef __amdgpu_buffer_rsrc_t rsrc_t;

constexpr int S = 2048, D = 640, H = 32, DH = 20, F = 1728, NQKV = 3 * D;
constexpr float LN_EPS = 1e-5f;
constexpr float LOG2E = 1.4426950408889634f;
constexpr float QSCALE = 0.22360679774997896f * LOG2E;

constexpr unsigned WS_EPOCH = 0;
constexpr unsigned FLAG_STRIDE = 64, FLAG_ARR = 256 * FLAG_STRIDE;
constexpr unsigned WS_F1 = 4096, WS_FP = WS_F1 + FLAG_ARR, WS_F2 = WS_FP + FLAG_ARR, WS_F3 = WS_F2 + FLAG_ARR, WS_F4 = WS_F3 + FLAG_ARR;
constexpr unsigned WS_ST1 = 131072;
constexpr unsigned WS_CSQ = WS_ST1 + 32768;
constexpr unsigned WS_PST = 262144;
constexpr unsigned WS_Q = 524288;
constexpr unsigned IMG = S * D * 2;
constexpr unsigned QK_TILE = 32 * 1280, QK_IMG = 64 * QK_TILE;
constexpr unsigned WS_K = WS_Q + QK_IMG;
constexpr unsigned WS_V = WS_K + QK_IMG;
constexpr unsigned OH_TILE = 21504, HH_TILE = 56320;
constexpr unsigned WS_O = WS_V + IMG;
constexpr unsigned WS_H = WS_O + 128 * OH_TILE;
constexpr unsigned WS_HH = WS_H + 128 * OH_TILE;
constexpr unsigned WS_WO = WS_HH + 128 * HH_TILE;
constexpr unsigned WS_W13 = WS_WO + D * D * 2;
constexpr unsigned WS_W2 = WS_W13 + D * F * 4;
constexpr unsigned WS_END = WS_W2 + F * D * 2;

DI unsigned cvtpk(float lo, float hi) { f32x2 v = {lo, hi}; bf16x2_t b = __builtin_convertvector(v, bf16x2_t); return __builtin_bit_cast(unsigned, b); }
DI float bflo(unsigned u) { return __uint_as_float(u << 16); }
DI float bfhi(unsigned u) { return __uint_as_float(u & 0xffff0000u); }
DI f32x4 mfma16(bf16x8 a, bf16x8 b, f32x4 c) { return __builtin_amdgcn_mfma_f32_16x16x32_bf16(a, b, c, 0, 0, 0); }
DI void mfma16_acc(f32x4& c, bf16x8 a, bf16x8 b) { asm volatile("v_mfma_f32_16x16x32_bf16 %0, %1, %2, %0" : "+v"(c) : "v"(a), "v"(b)); }
DI void mfma_fence() { __builtin_amdgcn_sched_barrier(0); asm volatile("s_nop 7\n\ts_nop 1" ::: "memory"); __builtin_amdgcn_sched_barrier(0); }
DI f32x16 mfma32(bf16x8 a, bf16x8 b, f32x16 c) { return __builtin_amdgcn_mfma_f32_32x32x16_bf16(a, b, c, 0, 0, 0); }
DI int rfl(int x) { return __builtin_amdgcn_readfirstlane(x); }
template <bool SC1>
DI void glds16(unsigned voff, const void* sbase, unsigned ldsdst) {
    unsigned keep;
    if (SC1) asm volatile("s_mov_b32 %0, m0\n\ts_mov_b32 m0, %3\n\ts_nop 0\n\tglobal_load_lds_dwordx4 %1, %2 sc1\n\ts_mov_b32 m0, %0" : "=&s"(keep) : "v"(voff), "s"(sbase), "s"(ldsdst) : "memory");
    else asm volatile("s_mov_b32 %0, m0\n\ts_mov_b32 m0, %3\n\ts_nop 0\n\tglobal_load_lds_dwordx4 %1, %2\n\ts_mov_b32 m0, %0" : "=&s"(keep) : "v"(voff), "s"(sbase), "s"(ldsdst) : "memory");
}
DI void glds4(const void* gptr, unsigned ldsdst) {
    unsigned keep;
    asm volatile("s_mov_b32 %0, m0\n\ts_mov_b32 m0, %2\n\ts_nop 0\n\tglobal_load_lds_dword %1, off\n\ts_mov_b32 m0, %0" : "=&s"(keep) : "v"(gptr), "s"(ldsdst) : "memory");
}
#define WAIT_VM(n) asm volatile("s_waitcnt vmcnt(" #n ")" ::: "memory")
#define WAIT_BAR(n) asm volatile("s_waitcnt vmcnt(" #n ") lgkmcnt(0)\n\ts_barrier" ::: "memory")
#define BAR_L() asm volatile("s_waitcnt lgkmcnt(0)\n\ts_barrier" ::: "memory")
#define SBAR() __builtin_amdgcn_sched_barrier(0)
#define PIN(x) asm volatile("" : "+v"(x))
#define PRIO(n) __builtin_amdgcn_s_setprio(n)
#define EX(v) __builtin_amdgcn_exp2f(v)
DI u32x4 lds_r128(lds_ptr p) { return *(const LAS u32x4*)p; }
DI s16x4 lds_tr(lds_ptr p) { return __builtin_bit_cast(s16x4, __builtin_amdgcn_ds_read_tr16_b64_v4i16((LAS v4i16_t*)p)); }
DI bf16x8 as_frag(u32x4 v) { return __builtin_bit_cast(bf16x8, v); }
DI bf16x8 cat_frag(s16x4 a, s16x4 b) { return (bf16x8){a[0], a[1], a[2], a[3], b[0], b[1], b[2], b[3]}; }
DI float swap32_max(float m) { auto r = __builtin_amdgcn_permlane32_swap(__float_as_uint(m), __float_as_uint(m), false, false); return fmaxf(__uint_as_float(r[0]), __uint_as_float(r[1])); }
DI float sum_x16(float v) { auto r = __builtin_amdgcn_permlane16_swap(__float_as_uint(v), __float_as_uint(v), false, false); return __uint_as_float(r[0]) + __uint_as_float(r[1]); }
DI float sum_x32(float v) { auto r = __builtin_amdgcn_permlane32_swap(__float_as_uint(v), __float_as_uint(v), false, false); return __uint_as_float(r[0]) + __uint_as_float(r[1]); }
DI u32x4 ld128(rsrc_t r, unsigned off) { return __builtin_amdgcn_raw_buffer_load_b128(r, off, 0, 16); }
DI u32x2 ld64(rsrc_t r, unsigned off) { return __builtin_amdgcn_raw_buffer_load_b64(r, off, 0, 16); }
DI unsigned ld32(rsrc_t r, unsigned off) { return __builtin_amdgcn_raw_buffer_load_b32(r, off, 0, 16); }
DI void st128(rsrc_t r, unsigned off, u32x4 v) { __builtin_amdgcn_raw_buffer_store_b128(v, r, off, 0, 16); }
DI void st64(rsrc_t r, unsigned off, u32x2 v) { __builtin_amdgcn_raw_buffer_store_b64(v, r, off, 0, 16); }

DI void ald128(u32x4& v, u32x4 rsw, unsigned voff, unsigned soff) { asm volatile("buffer_load_dwordx4 %0, %1, %2, %3 offen sc1" : "=&v"(v) : "v"(voff), "s"(rsw), "s"(soff) : "memory"); }
template <int N> DI void ald_wait(u32x4& v) { asm volatile("s_waitcnt vmcnt(%1)" : "+v"(v) : "i"(N) : "memory"); }
struct Ctx {
    const uint16_t *x, *wqkv, *wo, *w1, *w3, *w2;
    char* ws; uint16_t* out;
    rsrc_t rs; u32x4 rsw; u64 tag; lds_ptr lds; unsigned lds0;
    int c, xcd, lb, tid, lane, w;
    mutable unsigned sc, sr;
};

#define STAMP(i)
#define STAMP_FLUSH()

DI u64 ld_flag(const char* p) { return __hip_atomic_load((const GAS u64*)p, __ATOMIC_RELAXED, __HIP_MEMORY_SCOPE_AGENT); }
DI void st_flag(char* p, u64 v) { __hip_atomic_store((GAS u64*)p, v, __ATOMIC_RELAXED, __HIP_MEMORY_SCOPE_AGENT); }
constexpr unsigned SPIN_CAP = 1u << 18;
DI void poll_flags(const Ctx& k, unsigned off) {
    for (unsigned spins = 0; spins < SPIN_CAP; ++spins) {
        if (__all(ld_flag(k.ws + off) == k.tag)) break;
        __builtin_amdgcn_s_sleep(1);
    }
}
DI void poll_flags2(const Ctx& k, unsigned off0, unsigned off1) {
    for (unsigned spins = 0; spins < SPIN_CAP; ++spins) {
        const u64 a = ld_flag(k.ws + off0), b = ld_flag(k.ws + off1);
        if (__all(a == k.tag && b == k.tag)) break;
        __builtin_amdgcn_s_sleep(1);
    }
}
DI void poll_all(const Ctx& k, unsigned arr) {
    for (unsigned spins = 0; spins < SPIN_CAP; ++spins) {
        const unsigned o = arr + k.lane * FLAG_STRIDE;
        const u64 a = ld_flag(k.ws + o), b = ld_flag(k.ws + o + 64 * FLAG_STRIDE), c = ld_flag(k.ws + o + 128 * FLAG_STRIDE), d = ld_flag(k.ws + o + 192 * FLAG_STRIDE);
        if (__all(a == k.tag && b == k.tag && c == k.tag && d == k.tag)) break;
        __builtin_amdgcn_s_sleep(1);
    }
}

constexpr int SU_LDS = 131072;
DI void startup_load(const Ctx& k) { glds4(k.ws + WS_EPOCH + (k.lane & 1) * 4, k.lds0 + SU_LDS + k.w * 256); }
DI void startup_tag(Ctx& k) {
    const u32x2 e = *(const LAS u32x2*)(k.lds + SU_LDS + k.w * 256);
    k.tag = (((u64)(unsigned)rfl(e.y) << 32) | (unsigned)rfl(e.x)) + 1;
}
DI void ln1_stats(const Ctx& k, int j) {
    u32x4 v[2][10];
#pragma unroll
    for (int p = 0; p < 2; ++p)
#pragma unroll
        for (int i = 0; i < 10; ++i) v[p][i] = *(const u32x4*)(k.x + (size_t)(128 * j + 16 * k.w + 8 * p + (k.lane >> 3)) * D + ((k.lane & 7) + 8 * i) * 8);
#pragma unroll
    for (int p = 0; p < 2; ++p) {
        float s = 0.f, q = 0.f;
#pragma unroll
        for (int i = 0; i < 10; ++i)
#pragma unroll
            for (int e = 0; e < 4; ++e) { const float a = bflo(v[p][i][e]), b = bfhi(v[p][i][e]); s += a + b; q = fmaf(a, a, fmaf(b, b, q)); }
#pragma unroll
        for (int o = 4; o >= 1; o >>= 1) { s += __shfl_xor(s, o); q += __shfl_xor(q, o); }
        const float mean = s * (1.0f / D), var = fmaxf(q * (1.0f / D) - mean * mean, 0.f);
        const float val = (k.lane & 1) ? 1.0f / sqrtf(var + LN_EPS) : mean;
        const int row = 128 * j + 16 * k.w + 8 * p + (k.lane >> 3);
        if ((k.lane & 7) < 2) st64(k.rs, WS_ST1 + row * 16 + (k.lane & 1) * 8, (u32x2){__float_as_uint(val), (unsigned)k.tag});
    }
}
DI void colsum_qkv(const Ctx& k, int cb) {
    float a[8] = {0, 0, 0, 0, 0, 0, 0, 0};
    u32x4 v[20];
#pragma unroll
    for (int i = 0; i < 20; ++i) v[i] = *(const u32x4*)(k.wqkv + (size_t)(k.w * 80 + i * 4 + (k.lane >> 4)) * NQKV + cb * 128 + (k.lane & 15) * 8);
#pragma unroll
    for (int i = 0; i < 20; ++i)
#pragma unroll
        for (int j = 0; j < 4; ++j) { a[2 * j] += bflo(v[i][j]); a[2 * j + 1] += bfhi(v[i][j]); }
    LAS float* red = (LAS float*)k.lds;
#pragma unroll
    for (int j = 0; j < 8; ++j) { a[j] += __shfl_xor(a[j], 16); a[j] += __shfl_xor(a[j], 32); }
    if (k.lane < 16) {
#pragma unroll
        for (int j = 0; j < 8; ++j) red[k.w * 128 + k.lane * 8 + j] = a[j];
    }
    BAR_L();
    if (k.tid < 128) {
        float t = 0.f;
#pragma unroll
        for (int i = 0; i < 8; ++i) t += red[i * 128 + k.tid];
        st64(k.rs, WS_CSQ + (cb * 128 + k.tid) * 8, (u32x2){__float_as_uint(t), (unsigned)k.tag});
    }
}

constexpr int PT_LDS = 131072;
DI int pt_off(int i) { return PT_LDS + (i < 3 ? i * 8192 : 24576 + (i - 3) * 4096); }
DI void pretile_load(const Ctx& k) {
    const int wv = k.w - 1;
#pragma unroll
    for (int i = 0; i < 5; ++i) {
        const int u = k.c + 256 * i;
        if (u < 540) {
            const int s = u % 20, fb = u / 20;
            const uint16_t* src = ((k.lane & 1) ? k.w3 : k.w1) + fb * 64 + (k.lane >> 1) * 2 + (size_t)32 * s * F;
            for (int r = wv; r < 32; r += 7) glds4(src + (size_t)r * F, k.lds0 + pt_off(i) + r * 256);
        } else {
            const int pc = (i & 1) ? wv - 3 : wv;
            if (pc >= 0 && pc < 4) {
                const bool is2 = u < 1080;
                const int uu = is2 ? u - 540 : u - 1080, s = uu / 10, nb = uu % 10, r = 8 * pc + (k.lane >> 3);
                glds16<false>((32 * s + r) * (D * 2) + nb * 128 + (k.lane & 7) * 16, is2 ? k.w2 : k.wo, k.lds0 + pt_off(i) + pc * 1024);
            }
        }
    }
}
struct PreTile { u32x4 v[5]; };
DI void pretile_read(const Ctx& k, PreTile& p) {
    const int g = k.lane >> 4, q = (k.lane >> 2) & 3, pp = k.lane & 3;
#pragma unroll
    for (int i = 0; i < 5; ++i) {
        const int u = k.c + 256 * i;
        const bool wide = u < 540;
        const int rs = wide ? 256 : 128, f = wide ? k.w : (k.w & 3);
        const lds_ptr a = k.lds + pt_off(i) + (8 * g + q) * rs + f * 32 + pp * 8;
        p.v[i] = __builtin_bit_cast(u32x4, cat_frag(lds_tr(a), lds_tr(a + 4 * rs)));
    }
}
DI void pretile_store(const Ctx& k, const PreTile& p, int i) {
    const int u = k.c + 256 * i;
    unsigned dst;
    if (u < 540) dst = WS_W13 + (((u / 20) * 8 + k.w) * 20 + u % 20) * 1024;
    else {
        const bool is2 = u < 1080;
        const int uu = is2 ? u - 540 : u - 1080, s = uu / 10, cfg = (uu % 10) * 4 + (k.w & 3), ct = cfg / 5, cf = cfg % 5;
        dst = is2 ? WS_W2 + ((ct * 54 + s) * 5 + cf) * 1024 : WS_WO + ((ct * 20 + s) * 5 + cf) * 1024;
    }
    st128(k.rs, dst + k.lane * 16, p.v[i]);
}

constexpr int P1_SLOT = 32768;
struct P1Frag { bf16x8 a[4], b[2]; };
template <bool ISV> DI void p1_mma(f32x4 (&acc)[4][2], const P1Frag& f, int m, int n) { if (ISV) mfma16_acc(acc[m][n], f.a[m], f.b[n]); else mfma16_acc(acc[m][n], f.b[n], f.a[m]); }
template <bool ISV>
DI void p1_body(Ctx& k, int rb, int cb) {
    const int lane = k.lane, w = k.w, th = w >> 2, cg = w & 3, t = lane & 15, g = lane >> 4;
    unsigned voffA[2], voffW[2];
#pragma unroll
    for (int i = 0; i < 2; ++i) {
        const int ra = 8 * (2 * w + i) + (lane >> 3), ca = (lane & 7) ^ ((ra >> 1) & 7);
        voffA[i] = ra * (D * 2) + ca * 16;
        const int rw = 4 * (2 * w + i) + (lane >> 4), fw = (rw & 3) | (((rw >> 3) & 1) << 2), cw = (lane & 15) ^ (fw << 1);
        voffW[i] = rw * (NQKV * 2) + cw * 16;
    }
    const char* gA = (const char*)k.x + (size_t)rb * 128 * D * 2;
    const char* gW = (const char*)k.wqkv + cb * 256;
    auto stage = [&](int kt) {
        const unsigned sl = k.lds0 + (kt & 3) * P1_SLOT + w * 2048;
#pragma unroll
        for (int i = 0; i < 2; ++i) glds16<false>(voffA[i], gA + kt * 128, sl + i * 1024);
#pragma unroll
        for (int i = 0; i < 2; ++i) glds16<false>(voffW[i], gW + (size_t)kt * 64 * NQKV * 2, sl + 16384 + i * 1024);
    };
    stage(0); stage(1); stage(2); stage(3);
    int aoff[2], woff[2];
    aoff[0] = (th * 64 + t) * 128 + ((g ^ (t >> 1)) << 4); aoff[1] = aoff[0] ^ 64;
    const int q4 = (lane >> 2) & 3, p = lane & 3, fw = q4 | ((g & 1) << 2);
#pragma unroll
    for (int n = 0; n < 2; ++n) woff[n] = 16384 + (8 * g + q4) * 256 + (((2 * cg + n) ^ fw) << 5) + p * 8;
    f32x4 acc[4][2];
#pragma unroll
    for (int m = 0; m < 4; ++m) { acc[m][0] = (f32x4){0, 0, 0, 0}; acc[m][1] = (f32x4){0, 0, 0, 0}; PIN(acc[m][0]); PIN(acc[m][1]); }
    mfma_fence();
    P1Frag f0, f1;
#define P1_RA(F_, sl, ks, m) F_.a[m] = as_frag(lds_r128(sl + aoff[ks] + (m) * 2048))
#define P1_RB(F_, sl, ks, n) F_.b[n] = cat_frag(lds_tr(sl + woff[n] + (ks) * 8192), lds_tr(sl + woff[n] + (ks) * 8192 + 1024))
#define P1_STEP(RD, FN, sl, ks, FC) do { \
        p1_mma<ISV>(acc, FC, 0, 0); SBAR(); if (RD) P1_RB(FN, sl, ks, 0); PIN(FC.b[1]); SBAR(); \
        p1_mma<ISV>(acc, FC, 0, 1); SBAR(); if (RD) P1_RA(FN, sl, ks, 0); PIN(FC.a[1]); SBAR(); \
        p1_mma<ISV>(acc, FC, 1, 0); SBAR(); if (RD) P1_RA(FN, sl, ks, 1); SBAR(); \
        p1_mma<ISV>(acc, FC, 1, 1); SBAR(); if (RD) P1_RB(FN, sl, ks, 1); PIN(FC.a[2]); SBAR(); \
        p1_mma<ISV>(acc, FC, 2, 0); SBAR(); if (RD) P1_RA(FN, sl, ks, 2); SBAR(); \
        p1_mma<ISV>(acc, FC, 2, 1); SBAR(); if (RD) P1_RA(FN, sl, ks, 3); PIN(FC.a[3]); SBAR(); \
        p1_mma<ISV>(acc, FC, 3, 0); SBAR(); \
        p1_mma<ISV>(acc, FC, 3, 1); SBAR(); } while (0)
    WAIT_BAR(12);
    STAMP(1);
    startup_tag(k);
    { const lds_ptr sl = k.lds; P1_RB(f0, sl, 0, 0); P1_RB(f0, sl, 0, 1); P1_RA(f0, sl, 0, 0); P1_RA(f0, sl, 0, 1); P1_RA(f0, sl, 0, 2); P1_RA(f0, sl, 0, 3); SBAR(); }
    u32x2 gr = {0, 0};
    PRIO(1);
    const unsigned groff = k.tid < 256 ? WS_ST1 + (rb * 128) * 16 + k.tid * 8 : WS_CSQ + (cb * 128 + (k.tid & 127)) * 8;
#pragma unroll
    for (int kt = 0; kt < 10; ++kt) {
        const lds_ptr sl = k.lds + (kt & 3) * P1_SLOT, sn = k.lds + ((kt + 1) & 3) * P1_SLOT;
        P1_STEP(true, f1, sl, 1, f0);
        if (kt < 9) {
            if (kt < 7) WAIT_BAR(8); else if (kt == 7) WAIT_BAR(4); else WAIT_BAR(0);
            if (kt == 6) gr = ld64(k.rs, groff);
            if (kt + 4 < 10) stage(kt + 4);
            SBAR();
        }
        P1_STEP(kt < 9, f0, sn, 0, f1);
    }
#undef P1_RA
#undef P1_RB
#undef P1_STEP
    PRIO(0);
    mfma_fence();
    STAMP(2);
    for (unsigned spins = 0; spins < SPIN_CAP; ++spins) {
        if (__all(gr.y == (unsigned)k.tag)) break;
        gr = ld64(k.rs, groff);
    }
    LAS float* sst = (LAS float*)k.lds;
    if (k.tid < 384) sst[k.tid] = __uint_as_float(gr.x);
    BAR_L();
    const LAS f32x2* st = (const LAS f32x2*)sst + th * 64;
    const LAS float* cs = sst + 256 + cg * 32;
    if (!ISV) {
        const float osc = cb < 5 ? QSCALE : 1.0f;
        const unsigned img = cb < 5 ? WS_Q : WS_K;
        const int cbs = cb % 5;
        unsigned dst[2]; f32x4 c4[2];
#pragma unroll
        for (int n = 0; n < 2; ++n) {
            const int cgi = cbs * 32 + cg * 8 + n * 4 + g, hd = cgi / 5, gi = cgi - 5 * hd;
            const int blk = gi == 4 ? 4 : ((gi & 1) << 1) | (gi >> 1);
            dst[n] = img + (rb * 4 + th * 2) * QK_TILE + hd * 1280 + blk * 256 + t * 8;
            c4[n] = *(const LAS f32x4*)(cs + n * 16 + 4 * g);
        }
#pragma unroll
        for (int m = 0; m < 4; ++m) {
            const f32x2 sm = st[m * 16 + t];
            const float r = sm.y * osc;
#pragma unroll
            for (int n = 0; n < 2; ++n) {
                const f32x4 y = (acc[m][n] - c4[n] * sm.x) * r;
                const unsigned o = dst[n] + (m >> 1) * QK_TILE + (m & 1) * 128;
                st64(k.rs, o, (u32x2){cvtpk(y[0], y[1]), cvtpk(y[2], y[3])});
            }
        }
    } else {
        const int pos = ((g & 1) << 1) | (g >> 1);
        const unsigned dst = WS_V + (rb * 8 + th * 4) * 20480 + ((cb - 10) * 128 + cg * 32 + t) * 32 + pos * 8;
        const float c1[2] = {cs[t], cs[16 + t]};
#pragma unroll
        for (int m = 0; m < 4; ++m) {
            f32x2 sm[4];
#pragma unroll
            for (int i = 0; i < 4; ++i) sm[i] = st[m * 16 + 4 * g + i];
#pragma unroll
            for (int n = 0; n < 2; ++n) {
                float y[4];
#pragma unroll
                for (int i = 0; i < 4; ++i) y[i] = (acc[m][n][i] - c1[n] * sm[i].x) * sm[i].y;
                st64(k.rs, dst + m * 20480 + n * 512, (u32x2){cvtpk(y[0], y[1]), cvtpk(y[2], y[3])});
            }
        }
    }
}

constexpr int AT_KT = 8192, AT_KB = 10240, AT_HALF = AT_KB + 10240, AT_BUF = 2 * AT_HALF;
constexpr int AT_ONES = AT_BUF, AT_ONES_BYTES = 10752, AT_KC = AT_ONES + AT_ONES_BYTES, AT_KC_BYTES = 2048, AT_FLAG = AT_KC + AT_KC_BYTES, WO_LDS = 61440, AT_RED = 0;
DI int at_slot(int i) { return i == 0 ? 0 : (i == 1 ? 61440 : 61440 + AT_BUF); }
struct AttnCtx {
    unsigned voff[5]; const char* gsrc; unsigned ldst, sstride;
    u32x4 qf0[2]; u32x2 qf1[2];
    int kofs, kt, vlane; bool vdata, hi;
};
DI void attn_stage(const AttnCtx& c, int sidx, int slot) {
#pragma unroll
    for (int i = 0; i < 5; ++i) glds16<true>(c.voff[i], c.gsrc + sidx * c.sstride, c.ldst + at_slot(slot) + i * 1024);
}
DI void attn_setup(const Ctx& k, AttnCtx& c, int h) {
    const int lane = k.lane, w = k.w, kh = w >> 2, r = lane & 31, hi = lane >> 5;
    const bool stV = (w >> 1) & 1;
#pragma unroll
    for (int i = 0; i < 5; ++i) {
        const int pc = 5 * (w & 1) + i;
        if (!stV) c.voff[i] = pc < 8 ? pc * QK_TILE + lane * 16 : ((pc - 8) * 4 + (lane >> 4)) * QK_TILE + 1024 + (lane & 15) * 16;
        else { const int ci = 64 * pc + lane, st = ci / 40, wi = ci % 40; c.voff[i] = st * 20480 + ((wi % 20) * 2 + wi / 20) * 16; }
    }
    c.gsrc = k.ws + (stV ? WS_V + h * 640 + kh * 64 * 20480 : WS_K + h * 1280 + kh * 32 * QK_TILE);
    c.sstride = stV ? 16 * 20480 : 8 * QK_TILE;
    c.ldst = k.lds0 + kh * AT_HALF + (stV ? AT_KB : 0) + (w & 1) * 5120;
    c.kofs = kh * AT_HALF + hi * 256 + r * 8;
    c.hi = hi;
    c.kt = hi ? AT_KC + r * 8 : kh * AT_HALF + AT_KT + r * 8;
    const int rr = r >= 28 ? r - 12 : r;
    c.vdata = rr < 20;
    c.vlane = c.vdata ? kh * AT_HALF + AT_KB + hi * 320 + rr * 16 : AT_ONES + 128;
    for (int i = k.tid; i < AT_ONES_BYTES / 4; i += 512) ((LAS unsigned*)(k.lds + AT_ONES))[i] = 0x3F803F80u;
    ((LAS unsigned*)(k.lds + AT_KC))[k.tid] = (k.tid & 1) ? 0u : 0x00003F80u;
    if (k.tid == 0) *(LAS unsigned*)(k.lds + AT_FLAG) = 0;
}
DI void attn_first_loads(const Ctx& k, AttnCtx& c, int h, int qb) {
    const int qg = k.w & 3, r = k.lane & 31, hi = k.lane >> 5;
    attn_stage(c, 0, 0);
#pragma unroll
    for (int i = 0; i < 2; ++i) {
        const unsigned qp = WS_Q + (qb * 8 + qg * 2 + i) * QK_TILE + h * 1280 + r * 8;
        const u32x2 a = ld64(k.rs, qp + hi * 256), b = ld64(k.rs, qp + hi * 256 + 512), e = ld64(k.rs, qp + 1024);
        c.qf0[i] = (u32x4){a.x, a.y, b.x, b.y};
        c.qf1[i] = hi ? (u32x2){0, 0} : e;
    }
    attn_stage(c, 1, 1);
}
DI bf16x8 kfrag0(lds_ptr p) { const u32x2 a = *(const LAS u32x2*)p, b = *(const LAS u32x2*)(p + 512); return as_frag((u32x4){a.x, a.y, b.x, b.y}); }
DI s16x4 kfrag1(lds_ptr p) { return __builtin_bit_cast(s16x4, *(const LAS u32x2*)p); }
DI s16x4 as_frag4(u32x2 v) { return __builtin_bit_cast(s16x4, v); }
DI f32x16 mfma32x8(s16x4 a, s16x4 b, f32x16 c) { return __builtin_amdgcn_mfma_f32_32x32x8bf16_1k(a, b, c, 0, 0, 0); }
DI void wo_prefetch(const Ctx& k, int ct) {
#pragma unroll 5
    for (int j = 0; j < 15; ++j) { int pc = k.w - 1 + 7 * j; if (pc >= 100) pc -= 7; glds16<true>(k.lane * 16, k.ws + WS_WO + ct * 102400 + pc * 1024, k.lds0 + WO_LDS + pc * 1024); }
}
DI void attn_fast(const Ctx& k, AttnCtx& c, const PreTile& pt, f32x16& oA, f32x16& oB, int ct3) {
    const lds_ptr lds = k.lds;
    const int hi = k.lane >> 5, kh = k.w >> 2;
    {
        const bf16x8 k0 = kfrag0(lds + c.kofs - kh * AT_HALF); const s16x4 k1 = kfrag1(lds + (hi ? c.kt : c.kt - kh * AT_HALF));
#pragma unroll
        for (int i = 0; i < 2; ++i) {
            f32x16 s = mfma32(k0, as_frag(c.qf0[i]), (f32x16){});
            s = mfma32x8(k1, as_frag4(c.qf1[i]), s);
            float m = s[0];
#pragma unroll
            for (int j = 1; j < 16; ++j) m = fmaxf(m, s[j]);
            m = swap32_max(m);
            if (hi) c.qf1[i].x = cvtpk(-m, 0.f) & 0xffffu;
        }
    }
    const bf16x8 qA0 = as_frag(c.qf0[0]), qB0 = as_frag(c.qf0[1]); const s16x4 qA1 = as_frag4(c.qf1[0]), qB1 = as_frag4(c.qf1[1]);
    f32x16 sA, sB, nA, nB;
    oA = (f32x16){}; oB = (f32x16){};
    bf16x8 kf0 = kfrag0(lds + c.kofs); s16x4 kf1 = kfrag1(lds + c.kt);
    sA = mfma32(kf0, qA0, (f32x16){}); sB = mfma32(kf0, qB0, (f32x16){});
    sA = mfma32x8(kf1, qA1, sA); sB = mfma32x8(kf1, qB1, sB);
    kf0 = kfrag0(lds + c.kofs + 1024); kf1 = kfrag1(lds + c.kt + 256);
    u32x4 pA0, pA1, pB0, pB1 = {0, 0, 0, 0};
    bf16x8 vf0, vf1 = {0, 0, 0, 0, 0, 0, 0, 0};
    u32x2 fa = {0, 0}, fb = fa, fc = fa, fd = fa;
#define GAP(MF, X, i, P, j) do { MF; SBAR(); X[i] = EX(X[i]); X[i + 1] = EX(X[i + 1]); X[i + 2] = EX(X[i + 2]); X[i + 3] = EX(X[i + 3]); \
        P[j] = cvtpk(X[i], X[i + 1]); P[j + 1] = cvtpk(X[i + 2], X[i + 3]); PIN(P); SBAR(); } while (0)
#define STEP(SA, SB, NA, NB, VP, KP, KT, HK) do { \
        GAP(NA = mfma32(kf0, qA0, (f32x16){}), SA, 0, pA0, 0); vf0 = as_frag(lds_r128(VP)); HK(0); SBAR(); \
        GAP(oB = mfma32(vf1, as_frag(pB1), oB), SA, 4, pA0, 2); vf1 = as_frag(lds_r128((VP) + 640)); HK(1); SBAR(); \
        GAP(NB = mfma32(kf0, qB0, (f32x16){}), SA, 8, pA1, 0); HK(2); SBAR(); \
        GAP(NA = mfma32x8(kf1, qA1, NA), SA, 12, pA1, 2); HK(3); SBAR(); \
        GAP(NB = mfma32x8(kf1, qB1, NB), SB, 0, pB0, 0); kf0 = kfrag0(KP); kf1 = kfrag1(KT); HK(4); SBAR(); \
        GAP(oA = mfma32(vf0, as_frag(pA0), oA), SB, 4, pB0, 2); HK(5); SBAR(); \
        GAP(oA = mfma32(vf1, as_frag(pA1), oA), SB, 8, pB1, 0); HK(6); SBAR(); \
        GAP(oB = mfma32(vf0, as_frag(pB0), oB), SB, 12, pB1, 2); HK(7); SBAR(); \
    } while (0)
#define HK_NONE(j)
#define STAGE(sidx, slot, nslot, PT, NST, HA, HB) do { \
        const int sb = at_slot(slot), sn = at_slot(nslot); \
        const lds_ptr vb = lds + (c.vdata ? c.vlane + sb : c.vlane); \
        lds_ptr k0 = lds + c.kofs + sb, k0n = lds + c.kofs + sn, k1 = lds + (hi ? c.kt : c.kt + sb), k1n = lds + (hi ? c.kt : c.kt + sn); \
        PIN(k0); PIN(k0n); PIN(k1); PIN(k1n); \
        STEP(sA, sB, nA, nB, vb, k0 + 2 * 1024, k1 + 2 * 256, HK_NONE); PT(0); \
        STEP(nA, nB, sA, sB, vb + 1280, k0 + 3 * 1024, k1 + 3 * 256, HK_NONE); PT(1); \
        STEP(sA, sB, nA, nB, vb + 2 * 1280, k0 + 4 * 1024, k1 + 4 * 256, HK_NONE); PT(2); \
        STEP(nA, nB, sA, sB, vb + 3 * 1280, k0 + 5 * 1024, k1 + 5 * 256, HK_NONE); PT(3); \
        STEP(sA, sB, nA, nB, vb + 4 * 1280, k0 + 6 * 1024, k1 + 6 * 256, HK_NONE); PT(4); \
        STEP(nA, nB, sA, sB, vb + 5 * 1280, k0 + 7 * 1024, k1 + 7 * 256, HK_NONE); \
        if ((sidx) == 3 && k.w == 0) {     \
            const unsigned tl = (unsigned)k.tag, th = (unsigned)(k.tag >> 32); \
            if (!__all(fa.x == tl && fa.y == th && fb.x == tl && fb.y == th && fc.x == tl && fc.y == th && fd.x == tl && fd.y == th)) poll_all(k, WS_FP); \
        } \
        WAIT_BAR(NST); \
        if ((sidx) < 2) attn_stage(c, (sidx) + 2, (nslot) == 2 ? 0 : (nslot) + 1); \
        if ((sidx) == 1 && k.tid == 0) st_flag(k.ws + WS_FP + k.c * FLAG_STRIDE, k.tag);     \
        if ((sidx) == 2 && k.w == 0) {     \
            const unsigned o = WS_FP + k.lane * FLAG_STRIDE; \
            fa = ld64(k.rs, o); fb = ld64(k.rs, o + 64 * FLAG_STRIDE); fc = ld64(k.rs, o + 128 * FLAG_STRIDE); fd = ld64(k.rs, o + 192 * FLAG_STRIDE); \
        } \
        SBAR(); \
        STEP(sA, sB, nA, nB, vb + 6 * 1280, k0n, k1n, HA); \
        STEP(nA, nB, sA, sB, vb + 7 * 1280, k0n + 1024, k1n + 256, HB); \
    } while (0)
#define PT_STORE(j) do { pretile_store(k, pt, j); SBAR(); } while (0)
#define PT_NONE(j)
    const char* wo_src = k.ws + WS_WO + ct3 * 102400;
#define WO_PIECE(n) do { int pc_ = k.w + 8 * (n); if (pc_ >= 100) pc_ -= 8; glds16<true>(k.lane * 16, wo_src + pc_ * 1024, k.lds0 + WO_LDS + pc_ * 1024); } while (0)
#define HK_WO_A(j) WO_PIECE(j)
#define HK_WO_B(j) do { if ((j) < 5) WO_PIECE(8 + (j)); } while (0)
    PRIO(1);
    STAGE(0, 0, 1, PT_STORE, 5, HK_NONE, HK_NONE);
    STAGE(1, 1, 2, PT_NONE, 0, HK_NONE, HK_NONE);
    STAGE(2, 2, 0, PT_NONE, 0, HK_NONE, HK_NONE);
    STAGE(3, 0, 1, PT_NONE, 0, HK_WO_A, HK_WO_B);
#undef STAGE
#undef STEP
#undef PT_STORE
#undef PT_NONE
#undef WO_PIECE
#undef HK_WO_A
#undef HK_WO_B
#undef HK_NONE
    PRIO(0);
    oB = mfma32(vf1, as_frag(pB1), oB);
#undef GAP
}
DI void attn_safe(const Ctx& k, AttnCtx& c, f32x16 (&o)[2], float (&mrun)[2]) {
    const lds_ptr lds = k.lds;
    attn_stage(c, 0, 0);
    if (k.lane >> 5) { c.qf1[0].x = 0; c.qf1[1].x = 0; }
    o[0] = (f32x16){}; o[1] = (f32x16){};
    mrun[0] = -INFINITY; mrun[1] = -INFINITY;
    for (int sidx = 0; sidx < 4; ++sidx) {
        const int sb = at_slot(sidx & 1);
        WAIT_BAR(0);
        if (sidx < 3) attn_stage(c, sidx + 1, (sidx + 1) & 1);
        const lds_ptr vb = lds + (c.vdata ? c.vlane + sb : c.vlane);
        for (int kt = 0; kt < 8; ++kt) {
            const bf16x8 k0 = kfrag0(lds + c.kofs + sb + kt * 1024); const s16x4 k1 = kfrag1(lds + (c.hi ? c.kt : c.kt + sb) + kt * 256);
            const bf16x8 v0 = as_frag(lds_r128(vb + kt * 1280)), v1 = as_frag(lds_r128(vb + kt * 1280 + 640));
#pragma unroll
            for (int i = 0; i < 2; ++i) {
                f32x16 s = mfma32(k0, as_frag(c.qf0[i]), (f32x16){});
                s = mfma32x8(k1, as_frag4(c.qf1[i]), s);
                float m = s[0];
#pragma unroll
                for (int j = 1; j < 16; ++j) m = fmaxf(m, s[j]);
                m = fmaxf(swap32_max(m), mrun[i]);
                const float alpha = EX(mrun[i] - m);
                mrun[i] = m;
                unsigned pk[8];
#pragma unroll
                for (int j = 0; j < 16; ++j) o[i][j] *= alpha;
#pragma unroll
                for (int j = 0; j < 8; ++j) pk[j] = cvtpk(EX(s[2 * j] - m), EX(s[2 * j + 1] - m));
                o[i] = mfma32(v0, as_frag((u32x4){pk[0], pk[1], pk[2], pk[3]}), o[i]);
                o[i] = mfma32(v1, as_frag((u32x4){pk[4], pk[5], pk[6], pk[7]}), o[i]);
            }
        }
    }
}
DI void p2_phase(const Ctx& k, AttnCtx& c, int h, int qb, int ct3) {
    const int lane = k.lane, qg = k.w & 3, kh = k.w >> 2, r = lane & 31, hi = lane >> 5;
    const lds_ptr lds = k.lds;
    attn_first_loads(k, c, h, qb);
    PreTile pt;
    pretile_read(k, pt);
    WAIT_BAR(5);
    STAMP(6);
    f32x16 o[2];
    attn_fast(k, c, pt, o[0], o[1], ct3);
    STAMP(7);
    LAS unsigned* flag = (LAS unsigned*)(lds + AT_FLAG);
    if (__any(hi && !(o[0][8] <= 5.764607523034235e17f && o[1][8] <= 5.764607523034235e17f)) && lane == 0) *flag = 1;
    BAR_L();
    const lds_ptr red = lds + AT_RED + qg * 8192 + lane * 16;
    auto put = [&](const f32x16& v, int slot) {
#pragma unroll
        for (int u = 0; u < 3; ++u) *(LAS f32x4*)(red + slot * 4096 + u * 1024) = (f32x4){v[4 * u], v[4 * u + 1], v[4 * u + 2], v[4 * u + 3]};
    };
    auto get = [&](int slot, int u) { return *(const LAS f32x4*)(red + slot * 4096 + u * 1024); };
    f32x4 of[3];
    if (*flag == 0) {
        if (kh) put(o[0], 0); else put(o[1], 1);
        BAR_L();
#pragma unroll
        for (int u = 0; u < 3; ++u) {
            const f32x4 p = get(kh, u);
#pragma unroll
            for (int j = 0; j < 4; ++j) of[u][j] = p[j] + (kh ? o[1][4 * u + j] : o[0][4 * u + j]);
        }
    } else {
        float mref[2];
        WAIT_BAR(0);
        attn_safe(k, c, o, mref);
        BAR_L();
        if (kh) put(o[0], 0); else put(o[1], 1);
        *(LAS float*)(red + (kh ? 0 : 4096) + 3072) = kh ? mref[0] : mref[1];
        BAR_L();
        const float m0 = kh ? mref[1] : mref[0], m1 = *(const LAS float*)(red + kh * 4096 + 3072), m = fmaxf(m0, m1);
        const float a0 = EX(m0 - m), a1 = EX(m1 - m);
#pragma unroll
        for (int u = 0; u < 3; ++u) {
            const f32x4 p = get(kh, u);
#pragma unroll
            for (int j = 0; j < 4; ++j) of[u][j] = p[j] * a1 + (kh ? o[1][4 * u + j] : o[0][4 * u + j]) * a0;
        }
        BAR_L();
        if (k.w) wo_prefetch(k, ct3);
    }
    {
        auto lr = __builtin_amdgcn_permlane32_swap(__float_as_uint(of[2][0]), __float_as_uint(of[2][0]), false, false);
        const float inv = 1.0f / __uint_as_float(lr[1]);
        const int tok = qb * 256 + qg * 64 + kh * 32 + r;
        const unsigned base = WS_O + (tok >> 4) * OH_TILE + (tok & 15) * 16;
        unsigned pk[3][2];
#pragma unroll
        for (int u = 0; u < 3; ++u) { pk[u][0] = cvtpk(of[u][0] * inv, of[u][1] * inv); pk[u][1] = cvtpk(of[u][2] * inv, of[u][3] * inv); }
        auto oaddr = [&](int col) { return base + (col >> 5) * 1024 + ((col >> 3) & 3) * 256 + (col & 7) * 2; };
        if ((h & 1) == 0) {
            auto s0 = __builtin_amdgcn_permlane32_swap(pk[0][0], pk[1][0], false, false);
            auto s1 = __builtin_amdgcn_permlane32_swap(pk[0][1], pk[1][1], false, false);
            st128(k.rs, oaddr(h * 20 + 8 * hi), (u32x4){s0[0], s1[0], s0[1], s1[1]});
            if (!hi) st64(k.rs, oaddr(h * 20 + 16), (u32x2){pk[2][0], pk[2][1]});
        } else {
            auto s0 = __builtin_amdgcn_permlane32_swap(pk[0][0], pk[2][0], false, false);
            auto s1 = __builtin_amdgcn_permlane32_swap(pk[0][1], pk[2][1], false, false);
            st128(k.rs, oaddr(h * 20 + 4 + 8 * hi), hi ? (u32x4){pk[1][0], pk[1][1], s0[0], s1[0]} : (u32x4){s0[1], s1[1], pk[1][0], pk[1][1]});
            if (!hi) st64(k.rs, oaddr(h * 20), (u32x2){s0[0], s1[0]});
        }
    }
}

DI int own_cf(int kh, int u) { return kh ? (u < 2 ? 3 + u : 4) : u; }
constexpr int RED_LDS = 143360;
struct P3Pre { u32x2 xv[5]; };
DI void p3_pre(const Ctx& k, int rb64, int ct, P3Pre& p) {
    const int tt = k.w & 3, t = k.lane & 15, g = k.lane >> 4;
    if (k.w < 4) {
#pragma unroll
        for (int cf = 0; cf < 5; ++cf) p.xv[cf] = *(const u32x2*)(k.x + (size_t)(rb64 * 64 + tt * 16 + t) * D + ct * 80 + cf * 16 + 4 * g);
    }
}
DI void w13_prefetch(const Ctx& k, int f0, int f);
DI void p3_phase(const Ctx& k, int rb64, int ct, int f0, const P3Pre& pre, u32x2 (&hv)[3]) {
    const int lane = k.lane, tt = k.w & 3, kh = k.w >> 2, t = lane & 15, g = lane >> 4;
    const unsigned ap = WS_O + (rb64 * 4 + tt) * OH_TILE + kh * 10240 + lane * 16;
    const lds_ptr wl = k.lds + WO_LDS + kh * 51200 + lane * 16;
    constexpr int WIN = 5;
    u32x4 a[10];
#pragma unroll
    for (int i = 0; i < WIN; ++i) a[i] = ld128(k.rs, ap + i * 1024);
    bf16x8 wf[2][5];
#pragma unroll
    for (int cf = 0; cf < 5; ++cf) wf[0][cf] = as_frag(lds_r128(wl + cf * 1024));
    f32x4 acc[5];
#pragma unroll
    for (int cf = 0; cf < 5; ++cf) { acc[cf] = (f32x4){0, 0, 0, 0}; PIN(acc[cf]); }
    mfma_fence();
    PRIO(1);
#pragma unroll
    for (int i = 0; i < 10; ++i) {
        if (i + WIN < 10) { a[i + WIN] = ld128(k.rs, ap + (i + WIN) * 1024); SBAR(); }
#pragma unroll
        for (int cf = 0; cf < 5; ++cf) {
            mfma16_acc(acc[cf], wf[i & 1][cf], as_frag(a[i])); SBAR();
            if (i + 1 < 10) wf[(i + 1) & 1][cf] = as_frag(lds_r128(wl + (i + 1) * 5120 + cf * 1024));
            if (cf < 4) PIN(wf[i & 1][cf + 1]); else if (i + 1 < 10) PIN(wf[(i + 1) & 1][0]);
            SBAR();
        }
    }
    PRIO(0);
    mfma_fence();
    STAMP(10);
    BAR_L();
    const lds_ptr red = k.lds + RED_LDS + tt * 5120 + lane * 16;
    if (kh) {
#pragma unroll
        for (int cf = 0; cf < 5; ++cf) *(LAS f32x4*)(red + cf * 1024) = acc[cf];
        BAR_L();
        w13_prefetch(k, f0, k.w - 1);
    } else {
        BAR_L();
        const int tok = rb64 * 64 + tt * 16 + t;
        float s = 0.f, q = 0.f;
        u32x2 hq[5];
#pragma unroll
        for (int cf = 0; cf < 5; ++cf) {
            const f32x4 v = acc[cf] + *(const LAS f32x4*)(red + cf * 1024);
            hq[cf] = (u32x2){cvtpk(v[0] + bflo(pre.xv[cf].x), v[1] + bfhi(pre.xv[cf].x)), cvtpk(v[2] + bflo(pre.xv[cf].y), v[3] + bfhi(pre.xv[cf].y))};
            const int col = ct * 80 + cf * 16 + 4 * g;
            st64(k.rs, WS_H + (tok >> 4) * OH_TILE + (col >> 5) * 1024 + ((col >> 3) & 3) * 256 + t * 16 + (col & 7) * 2, hq[cf]);
            const float h0 = bflo(hq[cf].x), h1 = bfhi(hq[cf].x), h2 = bflo(hq[cf].y), h3 = bfhi(hq[cf].y);
            s += (h0 + h1) + (h2 + h3); q += (h0 * h0 + h1 * h1) + (h2 * h2 + h3 * h3);
        }
        s = sum_x32(sum_x16(s)); q = sum_x32(sum_x16(q));
        if (g == 0) st64(k.rs, WS_PST + (tok >> 4) * 2048 + (ct * 2) * 128 + t * 8, (u32x2){__float_as_uint(s), __float_as_uint(q)});
        if (g == 1) st64(k.rs, WS_PST + (tok >> 4) * 2048 + (ct * 2 + 1) * 128 + t * 8, (u32x2){0u, 0u});
        hv[0] = hq[0]; hv[1] = hq[1]; hv[2] = hq[2];
        const lds_ptr stash = k.lds + RED_LDS + tt * 5120 + lane * 8;
        *(LAS u32x2*)(stash + 3072) = hq[3]; *(LAS u32x2*)(stash + 4096) = hq[4];
    }
}
DI void pair_reduce(lds_ptr red0, int tt, int kh, int lane, const f32x4 (&acc)[5], f32x4 (&own)[3]) {
    const lds_ptr red = red0 + tt * 5120 + lane * 16;
    if (kh == 0) { *(LAS f32x4*)(red + 3072) = acc[3]; *(LAS f32x4*)(red + 4096) = acc[4]; }
    else { *(LAS f32x4*)(red) = acc[0]; *(LAS f32x4*)(red + 1024) = acc[1]; *(LAS f32x4*)(red + 2048) = acc[2]; }
    BAR_L();
    if (kh == 0) {
#pragma unroll
        for (int u = 0; u < 3; ++u) own[u] = acc[u] + *(const LAS f32x4*)(red + u * 1024);
    } else {
        own[0] = acc[3] + *(const LAS f32x4*)(red + 3072); own[1] = acc[4] + *(const LAS f32x4*)(red + 4096); own[2] = own[1];
    }
}
template <int N, int NP>
DI void w2_stage(const Ctx& k, int ct, int b, int s0, int w0) {
    const int nw = 8 - w0;
#pragma unroll
    for (int j = 0; j < NP; ++j) {
        int q = k.w - w0 + nw * j;
        if (q >= 10 * N) q -= nw;
        const int hf = q >= 5 * N, rem = q - hf * 5 * N, n = rem / 5;
        glds16<true>(k.lane * 16, k.ws + WS_W2 + (ct * 54 + hf * 27 + s0) * 5120 + rem * 1024, k.lds0 + n * 20480 + b * 10240 + hf * 5120 + (rem - 5 * n) * 1024);
    }
}
DI void p5_phase(const Ctx& k, int rb64, int ct, u32x2 (&hv)[3]) {
    const int lane = k.lane, tt = k.w & 3, kh = k.w >> 2, t = lane & 15, g = lane >> 4;
    const unsigned ap = WS_HH + (rb64 * 4 + tt) * HH_TILE + kh * 27 * 1024 + lane * 16;
    constexpr int WIN = 6;
    u32x4 a[27];
#pragma unroll
    for (int i = 0; i < WIN; ++i) ald128(a[i], k.rsw, ap, i * 1024);
    if (kh) {
        const lds_ptr stash = k.lds + RED_LDS + tt * 5120 + lane * 8;
        hv[0] = *(const LAS u32x2*)(stash + 3072); hv[1] = *(const LAS u32x2*)(stash + 4096); hv[2] = hv[1];
    }
    lds_ptr wb[3] = {k.lds + lane * 16 + kh * 5120, k.lds + lane * 16 + kh * 5120 + 61440, k.lds + lane * 16 + kh * 5120 + 122880};
    PIN(wb[0]); PIN(wb[1]); PIN(wb[2]);
    auto wread = [&](int n, int cf) {
        const int m = n % 7;
        return as_frag(lds_r128(wb[m / 3] + (m % 3) * 20480 + ((n / 7) & 1) * 10240 + cf * 1024));
    };
    bf16x8 wf[2][5];
#pragma unroll
    for (int cf = 0; cf < 5; ++cf) wf[0][cf] = wread(0, cf);
    f32x4 acc[5];
#pragma unroll
    for (int cf = 0; cf < 5; ++cf) { acc[cf] = (f32x4){0, 0, 0, 0}; PIN(acc[cf]); }
    mfma_fence();
    PRIO(1);
#define P5_STEP(i, NW) do { \
        if ((i) == 7) { BAR_L(); w2_stage<7, 9>(k, ct, 0, 14, 0); } \
        if ((i) == 13) { WAIT_BAR(6); w2_stage<6, 8>(k, ct, 1, 21, 0); } \
        if ((i) == 20) { WAIT_BAR(6); } \
        if ((i) + WIN < 27) ald128(a[(i) + WIN], k.rsw, ap, ((i) + WIN) * 1024); \
        ald_wait<NW>(a[i]); SBAR(); \
        _Pragma("unroll") for (int cf = 0; cf < 5; ++cf) { \
            mfma16_acc(acc[cf], wf[(i) & 1][cf], as_frag(a[i])); SBAR(); \
            if ((i) + 1 < 27) wf[((i) + 1) & 1][cf] = wread((i) + 1, cf); \
            if (cf < 4) PIN(wf[(i) & 1][cf + 1]); else if ((i) + 1 < 27) PIN(wf[((i) + 1) & 1][0]); \
            SBAR(); } } while (0)
    P5_STEP(0, 6); P5_STEP(1, 6); P5_STEP(2, 6); P5_STEP(3, 6); P5_STEP(4, 6); P5_STEP(5, 6); P5_STEP(6, 6);
    P5_STEP(7, 15); P5_STEP(8, 15); P5_STEP(9, 15); P5_STEP(10, 15); P5_STEP(11, 15); P5_STEP(12, 15);
    P5_STEP(13, 14); P5_STEP(14, 14); P5_STEP(15, 14); P5_STEP(16, 14); P5_STEP(17, 14); P5_STEP(18, 14); P5_STEP(19, 6);
    P5_STEP(20, 6); P5_STEP(21, 5); P5_STEP(22, 4); P5_STEP(23, 3); P5_STEP(24, 2); P5_STEP(25, 1); P5_STEP(26, 0);
#undef P5_STEP
    PRIO(0);
    mfma_fence();
    STAMP(16);
    f32x4 own[3];
    pair_reduce(k.lds + RED_LDS, tt, kh, lane, acc, own);
    const int tok = rb64 * 64 + tt * 16 + t;
#pragma unroll
    for (int u = 0; u < 3; ++u) {
        if (u < 2 || kh == 0) {
            const int col = ct * 80 + own_cf(kh, u) * 16 + 4 * g;
            *(u32x2*)(k.out + (size_t)tok * D + col) = (u32x2){cvtpk(own[u][0] + bflo(hv[u].x), own[u][1] + bfhi(hv[u].x)), cvtpk(own[u][2] + bflo(hv[u].y), own[u][3] + bfhi(hv[u].y))};
        }
    }
}

constexpr int P4_CS = 143360;
DI void w13_prefetch(const Ctx& k, int f0, int f) {
    const int p0 = f * 20;
#pragma unroll 4
    for (int j = 0; j < 20; ++j) glds16<true>(k.lane * 16, k.ws + WS_W13 + f0 * 20480 + (p0 + j) * 1024, k.lds0 + (p0 + j) * 1024);
}
DI void p4_colsum(const Ctx& k, int f) {
    const int t = k.lane & 15, g = k.lane >> 4;
    u32x4 wv[20];
#pragma unroll
    for (int s = 0; s < 20; ++s) wv[s] = lds_r128(k.lds + (f * 20 + s) * 1024 + k.lane * 16);
    f32x4 c0 = {0, 0, 0, 0}, c1 = {0, 0, 0, 0};
    const bf16x8 ones = {0x3F80, 0x3F80, 0x3F80, 0x3F80, 0x3F80, 0x3F80, 0x3F80, 0x3F80};
#pragma unroll
    for (int s = 0; s < 20; s += 2) { c0 = mfma16(as_frag(wv[s]), ones, c0); c1 = mfma16(as_frag(wv[s + 1]), ones, c1); }
    if (t == 0) *(LAS f32x4*)(k.lds + P4_CS + f * 64 + g * 16) = c0 + c1;
}
DI void p4_phase(const Ctx& k, int rb, int f0, int nf, int ct5) {
    const int lane = k.lane, w = k.w, t = lane & 15, g = lane >> 4;
    const lds_ptr lds = k.lds;
    const unsigned ap = WS_H + (rb * 16 + w * 2) * OH_TILE + lane * 16;
    u32x4 a0[20], a1[20];
#define P4_LD(s) do { a0[s] = ld128(k.rs, ap + (s) * 1024); a1[s] = ld128(k.rs, ap + OH_TILE + (s) * 1024); } while (0)
    u32x2 pv[2][4];
#pragma unroll
    for (int i = 0; i < 2; ++i)
#pragma unroll
        for (int j = 0; j < 4; ++j) pv[i][j] = ld64(k.rs, WS_PST + (rb * 16 + w * 2 + i) * 2048 + (4 * g + j) * 128 + t * 8);
    P4_LD(0); P4_LD(1); P4_LD(2); P4_LD(3);
    lds_ptr wb[3] = {lds + lane * 16, lds + lane * 16 + 61440, lds + lane * 16 + 122880};
    PIN(wb[1]); PIN(wb[2]);
    bf16x8 wf[2][7];
#pragma unroll
    for (int f = 0; f < 7; ++f) wf[0][f] = as_frag(lds_r128(wb[f / 3] + (f % 3) * 20480));
    f32x4 acc[2][7];
#pragma unroll
    for (int f = 0; f < 7; ++f) { acc[0][f] = (f32x4){0, 0, 0, 0}; acc[1][f] = (f32x4){0, 0, 0, 0}; PIN(acc[0][f]); PIN(acc[1][f]); }
    float mean[2], rstd[2], vpe[2];
    mfma_fence();
    STAMP(13);
    PRIO(1);
#pragma unroll
    for (int s = 0; s < 20; ++s) {
        if (s + 4 < 20) P4_LD(s + 4);
        SBAR();
        if (s == 16) { __builtin_amdgcn_s_barrier(); w2_stage<7, 9>(k, ct5, 0, 0, 0); SBAR(); }
        if (s == 4) {
#pragma unroll
            for (int i = 0; i < 2; ++i) {
                float sm = (__uint_as_float(pv[i][0].x) + __uint_as_float(pv[i][1].x)) + (__uint_as_float(pv[i][2].x) + __uint_as_float(pv[i][3].x));
                float q = (__uint_as_float(pv[i][0].y) + __uint_as_float(pv[i][1].y)) + (__uint_as_float(pv[i][2].y) + __uint_as_float(pv[i][3].y));
                sm = sum_x32(sum_x16(sm)); q = sum_x32(sum_x16(q));
                mean[i] = sm * (1.0f / D);
                vpe[i] = fmaxf(q * (1.0f / D) - mean[i] * mean[i], 0.f) + LN_EPS;
                rstd[i] = __builtin_amdgcn_rsqf(vpe[i]);
            }
            PIN(mean[0]); PIN(mean[1]); PIN(rstd[0]); PIN(rstd[1]); PIN(vpe[0]); PIN(vpe[1]);
            SBAR();
        }
#pragma unroll
        for (int f = 0; f < 7; ++f) {
            mfma16_acc(acc[0][f], wf[s & 1][f], as_frag(a0[s])); SBAR();
            if (s + 1 < 20) wf[(s + 1) & 1][f] = as_frag(lds_r128(wb[f / 3] + (f % 3) * 20480 + (s + 1) * 1024));
            if (f < 6) PIN(wf[s & 1][f + 1]); else if (s + 1 < 20) PIN(wf[(s + 1) & 1][0]);
            SBAR();
            mfma16_acc(acc[1][f], wf[s & 1][f], as_frag(a1[s])); SBAR();
        }
    }
#undef P4_LD
    PRIO(0);
    mfma_fence();
    STAMP(14);
    f32x4 cs[7];
#pragma unroll
    for (int f = 0; f < 7; ++f) cs[f] = *(const LAS f32x4*)(lds + P4_CS + f * 64 + g * 16);
#pragma unroll
    for (int i = 0; i < 2; ++i) {
        const float ce = -LOG2E * rstd[i], ir2 = vpe[i], nm = -mean[i];
        const f32x2 ce2 = {ce, ce}, ir22 = {ir2, ir2}, nm2 = {nm, nm};
        unsigned d[8];
#pragma unroll
        for (int f = 0; f < 7; ++f) {
            const f32x2 ga = __builtin_elementwise_fma(nm2, __builtin_shufflevector(cs[f], cs[f], 0, 1), __builtin_shufflevector(acc[i][f], acc[i][f], 0, 1));
            const f32x2 ua = __builtin_elementwise_fma(nm2, __builtin_shufflevector(cs[f], cs[f], 2, 3), __builtin_shufflevector(acc[i][f], acc[i][f], 2, 3));
            const f32x2 t2 = ga * ce2;
            const f32x2 e2 = {EX(t2.x), EX(t2.y)};
            const f32x2 d2 = __builtin_elementwise_fma(e2, ir22, ir22);
            const f32x2 r2 = {__builtin_amdgcn_rcpf(d2.x), __builtin_amdgcn_rcpf(d2.y)};
            const f32x2 o2 = (ga * ua) * r2;
            d[f] = cvtpk(o2.x, o2.y);
        }
        d[7] = 0;
        const unsigned dst = WS_HH + (rb * 16 + w * 2 + i) * HH_TILE + f0 * 256 + lane * 16;
#pragma unroll
        for (int u = 0; u < 2; ++u) {
            auto a = __builtin_amdgcn_permlane32_swap(d[4 * u], d[4 * u + 2], false, false);
            auto b = __builtin_amdgcn_permlane32_swap(d[4 * u + 1], d[4 * u + 3], false, false);
            auto e = __builtin_amdgcn_permlane16_swap(a[0], b[0], false, false);
            auto f = __builtin_amdgcn_permlane16_swap(a[1], b[1], false, false);
            if (4 * u + g < nf) st128(k.rs, dst + u * 1024, (u32x4){e[0], e[1], f[0], f[1]});
        }
    }
}

constexpr int LDS_BYTES = 163840;
__global__ __launch_bounds__(512, 2) void fused_block(const uint16_t* __restrict__ x, const uint16_t* __restrict__ wqkv, const uint16_t* __restrict__ wo,
                                                      const uint16_t* __restrict__ w1, const uint16_t* __restrict__ w3, const uint16_t* __restrict__ w2,
                                                      char* __restrict__ ws, uint16_t* __restrict__ out) {
    extern __shared__ __attribute__((aligned(16))) char smem[];
    Ctx k;
    k.x = x; k.wqkv = wqkv; k.wo = wo; k.w1 = w1; k.w3 = w3; k.w2 = w2; k.ws = ws; k.out = out;
    k.rs = __builtin_amdgcn_make_buffer_rsrc(ws, 0, WS_END + 20480, 0x00020000);
    k.lds = (lds_ptr)smem; k.lds0 = (unsigned)(uintptr_t)smem;
    k.c = blockIdx.x; k.xcd = k.c & 7; k.lb = k.c >> 3;
    k.tid = threadIdx.x; k.lane = k.tid & 63; k.w = rfl(k.tid >> 6);
    k.rsw = (u32x4){(unsigned)(uintptr_t)ws, (unsigned)((uintptr_t)ws >> 32) & 0xffffu, WS_END + 20480, 0x00020000u};
    k.tag = 0;
    k.sc = 0; k.sr = 0;
    STAMP(0);
    startup_load(k);
    const int rb1 = (k.xcd >> 2) * 8 + (k.lb & 7), cb1 = (k.xcd & 3) * 4 + (k.lb >> 3);
    if (cb1 < 15) {
        if (cb1 >= 10) p1_body<true>(k, rb1, cb1); else p1_body<false>(k, rb1, cb1);
        STAMP(3);
        WAIT_BAR(0);
        if (k.tid == 0) st_flag(ws + WS_F1 + (rb1 * 16 + cb1) * FLAG_STRIDE, k.tag);
    } else {
        WAIT_VM(0);
        startup_tag(k);
        ln1_stats(k, rb1);
        if (rb1 < 15) colsum_qkv(k, rb1);
        WAIT_BAR(0);
    }
    STAMP(4);
    if (k.w) pretile_load(k);
    const int h = k.xcd * 4 + (k.lb & 3), qb = k.lb >> 2;
    const int rb64 = k.xcd * 4 + (k.lb >> 3), ct = k.lb & 7;
    const int rb4 = (k.xcd >> 1) * 2 + (k.lb >> 4), cb4 = (k.xcd & 1) * 16 + (k.lb & 15);
    const int f0 = cb4 < 24 ? cb4 * 7 : 168 + (cb4 - 24) * 6, nf = cb4 < 24 ? 7 : 6;
    AttnCtx ac;
    attn_setup(k, ac, h);
    if (k.w == 0) {
        const int c0 = (20 * h) >> 7, c1 = (20 * h + 19) >> 7, j = k.lane & 15, sel = k.lane >> 4;
        const int cbk = (sel & 1) ? c1 : c0;
        const unsigned o0 = WS_F1 + (j * 16 + (sel < 2 ? 5 : 10) + cbk) * FLAG_STRIDE;
        const unsigned o1 = WS_F1 + ((2 * qb + (k.lane & 1)) * 16 + ((k.lane & 2) ? c1 : c0)) * FLAG_STRIDE;
        poll_flags2(k, o0, o1);
    }
    WAIT_BAR(0);
    STAMP(5);
    p2_phase(k, ac, h, qb, ct);
    STAMP(8);
    WAIT_BAR(0);
    if (k.tid == 0) st_flag(ws + WS_F2 + (qb * 32 + h) * FLAG_STRIDE, k.tag);
    P3Pre pre;
    p3_pre(k, rb64, ct, pre);
    if (k.w == 0) poll_flags(k, WS_F2 + (k.xcd * 32 + (k.lane & 31)) * FLAG_STRIDE);
    else if (k.w <= 3) w13_prefetch(k, f0, k.w - 1);
    BAR_L();
    if (k.c == 0 && k.tid == 0) st_flag(ws + WS_EPOCH, k.tag);
    STAMP(9);
    u32x2 hv[3];
    p3_phase(k, rb64, ct, f0, pre, hv);
    STAMP(11);
    if (k.w < 4) WAIT_VM(0);
    BAR_L();
    if (k.tid == 0) st_flag(ws + WS_F3 + (k.xcd * 32 + k.lb) * FLAG_STRIDE, k.tag);
    if (k.w == 0) poll_flags(k, WS_F3 + (rb4 * 32 + (k.lane & 31)) * FLAG_STRIDE);
    else {
        WAIT_VM(0);
        p4_colsum(k, k.w - 1);
    }
    WAIT_BAR(0);
    STAMP(12);
    p4_phase(k, rb4, f0, nf, ct);
    STAMP(15);
    WAIT_BAR(0);
    if (k.tid == 0) st_flag(ws + WS_F4 + (rb4 * 32 + cb4) * FLAG_STRIDE, k.tag);
    if (k.w == 0) poll_flags(k, WS_F4 + ((rb64 >> 2) * 32 + (k.lane & 31)) * FLAG_STRIDE);
    else {
        w2_stage<7, 10>(k, ct, 1, 7, 1);
        const int li = (k.w - 1) * 64 + k.lane;
        if (k.w <= 5) {
            const int ln = (k.lb >> 3) * 260 + (li < 260 ? li : li - 64), hf = ln >= 520;
            const unsigned warm = ld32(k.rs, WS_W2 + (ct * 54 + hf * 27 + 14) * 5120 + (ln - hf * 520) * 128);
            asm volatile("" :: "v"(warm));
        }
    }
    WAIT_BAR(0);
    STAMP(17);
    p5_phase(k, rb64, ct, hv);
    STAMP(18);
    STAMP_FLUSH();
}

extern "C" void kernel_launch(void* const* d_in, const int* in_sizes, int n_in, void* d_out, int out_size, void* d_ws, size_t ws_size, hipStream_t stream) {
    (void)hipFuncSetAttribute((const void*)fused_block, hipFuncAttributeMaxDynamicSharedMemorySize, LDS_BYTES);
    fused_block<<<256, 512, LDS_BYTES, stream>>>((const uint16_t*)d_in[0], (const uint16_t*)d_in[1], (const uint16_t*)d_in[2], (const uint16_t*)d_in[4],
                                                 (const uint16_t*)d_in[5], (const uint16_t*)d_in[6], (char*)d_ws, (uint16_t*)d_out);
}
```

```cpp
#include <hip/hip_runtime.h>
#include <hip/hip_bf16.h>
#include <stdint.h>

#define DI __device__ __forceinline__
#define LAS __attribute__((address_space(3)))
#define GAS __attribute__((address_space(1)))
typedef short bf16x8 __attribute__((ext_vector_type(8)));
typedef short s16x4 __attribute__((ext_vector_type(4)));
typedef float f32x2 __attribute__((ext_vector_type(2)));
typedef float f32x4 __attribute__((ext_vector_type(4)));
typedef float f32x16 __attribute__((ext_vector_type(16)));
typedef unsigned u32x2 __attribute__((ext_vector_type(2)));
typedef unsigned u32x4 __attribute__((ext_vector_type(4)));
typedef unsigned long long u64;
typedef __bf16 bf16x2_t __attribute__((ext_vector_type(2)));
typedef short v4i16_t __attribute__((ext_vector_type(4)));
typedef LAS char* lds_ptr;
typedef __amdgpu_buffer_rsrc_t rsrc_t;

constexpr int S = 2048, D = 640, H = 32, DH = 20, F = 1728, NQKV = 3 * D;
constexpr float LN_EPS = 1e-5f;
constexpr float LOG2E = 1.4426950408889634f;
constexpr float QSCALE = 0.22360679774997896f * LOG2E;

constexpr unsigned WS_EPOCH = 0;
constexpr unsigned FLAG_STRIDE = 64, FLAG_ARR = 256 * FLAG_STRIDE;
constexpr unsigned WS_F1 = 4096, WS_FP = WS_F1 + FLAG_ARR, WS_F2 = WS_FP + FLAG_ARR, WS_F3 = WS_F2 + FLAG_ARR, WS_F4 = WS_F3 + FLAG_ARR;
constexpr unsigned WS_ST1 = 131072;
constexpr unsigned WS_CSQ = WS_ST1 + 32768;
constexpr unsigned WS_PST = 262144;
constexpr unsigned WS_Q = 524288;
constexpr unsigned IMG = S * D * 2;
constexpr unsigned QK_TILE = 32 * 1280, QK_IMG = 64 * QK_TILE;
constexpr unsigned WS_K = WS_Q + QK_IMG;
constexpr unsigned WS_V = WS_K + QK_IMG;
constexpr unsigned OH_TILE = 21504, HH_TILE = 56320;
constexpr unsigned WS_O = WS_V + IMG;
constexpr unsigned WS_H = WS_O + 128 * OH_TILE;
constexpr unsigned WS_HH = WS_H + 128 * OH_TILE;
constexpr unsigned WS_WO = WS_HH + 128 * HH_TILE;
constexpr unsigned WS_W13 = WS_WO + D * D * 2;
constexpr unsigned WS_W2 = WS_W13 + D * F * 4;
constexpr unsigned WS_END = WS_W2 + F * D * 2;

DI unsigned cvtpk(float lo, float hi) { f32x2 v = {lo, hi}; bf16x2_t b = __builtin_convertvector(v, bf16x2_t); return __builtin_bit_cast(unsigned, b); }
DI float bflo(unsigned u) { return __uint_as_float(u << 16); }
DI float bfhi(unsigned u) { return __uint_as_float(u & 0xffff0000u); }
DI f32x4 mfma16(bf16x8 a, bf16x8 b, f32x4 c) { return __builtin_amdgcn_mfma_f32_16x16x32_bf16(a, b, c, 0, 0, 0); }
DI void mfma16_acc(f32x4& c, bf16x8 a, bf16x8 b) { asm volatile("v_mfma_f32_16x16x32_bf16 %0, %1, %2, %0" : "+v"(c) : "v"(a), "v"(b)); }
DI void mfma_fence() { __builtin_amdgcn_sched_barrier(0); asm volatile("s_nop 7\n\ts_nop 1" ::: "memory"); __builtin_amdgcn_sched_barrier(0); }
DI f32x16 mfma32(bf16x8 a, bf16x8 b, f32x16 c) { return __builtin_amdgcn_mfma_f32_32x32x16_bf16(a, b, c, 0, 0, 0); }
DI int rfl(int x) { return __builtin_amdgcn_readfirstlane(x); }
template <bool SC1>
DI void glds16(unsigned voff, const void* sbase, unsigned ldsdst) {
    unsigned keep;
    if (SC1) asm volatile("s_mov_b32 %0, m0\n\ts_mov_b32 m0, %3\n\ts_nop 0\n\tglobal_load_lds_dwordx4 %1, %2 sc1\n\ts_mov_b32 m0, %0" : "=&s"(keep) : "v"(voff), "s"(sbase), "s"(ldsdst) : "memory");
    else asm volatile("s_mov_b32 %0, m0\n\ts_mov_b32 m0, %3\n\ts_nop 0\n\tglobal_load_lds_dwordx4 %1, %2\n\ts_mov_b32 m0, %0" : "=&s"(keep) : "v"(voff), "s"(sbase), "s"(ldsdst) : "memory");
}
DI void glds4(const void* gptr, unsigned ldsdst) {
    unsigned keep;
    asm volatile("s_mov_b32 %0, m0\n\ts_mov_b32 m0, %2\n\ts_nop 0\n\tglobal_load_lds_dword %1, off\n\ts_mov_b32 m0, %0" : "=&s"(keep) : "v"(gptr), "s"(ldsdst) : "memory");
}
#define WAIT_VM(n) asm volatile("s_waitcnt vmcnt(" #n ")" ::: "memory")
#define WAIT_BAR(n) asm volatile("s_waitcnt vmcnt(" #n ") lgkmcnt(0)\n\ts_barrier" ::: "memory")
#define BAR_L() asm volatile("s_waitcnt lgkmcnt(0)\n\ts_barrier" ::: "memory")
#define SBAR() __builtin_amdgcn_sched_barrier(0)
#define PIN(x) asm volatile("" : "+v"(x))
#define PRIO(n) __builtin_amdgcn_s_setprio(n)
#define EX(v) __builtin_amdgcn_exp2f(v)
DI u32x4 lds_r128(lds_ptr p) { return *(const LAS u32x4*)p; }
DI s16x4 lds_tr(lds_ptr p) { return __builtin_bit_cast(s16x4, __builtin_amdgcn_ds_read_tr16_b64_v4i16((LAS v4i16_t*)p)); }
DI bf16x8 as_frag(u32x4 v) { return __builtin_bit_cast(bf16x8, v); }
DI bf16x8 cat_frag(s16x4 a, s16x4 b) { return (bf16x8){a[0], a[1], a[2], a[3], b[0], b[1], b[2], b[3]}; }
DI float swap32_max(float m) { auto r = __builtin_amdgcn_permlane32_swap(__float_as_uint(m), __float_as_uint(m), false, false); return fmaxf(__uint_as_float(r[0]), __uint_as_float(r[1])); }
DI float sum_x16(float v) { auto r = __builtin_amdgcn_permlane16_swap(__float_as_uint(v), __float_as_uint(v), false, false); return __uint_as_float(r[0]) + __uint_as_float(r[1]); }
DI float sum_x32(float v) { auto r = __builtin_amdgcn_permlane32_swap(__float_as_uint(v), __float_as_uint(v), false, false); return __uint_as_float(r[0]) + __uint_as_float(r[1]); }
DI u32x4 ld128(rsrc_t r, unsigned off) { return __builtin_amdgcn_raw_buffer_load_b128(r, off, 0, 16); }
DI u32x2 ld64(rsrc_t r, unsigned off) { return __builtin_amdgcn_raw_buffer_load_b64(r, off, 0, 16); }
DI unsigned ld32(rsrc_t r, unsigned off) { return __builtin_amdgcn_raw_buffer_load_b32(r, off, 0, 16); }
DI void st128(rsrc_t r, unsigned off, u32x4 v) { __builtin_amdgcn_raw_buffer_store_b128(v, r, off, 0, 16); }
DI void st64(rsrc_t r, unsigned off, u32x2 v) { __builtin_amdgcn_raw_buffer_store_b64(v, r, off, 0, 16); }

DI void ald128(u32x4& v, u32x4 rsw, unsigned voff, unsigned soff) { asm volatile("buffer_load_dwordx4 %0, %1, %2, %3 offen sc1" : "=&v"(v) : "v"(voff), "s"(rsw), "s"(soff) : "memory"); }
template <int N> DI void ald_wait(u32x4& v) { asm volatile("s_waitcnt vmcnt(%1)" : "+v"(v) : "i"(N) : "memory"); }
struct Ctx {
    const uint16_t *x, *wqkv, *wo, *w1, *w3, *w2;
    char* ws; uint16_t* out;
    rsrc_t rs; u32x4 rsw; u64 tag; lds_ptr lds; unsigned lds0;
    int c, xcd, lb, tid, lane, w;
    mutable unsigned sc, sr;
};

#define STAMP(i)
#define STAMP_FLUSH()

DI u64 ld_flag(const char* p) { return __hip_atomic_load((const GAS u64*)p, __ATOMIC_RELAXED, __HIP_MEMORY_SCOPE_AGENT); }
DI void st_flag(char* p, u64 v) { __hip_atomic_store((GAS u64*)p, v, __ATOMIC_RELAXED, __HIP_MEMORY_SCOPE_AGENT); }
constexpr unsigned SPIN_CAP = 1u << 18;
DI void poll_flags(const Ctx& k, unsigned off) {
    u64 a = ld_flag(k.ws + off);
    __builtin_amdgcn_s_sleep(8);
    for (unsigned spins = 0; spins < SPIN_CAP; ++spins) {
        const u64 b = ld_flag(k.ws + off);
        if (__all(a == k.tag)) break;
        a = ld_flag(k.ws + off);
        if (__all(b == k.tag)) break;
    }
}
DI void poll_flags2(const Ctx& k, unsigned off0, unsigned off1) {
    u64 a0 = ld_flag(k.ws + off0), a1 = ld_flag(k.ws + off1);
    __builtin_amdgcn_s_sleep(8);
    for (unsigned spins = 0; spins < SPIN_CAP; ++spins) {
        const u64 b0 = ld_flag(k.ws + off0), b1 = ld_flag(k.ws + off1);
        if (__all(a0 == k.tag && a1 == k.tag)) break;
        a0 = ld_flag(k.ws + off0); a1 = ld_flag(k.ws + off1);
        if (__all(b0 == k.tag && b1 == k.tag)) break;
    }
}
DI void poll_all(const Ctx& k, unsigned arr) {
    for (unsigned spins = 0; spins < SPIN_CAP; ++spins) {
        const unsigned o = arr + k.lane * FLAG_STRIDE;
        const u64 a = ld_flag(k.ws + o), b = ld_flag(k.ws + o + 64 * FLAG_STRIDE), c = ld_flag(k.ws + o + 128 * FLAG_STRIDE), d = ld_flag(k.ws + o + 192 * FLAG_STRIDE);
        if (__all(a == k.tag && b == k.tag && c == k.tag && d == k.tag)) break;
        __builtin_amdgcn_s_sleep(1);
    }
}

constexpr int SU_LDS = 131072;
DI void startup_load(const Ctx& k) { glds4(k.ws + WS_EPOCH + (k.lane & 1) * 4, k.lds0 + SU_LDS + k.w * 256); }
DI void startup_tag(Ctx& k) {
    const u32x2 e = *(const LAS u32x2*)(k.lds + SU_LDS + k.w * 256);
    k.tag = (((u64)(unsigned)rfl(e.y) << 32) | (unsigned)rfl(e.x)) + 1;
}
DI void ln1_stats(const Ctx& k, int j) {
    u32x4 v[2][10];
#pragma unroll
    for (int p = 0; p < 2; ++p)
#pragma unroll
        for (int i = 0; i < 10; ++i) v[p][i] = *(const u32x4*)(k.x + (size_t)(128 * j + 16 * k.w + 8 * p + (k.lane >> 3)) * D + ((k.lane & 7) + 8 * i) * 8);
#pragma unroll
    for (int p = 0; p < 2; ++p) {
        float s = 0.f, q = 0.f;
#pragma unroll
        for (int i = 0; i < 10; ++i)
#pragma unroll
            for (int e = 0; e < 4; ++e) { const float a = bflo(v[p][i][e]), b = bfhi(v[p][i][e]); s += a + b; q = fmaf(a, a, fmaf(b, b, q)); }
#pragma unroll
        for (int o = 4; o >= 1; o >>= 1) { s += __shfl_xor(s, o); q += __shfl_xor(q, o); }
        const float mean = s * (1.0f / D), var = fmaxf(q * (1.0f / D) - mean * mean, 0.f);
        const float val = (k.lane & 1) ? 1.0f / sqrtf(var + LN_EPS) : mean;
        const int row = 128 * j + 16 * k.w + 8 * p + (k.lane >> 3);
        if ((k.lane & 7) < 2) st64(k.rs, WS_ST1 + row * 16 + (k.lane & 1) * 8, (u32x2){__float_as_uint(val), (unsigned)k.tag});
    }
}
DI void colsum_qkv(const Ctx& k, int cb) {
    float a[8] = {0, 0, 0, 0, 0, 0, 0, 0};
    u32x4 v[20];
#pragma unroll
    for (int i = 0; i < 20; ++i) v[i] = *(const u32x4*)(k.wqkv + (size_t)(k.w * 80 + i * 4 + (k.lane >> 4)) * NQKV + cb * 128 + (k.lane & 15) * 8);
#pragma unroll
    for (int i = 0; i < 20; ++i)
#pragma unroll
        for (int j = 0; j < 4; ++j) { a[2 * j] += bflo(v[i][j]); a[2 * j + 1] += bfhi(v[i][j]); }
    LAS float* red = (LAS float*)k.lds;
#pragma unroll
    for (int j = 0; j < 8; ++j) { a[j] += __shfl_xor(a[j], 16); a[j] += __shfl_xor(a[j], 32); }
    if (k.lane < 16) {
#pragma unroll
        for (int j = 0; j < 8; ++j) red[k.w * 128 + k.lane * 8 + j] = a[j];
    }
    BAR_L();
    if (k.tid < 128) {
        float t = 0.f;
#pragma unroll
        for (int i = 0; i < 8; ++i) t += red[i * 128 + k.tid];
        st64(k.rs, WS_CSQ + (cb * 128 + k.tid) * 8, (u32x2){__float_as_uint(t), (unsigned)k.tag});
    }
}

constexpr int PT_LDS = 131072;
DI int pt_off(int i) { return PT_LDS + (i < 3 ? i * 8192 : 24576 + (i - 3) * 4096); }
DI void pretile_load(const Ctx& k) {
    const int wv = k.w - 1;
#pragma unroll
    for (int i = 0; i < 5; ++i) {
        const int u = k.c + 256 * i;
        if (u < 540) {
            const int s = u % 20, fb = u / 20;
            const uint16_t* src = ((k.lane & 1) ? k.w3 : k.w1) + fb * 64 + (k.lane >> 1) * 2 + (size_t)32 * s * F;
            for (int r = wv; r < 32; r += 7) glds4(src + (size_t)r * F, k.lds0 + pt_off(i) + r * 256);
        } else {
            const int pc = (i & 1) ? wv - 3 : wv;
            if (pc >= 0 && pc < 4) {
                const bool is2 = u < 1080;
                const int uu = is2 ? u - 540 : u - 1080, s = uu / 10, nb = uu % 10, r = 8 * pc + (k.lane >> 3);
                glds16<false>((32 * s + r) * (D * 2) + nb * 128 + (k.lane & 7) * 16, is2 ? k.w2 : k.wo, k.lds0 + pt_off(i) + pc * 1024);
            }
        }
    }
}
struct PreTile { u32x4 v[5]; };
DI void pretile_read(const Ctx& k, PreTile& p) {
    const int g = k.lane >> 4, q = (k.lane >> 2) & 3, pp = k.lane & 3;
#pragma unroll
    for (int i = 0; i < 5; ++i) {
        const int u = k.c + 256 * i;
        const bool wide = u < 540;
        const int rs = wide ? 256 : 128, f = wide ? k.w : (k.w & 3);
        const lds_ptr a = k.lds + pt_off(i) + (8 * g + q) * rs + f * 32 + pp * 8;
        p.v[i] = __builtin_bit_cast(u32x4, cat_frag(lds_tr(a), lds_tr(a + 4 * rs)));
    }
}
DI void pretile_store(const Ctx& k, const PreTile& p, int i) {
    const int u = k.c + 256 * i;
    unsigned dst;
    if (u < 540) dst = WS_W13 + (((u / 20) * 8 + k.w) * 20 + u % 20) * 1024;
    else {
        const bool is2 = u < 1080;
        const int uu = is2 ? u - 540 : u - 1080, s = uu / 10, cfg = (uu % 10) * 4 + (k.w & 3), ct = cfg / 5, cf = cfg % 5;
        dst = is2 ? WS_W2 + ((ct * 54 + s) * 5 + cf) * 1024 : WS_WO + ((ct * 20 + s) * 5 + cf) * 1024;
    }
    st128(k.rs, dst + k.lane * 16, p.v[i]);
}

constexpr int P1_SLOT = 32768;
struct P1Frag { bf16x8 a[4], b[2]; };
template <bool ISV> DI void p1_mma(f32x4 (&acc)[4][2], const P1Frag& f, int m, int n) { if (ISV) mfma16_acc(acc[m][n], f.a[m], f.b[n]); else mfma16_acc(acc[m][n], f.b[n], f.a[m]); }
template <bool ISV>
DI void p1_body(Ctx& k, int rb, int cb) {
    const int lane = k.lane, w = k.w, th = w >> 2, cg = w & 3, t = lane & 15, g = lane >> 4;
    unsigned voffA[2], voffW[2];
#pragma unroll
    for (int i = 0; i < 2; ++i) {
        const int ra = 8 * (2 * w + i) + (lane >> 3), ca = (lane & 7) ^ ((ra >> 1) & 7);
        voffA[i] = ra * (D * 2) + ca * 16;
        const int rw = 4 * (2 * w + i) + (lane >> 4), fw = (rw & 3) | (((rw >> 3) & 1) << 2), cw = (lane & 15) ^ (fw << 1);
        voffW[i] = rw * (NQKV * 2) + cw * 16;
    }
    const char* gA = (const char*)k.x + (size_t)rb * 128 * D * 2;
    const char* gW = (const char*)k.wqkv + cb * 256;
    auto stage = [&](int kt) {
        const unsigned sl = k.lds0 + (kt & 3) * P1_SLOT + w * 2048;
#pragma unroll
        for (int i = 0; i < 2; ++i) glds16<false>(voffA[i], gA + kt * 128, sl + i * 1024);
#pragma unroll
        for (int i = 0; i < 2; ++i) glds16<false>(voffW[i], gW + (size_t)kt * 64 * NQKV * 2, sl + 16384 + i * 1024);
    };
    stage(0); stage(1); stage(2); stage(3);
    int aoff[2], woff[2];
    aoff[0] = (th * 64 + t) * 128 + ((g ^ (t >> 1)) << 4); aoff[1] = aoff[0] ^ 64;
    const int q4 = (lane >> 2) & 3, p = lane & 3, fw = q4 | ((g & 1) << 2);
#pragma unroll
    for (int n = 0; n < 2; ++n) woff[n] = 16384 + (8 * g + q4) * 256 + (((2 * cg + n) ^ fw) << 5) + p * 8;
    f32x4 acc[4][2];
#pragma unroll
    for (int m = 0; m < 4; ++m) { acc[m][0] = (f32x4){0, 0, 0, 0}; acc[m][1] = (f32x4){0, 0, 0, 0}; PIN(acc[m][0]); PIN(acc[m][1]); }
    mfma_fence();
    P1Frag f0, f1;
#define P1_RA(F_, sl, ks, m) F_.a[m] = as_frag(lds_r128(sl + aoff[ks] + (m) * 2048))
#define P1_RB(F_, sl, ks, n) F_.b[n] = cat_frag(lds_tr(sl + woff[n] + (ks) * 8192), lds_tr(sl + woff[n] + (ks) * 8192 + 1024))
#define P1_STEP(RD, FN, sl, ks, FC) do { \
        p1_mma<ISV>(acc, FC, 0, 0); SBAR(); if (RD) P1_RB(FN, sl, ks, 0); PIN(FC.b[1]); SBAR(); \
        p1_mma<ISV>(acc, FC, 0, 1); SBAR(); if (RD) P1_RA(FN, sl, ks, 0); PIN(FC.a[1]); SBAR(); \
        p1_mma<ISV>(acc, FC, 1, 0); SBAR(); if (RD) P1_RA(FN, sl, ks, 1); SBAR(); \
        p1_mma<ISV>(acc, FC, 1, 1); SBAR(); if (RD) P1_RB(FN, sl, ks, 1); PIN(FC.a[2]); SBAR(); \
        p1_mma<ISV>(acc, FC, 2, 0); SBAR(); if (RD) P1_RA(FN, sl, ks, 2); SBAR(); \
        p1_mma<ISV>(acc, FC, 2, 1); SBAR(); if (RD) P1_RA(FN, sl, ks, 3); PIN(FC.a[3]); SBAR(); \
        p1_mma<ISV>(acc, FC, 3, 0); SBAR(); \
        p1_mma<ISV>(acc, FC, 3, 1); SBAR(); } while (0)
    WAIT_BAR(12);
    STAMP(1);
    startup_tag(k);
    { const lds_ptr sl = k.lds; P1_RB(f0, sl, 0, 0); P1_RB(f0, sl, 0, 1); P1_RA(f0, sl, 0, 0); P1_RA(f0, sl, 0, 1); P1_RA(f0, sl, 0, 2); P1_RA(f0, sl, 0, 3); SBAR(); }
    u32x2 gr = {0, 0};
    PRIO(1);
    const unsigned groff = k.tid < 256 ? WS_ST1 + (rb * 128) * 16 + k.tid * 8 : WS_CSQ + (cb * 128 + (k.tid & 127)) * 8;
#pragma unroll
    for (int kt = 0; kt < 10; ++kt) {
        const lds_ptr sl = k.lds + (kt & 3) * P1_SLOT, sn = k.lds + ((kt + 1) & 3) * P1_SLOT;
        P1_STEP(true, f1, sl, 1, f0);
        if (kt < 9) {
            if (kt < 7) WAIT_BAR(8); else if (kt == 7) WAIT_BAR(4); else WAIT_BAR(0);
            if (kt == 6) gr = ld64(k.rs, groff);
            if (kt + 4 < 10) stage(kt + 4);
            SBAR();
        }
        P1_STEP(kt < 9, f0, sn, 0, f1);
    }
#undef P1_RA
#undef P1_RB
#undef P1_STEP
    PRIO(0);
    mfma_fence();
    STAMP(2);
    for (unsigned spins = 0; spins < SPIN_CAP; ++spins) {
        if (__all(gr.y == (unsigned)k.tag)) break;
        gr = ld64(k.rs, groff);
    }
    LAS float* sst = (LAS float*)k.lds;
    if (k.tid < 384) sst[k.tid] = __uint_as_float(gr.x);
    BAR_L();
    const LAS f32x2* st = (const LAS f32x2*)sst + th * 64;
    const LAS float* cs = sst + 256 + cg * 32;
    if (!ISV) {
        const float osc = cb < 5 ? QSCALE : 1.0f;
        const unsigned img = cb < 5 ? WS_Q : WS_K;
        const int cbs = cb % 5;
        unsigned dst[2]; f32x4 c4[2];
#pragma unroll
        for (int n = 0; n < 2; ++n) {
            const int cgi = cbs * 32 + cg * 8 + n * 4 + g, hd = cgi / 5, gi = cgi - 5 * hd;
            const int blk = gi == 4 ? 4 : ((gi & 1) << 1) | (gi >> 1);
            dst[n] = img + (rb * 4 + th * 2) * QK_TILE + hd * 1280 + blk * 256 + t * 8;
            c4[n] = *(const LAS f32x4*)(cs + n * 16 + 4 * g);
        }
#pragma unroll
        for (int m = 0; m < 4; ++m) {
            const f32x2 sm = st[m * 16 + t];
            const float r = sm.y * osc;
#pragma unroll
            for (int n = 0; n < 2; ++n) {
                const f32x4 y = (acc[m][n] - c4[n] * sm.x) * r;
                const unsigned o = dst[n] + (m >> 1) * QK_TILE + (m & 1) * 128;
                st64(k.rs, o, (u32x2){cvtpk(y[0], y[1]), cvtpk(y[2], y[3])});
            }
        }
    } else {
        const int pos = ((g & 1) << 1) | (g >> 1);
        const unsigned dst = WS_V + (rb * 8 + th * 4) * 20480 + ((cb - 10) * 128 + cg * 32 + t) * 32 + pos * 8;
        const float c1[2] = {cs[t], cs[16 + t]};
#pragma unroll
        for (int m = 0; m < 4; ++m) {
            f32x2 sm[4];
#pragma unroll
            for (int i = 0; i < 4; ++i) sm[i] = st[m * 16 + 4 * g + i];
#pragma unroll
            for (int n = 0; n < 2; ++n) {
                float y[4];
#pragma unroll
                for (int i = 0; i < 4; ++i) y[i] = (acc[m][n][i] - c1[n] * sm[i].x) * sm[i].y;
                st64(k.rs, dst + m * 20480 + n * 512, (u32x2){cvtpk(y[0], y[1]), cvtpk(y[2], y[3])});
            }
        }
    }
}

constexpr int AT_KT = 8192, AT_KB = 10240, AT_HALF = AT_KB + 10240, AT_BUF = 2 * AT_HALF;
constexpr int AT_ONES = AT_BUF, AT_ONES_BYTES = 10752, AT_KC = AT_ONES + AT_ONES_BYTES, AT_KC_BYTES = 2048, AT_FLAG = AT_KC + AT_KC_BYTES, WO_LDS = 61440, AT_RED = 0;
DI int at_slot(int i) { return i == 0 ? 0 : (i == 1 ? 61440 : 61440 + AT_BUF); }
struct AttnCtx {
    unsigned voff[5]; const char* gsrc; unsigned ldst, sstride;
    u32x4 qf0[2]; u32x2 qf1[2];
    int kofs, kt, vlane; bool vdata, hi;
};
DI void attn_stage(const AttnCtx& c, int sidx, int slot) {
#pragma unroll
    for (int i = 0; i < 5; ++i) glds16<true>(c.voff[i], c.gsrc + sidx * c.sstride, c.ldst + at_slot(slot) + i * 1024);
}
DI void attn_setup(const Ctx& k, AttnCtx& c, int h) {
    const int lane = k.lane, w = k.w, kh = w >> 2, r = lane & 31, hi = lane >> 5;
    const bool stV = (w >> 1) & 1;
#pragma unroll
    for (int i = 0; i < 5; ++i) {
        const int pc = 5 * (w & 1) + i;
        if (!stV) c.voff[i] = pc < 8 ? pc * QK_TILE + lane * 16 : ((pc - 8) * 4 + (lane >> 4)) * QK_TILE + 1024 + (lane & 15) * 16;
        else { const int ci = 64 * pc + lane, st = ci / 40, wi = ci % 40; c.voff[i] = st * 20480 + ((wi % 20) * 2 + wi / 20) * 16; }
    }
    c.gsrc = k.ws + (stV ? WS_V + h * 640 + kh * 64 * 20480 : WS_K + h * 1280 + kh * 32 * QK_TILE);
    c.sstride = stV ? 16 * 20480 : 8 * QK_TILE;
    c.ldst = k.lds0 + kh * AT_HALF + (stV ? AT_KB : 0) + (w & 1) * 5120;
    c.kofs = kh * AT_HALF + hi * 256 + r * 8;
    c.hi = hi;
    c.kt = hi ? AT_KC + r * 8 : kh * AT_HALF + AT_KT + r * 8;
    c.vdata = r < 20;
    c.vlane = c.vdata ? kh * AT_HALF + AT_KB + hi * 320 + r * 16 : AT_ONES + (r == 20 ? 128 : r < 28 ? 144 : hi ? 16 : 208);
    for (int i = k.tid; i < AT_ONES_BYTES / 4; i += 512) ((LAS unsigned*)(k.lds + AT_ONES))[i] = (i & 4) ? 0u : 0x3F803F80u;
    ((LAS unsigned*)(k.lds + AT_KC))[k.tid] = (k.tid & 1) ? 0u : 0x00003F80u;
    if (k.tid == 0) *(LAS unsigned*)(k.lds + AT_FLAG) = 0;
}
DI void attn_first_loads(const Ctx& k, AttnCtx& c, int h, int qb) {
    const int qg = k.w & 3, r = k.lane & 31, hi = k.lane >> 5;
    attn_stage(c, 0, 0);
#pragma unroll
    for (int i = 0; i < 2; ++i) {
        const unsigned qp = WS_Q + (qb * 8 + qg * 2 + i) * QK_TILE + h * 1280 + r * 8;
        const u32x2 a = ld64(k.rs, qp + hi * 256), b = ld64(k.rs, qp + hi * 256 + 512), e = ld64(k.rs, qp + 1024);
        c.qf0[i] = (u32x4){a.x, a.y, b.x, b.y};
        c.qf1[i] = hi ? (u32x2){0, 0} : e;
    }
    attn_stage(c, 1, 1);
}
DI bf16x8 kfrag0(lds_ptr p) { const u32x2 a = *(const LAS u32x2*)p, b = *(const LAS u32x2*)(p + 512); return as_frag((u32x4){a.x, a.y, b.x, b.y}); }
DI s16x4 kfrag1(lds_ptr p) { return __builtin_bit_cast(s16x4, *(const LAS u32x2*)p); }
DI s16x4 as_frag4(u32x2 v) { return __builtin_bit_cast(s16x4, v); }
DI f32x16 mfma32x8(s16x4 a, s16x4 b, f32x16 c) { return __builtin_amdgcn_mfma_f32_32x32x8bf16_1k(a, b, c, 0, 0, 0); }
DI void wo_prefetch(const Ctx& k, int ct) {
#pragma unroll 5
    for (int j = 0; j < 15; ++j) { int pc = k.w - 1 + 7 * j; if (pc >= 100) pc -= 7; glds16<true>(k.lane * 16, k.ws + WS_WO + ct * 102400 + pc * 1024, k.lds0 + WO_LDS + pc * 1024); }
}
DI void attn_fast(const Ctx& k, AttnCtx& c, const PreTile& pt, f32x16& oA, f32x16& oB, int ct3) {
    const lds_ptr lds = k.lds;
    const int hi = k.lane >> 5, kh = k.w >> 2;
    {
        const bf16x8 k0 = kfrag0(lds + c.kofs - kh * AT_HALF); const s16x4 k1 = kfrag1(lds + (hi ? c.kt : c.kt - kh * AT_HALF));
#pragma unroll
        for (int i = 0; i < 2; ++i) {
            f32x16 s = mfma32(k0, as_frag(c.qf0[i]), (f32x16){});
            s = mfma32x8(k1, as_frag4(c.qf1[i]), s);
            float m = s[0];
#pragma unroll
            for (int j = 1; j < 16; ++j) m = fmaxf(m, s[j]);
            m = swap32_max(m);
            if (hi) c.qf1[i].x = cvtpk(-m, 0.f) & 0xffffu;
        }
    }
    const bf16x8 qA0 = as_frag(c.qf0[0]), qB0 = as_frag(c.qf0[1]); const s16x4 qA1 = as_frag4(c.qf1[0]), qB1 = as_frag4(c.qf1[1]);
    f32x16 sA, sB, nA, nB;
    oA = (f32x16){}; oB = (f32x16){};
    bf16x8 kf0 = kfrag0(lds + c.kofs); s16x4 kf1 = kfrag1(lds + c.kt);
    sA = mfma32(kf0, qA0, (f32x16){}); sB = mfma32(kf0, qB0, (f32x16){});
    sA = mfma32x8(kf1, qA1, sA); sB = mfma32x8(kf1, qB1, sB);
    kf0 = kfrag0(lds + c.kofs + 1024); kf1 = kfrag1(lds + c.kt + 256);
    u32x4 pA0, pA1, pB0, pB1 = {0, 0, 0, 0};
    bf16x8 vf0, vf1 = {0, 0, 0, 0, 0, 0, 0, 0};
    u32x2 fa = {0, 0}, fb = fa, fc = fa, fd = fa;
#define GAP(MF, X, i, P, j) do { MF; SBAR(); X[i] = EX(X[i]); X[i + 1] = EX(X[i + 1]); X[i + 2] = EX(X[i + 2]); X[i + 3] = EX(X[i + 3]); \
        P[j] = cvtpk(X[i], X[i + 1]); P[j + 1] = cvtpk(X[i + 2], X[i + 3]); PIN(P); SBAR(); } while (0)
#define STEP(SA, SB, NA, NB, VP, KP, KT, HK) do { \
        GAP(NA = mfma32(kf0, qA0, (f32x16){}), SA, 0, pA0, 0); vf0 = as_frag(lds_r128(VP)); HK(0); SBAR(); \
        GAP(oB = mfma32(vf1, as_frag(pB1), oB), SA, 4, pA0, 2); vf1 = as_frag(lds_r128((VP) + 640)); HK(1); SBAR(); \
        GAP(NB = mfma32(kf0, qB0, (f32x16){}), SA, 8, pA1, 0); HK(2); SBAR(); \
        GAP(NA = mfma32x8(kf1, qA1, NA), SA, 12, pA1, 2); HK(3); SBAR(); \
        GAP(NB = mfma32x8(kf1, qB1, NB), SB, 0, pB0, 0); kf0 = kfrag0(KP); kf1 = kfrag1(KT); HK(4); SBAR(); \
        GAP(oA = mfma32(vf0, as_frag(pA0), oA), SB, 4, pB0, 2); HK(5); SBAR(); \
        GAP(oA = mfma32(vf1, as_frag(pA1), oA), SB, 8, pB1, 0); HK(6); SBAR(); \
        GAP(oB = mfma32(vf0, as_frag(pB0), oB), SB, 12, pB1, 2); HK(7); SBAR(); \
    } while (0)
#define HK_NONE(j)
#define STAGE(sidx, slot, nslot, PT, NST, HA, HB) do { \
        const int sb = at_slot(slot), sn = at_slot(nslot); \
        const lds_ptr vb = lds + (c.vdata ? c.vlane + sb : c.vlane); \
        lds_ptr k0 = lds + c.kofs + sb, k0n = lds + c.kofs + sn, k1 = lds + (hi ? c.kt : c.kt + sb), k1n = lds + (hi ? c.kt : c.kt + sn); \
        PIN(k0); PIN(k0n); PIN(k1); PIN(k1n); \
        STEP(sA, sB, nA, nB, vb, k0 + 2 * 1024, k1 + 2 * 256, HK_NONE); PT(0); \
        STEP(nA, nB, sA, sB, vb + 1280, k0 + 3 * 1024, k1 + 3 * 256, HK_NONE); PT(1); \
        STEP(sA, sB, nA, nB, vb + 2 * 1280, k0 + 4 * 1024, k1 + 4 * 256, HK_NONE); PT(2); \
        STEP(nA, nB, sA, sB, vb + 3 * 1280, k0 + 5 * 1024, k1 + 5 * 256, HK_NONE); PT(3); \
        STEP(sA, sB, nA, nB, vb + 4 * 1280, k0 + 6 * 1024, k1 + 6 * 256, HK_NONE); PT(4); \
        STEP(nA, nB, sA, sB, vb + 5 * 1280, k0 + 7 * 1024, k1 + 7 * 256, HK_NONE); \
        if ((sidx) == 3 && k.w == 0) {     \
            const unsigned tl = (unsigned)k.tag, th = (unsigned)(k.tag >> 32); \
            if (!__all(fa.x == tl && fa.y == th && fb.x == tl && fb.y == th && fc.x == tl && fc.y == th && fd.x == tl && fd.y == th)) poll_all(k, WS_FP); \
        } \
        WAIT_BAR(NST); \
        if ((sidx) < 2) attn_stage(c, (sidx) + 2, (nslot) == 2 ? 0 : (nslot) + 1); \
        if ((sidx) == 1 && k.tid == 0) st_flag(k.ws + WS_FP + k.c * FLAG_STRIDE, k.tag);     \
        if ((sidx) == 2 && k.w == 0) {     \
            const unsigned o = WS_FP + k.lane * FLAG_STRIDE; \
            fa = ld64(k.rs, o); fb = ld64(k.rs, o + 64 * FLAG_STRIDE); fc = ld64(k.rs, o + 128 * FLAG_STRIDE); fd = ld64(k.rs, o + 192 * FLAG_STRIDE); \
        } \
        SBAR(); \
        STEP(sA, sB, nA, nB, vb + 6 * 1280, k0n, k1n, HA); \
        STEP(nA, nB, sA, sB, vb + 7 * 1280, k0n + 1024, k1n + 256, HB); \
    } while (0)
#define PT_STORE(j) do { pretile_store(k, pt, j); SBAR(); } while (0)
#define PT_NONE(j)
    const char* wo_src = k.ws + WS_WO + ct3 * 102400;
#define WO_PIECE(n) do { int pc_ = k.w + 8 * (n); if (pc_ >= 100) pc_ -= 8; glds16<true>(k.lane * 16, wo_src + pc_ * 1024, k.lds0 + WO_LDS + pc_ * 1024); } while (0)
#define HK_WO_A(j) WO_PIECE(j)
#define HK_WO_B(j) do { if ((j) < 5) WO_PIECE(8 + (j)); } while (0)
    PRIO(1);
    STAGE(0, 0, 1, PT_STORE, 5, HK_NONE, HK_NONE);
    STAGE(1, 1, 2, PT_NONE, 0, HK_NONE, HK_NONE);
    STAGE(2, 2, 0, PT_NONE, 0, HK_NONE, HK_NONE);
    STAGE(3, 0, 1, PT_NONE, 0, HK_WO_A, HK_WO_B);
#undef STAGE
#undef STEP
#undef PT_STORE
#undef PT_NONE
#undef WO_PIECE
#undef HK_WO_A
#undef HK_WO_B
#undef HK_NONE
    PRIO(0);
    oB = mfma32(vf1, as_frag(pB1), oB);
#undef GAP
}
DI void attn_safe(const Ctx& k, AttnCtx& c, f32x16 (&o)[2], float (&mrun)[2]) {
    const lds_ptr lds = k.lds;
    attn_stage(c, 0, 0);
    if (k.lane >> 5) { c.qf1[0].x = 0; c.qf1[1].x = 0; }
    o[0] = (f32x16){}; o[1] = (f32x16){};
    mrun[0] = -INFINITY; mrun[1] = -INFINITY;
    for (int sidx = 0; sidx < 4; ++sidx) {
        const int sb = at_slot(sidx & 1);
        WAIT_BAR(0);
        if (sidx < 3) attn_stage(c, sidx + 1, (sidx + 1) & 1);
        const lds_ptr vb = lds + (c.vdata ? c.vlane + sb : c.vlane);
        for (int kt = 0; kt < 8; ++kt) {
            const bf16x8 k0 = kfrag0(lds + c.kofs + sb + kt * 1024); const s16x4 k1 = kfrag1(lds + (c.hi ? c.kt : c.kt + sb) + kt * 256);
            const bf16x8 v0 = as_frag(lds_r128(vb + kt * 1280)), v1 = as_frag(lds_r128(vb + kt * 1280 + 640));
#pragma unroll
            for (int i = 0; i < 2; ++i) {
                f32x16 s = mfma32(k0, as_frag(c.qf0[i]), (f32x16){});
                s = mfma32x8(k1, as_frag4(c.qf1[i]), s);
                float m = s[0];
#pragma unroll
                for (int j = 1; j < 16; ++j) m = fmaxf(m, s[j]);
                m = fmaxf(swap32_max(m), mrun[i]);
                const float alpha = EX(mrun[i] - m);
                mrun[i] = m;
                unsigned pk[8];
#pragma unroll
                for (int j = 0; j < 16; ++j) o[i][j] *= alpha;
#pragma unroll
                for (int j = 0; j < 8; ++j) pk[j] = cvtpk(EX(s[2 * j] - m), EX(s[2 * j + 1] - m));
                o[i] = mfma32(v0, as_frag((u32x4){pk[0], pk[1], pk[2], pk[3]}), o[i]);
                o[i] = mfma32(v1, as_frag((u32x4){pk[4], pk[5], pk[6], pk[7]}), o[i]);
            }
        }
    }
}
DI void p2_phase(const Ctx& k, AttnCtx& c, int h, int qb, int ct3) {
    const int lane = k.lane, qg = k.w & 3, kh = k.w >> 2, r = lane & 31, hi = lane >> 5;
    const lds_ptr lds = k.lds;
    attn_first_loads(k, c, h, qb);
    PreTile pt;
    pretile_read(k, pt);
    WAIT_BAR(5);
    STAMP(6);
    f32x16 o[2];
    attn_fast(k, c, pt, o[0], o[1], ct3);
    STAMP(7);
    LAS unsigned* flag = (LAS unsigned*)(lds + AT_FLAG);
    if (__any(hi && !(o[0][8] <= 5.764607523034235e17f && o[1][8] <= 5.764607523034235e17f)) && lane == 0) *flag = 1;
    BAR_L();
    const lds_ptr red = lds + AT_RED + qg * 8192 + lane * 16;
    auto put = [&](const f32x16& v, int slot) {
#pragma unroll
        for (int u = 0; u < 3; ++u) *(LAS f32x4*)(red + slot * 4096 + u * 1024) = (f32x4){v[4 * u], v[4 * u + 1], v[4 * u + 2], v[4 * u + 3]};
    };
    auto get = [&](int slot, int u) { return *(const LAS f32x4*)(red + slot * 4096 + u * 1024); };
    f32x4 of[3];
    if (*flag == 0) {
        if (kh) put(o[0], 0); else put(o[1], 1);
        BAR_L();
#pragma unroll
        for (int u = 0; u < 3; ++u) {
            const f32x4 p = get(kh, u);
#pragma unroll
            for (int j = 0; j < 4; ++j) of[u][j] = p[j] + (kh ? o[1][4 * u + j] : o[0][4 * u + j]);
        }
    } else {
        float mref[2];
        WAIT_BAR(0);
        attn_safe(k, c, o, mref);
        BAR_L();
        if (kh) put(o[0], 0); else put(o[1], 1);
        *(LAS float*)(red + (kh ? 0 : 4096) + 3072) = kh ? mref[0] : mref[1];
        BAR_L();
        const float m0 = kh ? mref[1] : mref[0], m1 = *(const LAS float*)(red + kh * 4096 + 3072), m = fmaxf(m0, m1);
        const float a0 = EX(m0 - m), a1 = EX(m1 - m);
#pragma unroll
        for (int u = 0; u < 3; ++u) {
            const f32x4 p = get(kh, u);
#pragma unroll
            for (int j = 0; j < 4; ++j) of[u][j] = p[j] * a1 + (kh ? o[1][4 * u + j] : o[0][4 * u + j]) * a0;
        }
        BAR_L();
        if (k.w) wo_prefetch(k, ct3);
    }
    {
        auto lr = __builtin_amdgcn_permlane32_swap(__float_as_uint(of[2][0]), __float_as_uint(of[2][0]), false, false);
        const float inv = 1.0f / __uint_as_float(lr[1]);
        const int tok = qb * 256 + qg * 64 + kh * 32 + r;
        const unsigned base = WS_O + (tok >> 4) * OH_TILE + (tok & 15) * 16;
        unsigned pk[3][2];
#pragma unroll
        for (int u = 0; u < 3; ++u) { pk[u][0] = cvtpk(of[u][0] * inv, of[u][1] * inv); pk[u][1] = cvtpk(of[u][2] * inv, of[u][3] * inv); }
        auto oaddr = [&](int col) { return base + (col >> 5) * 1024 + ((col >> 3) & 3) * 256 + (col & 7) * 2; };
        if ((h & 1) == 0) {
            auto s0 = __builtin_amdgcn_permlane32_swap(pk[0][0], pk[1][0], false, false);
            auto s1 = __builtin_amdgcn_permlane32_swap(pk[0][1], pk[1][1], false, false);
            st128(k.rs, oaddr(h * 20 + 8 * hi), (u32x4){s0[0], s1[0], s0[1], s1[1]});
            if (!hi) st64(k.rs, oaddr(h * 20 + 16), (u32x2){pk[2][0], pk[2][1]});
        } else {
            auto s0 = __builtin_amdgcn_permlane32_swap(pk[0][0], pk[2][0], false, false);
            auto s1 = __builtin_amdgcn_permlane32_swap(pk[0][1], pk[2][1], false, false);
            st128(k.rs, oaddr(h * 20 + 4 + 8 * hi), hi ? (u32x4){pk[1][0], pk[1][1], s0[0], s1[0]} : (u32x4){s0[1], s1[1], pk[1][0], pk[1][1]});
            if (!hi) st64(k.rs, oaddr(h * 20), (u32x2){s0[0], s1[0]});
        }
    }
}

DI int own_cf(int kh, int u) { return kh ? (u < 2 ? 3 + u : 4) : u; }
constexpr int RED_LDS = 143360;
struct P3Pre { u32x2 xv[5]; };
DI void p3_pre(const Ctx& k, int rb64, int ct, P3Pre& p) {
    const int tt = k.w & 3, t = k.lane & 15, g = k.lane >> 4;
    if (k.w < 4) {
#pragma unroll
        for (int cf = 0; cf < 5; ++cf) p.xv[cf] = *(const u32x2*)(k.x + (size_t)(rb64 * 64 + tt * 16 + t) * D + ct * 80 + cf * 16 + 4 * g);
    }
}
DI void w13_prefetch(const Ctx& k, int f0, int f);
DI void p3_phase(const Ctx& k, int rb64, int ct, int f0, const P3Pre& pre, u32x2 (&hv)[3]) {
    const int lane = k.lane, tt = k.w & 3, kh = k.w >> 2, t = lane & 15, g = lane >> 4;
    const unsigned ap = WS_O + (rb64 * 4 + tt) * OH_TILE + kh * 10240 + lane * 16;
    const lds_ptr wl = k.lds + WO_LDS + kh * 51200 + lane * 16;
    constexpr int WIN = 5;
    u32x4 a[10];
#pragma unroll
    for (int i = 0; i < WIN; ++i) a[i] = ld128(k.rs, ap + i * 1024);
    bf16x8 wf[2][5];
#pragma unroll
    for (int cf = 0; cf < 5; ++cf) wf[0][cf] = as_frag(lds_r128(wl + cf * 1024));
    f32x4 acc[5];
#pragma unroll
    for (int cf = 0; cf < 5; ++cf) { acc[cf] = (f32x4){0, 0, 0, 0}; PIN(acc[cf]); }
    mfma_fence();
    PRIO(1);
#pragma unroll
    for (int i = 0; i < 10; ++i) {
        if (i + WIN < 10) { a[i + WIN] = ld128(k.rs, ap + (i + WIN) * 1024); SBAR(); }
#pragma unroll
        for (int cf = 0; cf < 5; ++cf) {
            mfma16_acc(acc[cf], wf[i & 1][cf], as_frag(a[i])); SBAR();
            if (i + 1 < 10) wf[(i + 1) & 1][cf] = as_frag(lds_r128(wl + (i + 1) * 5120 + cf * 1024));
            if (cf < 4) PIN(wf[i & 1][cf + 1]); else if (i + 1 < 10) PIN(wf[(i + 1) & 1][0]);
            SBAR();
        }
    }
    PRIO(0);
    mfma_fence();
    STAMP(10);
    BAR_L();
    const lds_ptr red = k.lds + RED_LDS + tt * 5120 + lane * 16;
    if (kh) {
#pragma unroll
        for (int cf = 0; cf < 5; ++cf) *(LAS f32x4*)(red + cf * 1024) = acc[cf];
        BAR_L();
        w13_prefetch(k, f0, k.w - 1);
    } else {
        BAR_L();
        const int tok = rb64 * 64 + tt * 16 + t;
        float s = 0.f, q = 0.f;
        u32x2 hq[5];
#pragma unroll
        for (int cf = 0; cf < 5; ++cf) {
            const f32x4 v = acc[cf] + *(const LAS f32x4*)(red + cf * 1024);
            hq[cf] = (u32x2){cvtpk(v[0] + bflo(pre.xv[cf].x), v[1] + bfhi(pre.xv[cf].x)), cvtpk(v[2] + bflo(pre.xv[cf].y), v[3] + bfhi(pre.xv[cf].y))};
            const int col = ct * 80 + cf * 16 + 4 * g;
            st64(k.rs, WS_H + (tok >> 4) * OH_TILE + (col >> 5) * 1024 + ((col >> 3) & 3) * 256 + t * 16 + (col & 7) * 2, hq[cf]);
            const float h0 = bflo(hq[cf].x), h1 = bfhi(hq[cf].x), h2 = bflo(hq[cf].y), h3 = bfhi(hq[cf].y);
            s += (h0 + h1) + (h2 + h3); q += (h0 * h0 + h1 * h1) + (h2 * h2 + h3 * h3);
        }
        s = sum_x32(sum_x16(s)); q = sum_x32(sum_x16(q));
        if (g == 0) st64(k.rs, WS_PST + (tok >> 4) * 2048 + (ct * 2) * 128 + t * 8, (u32x2){__float_as_uint(s), __float_as_uint(q)});
        if (g == 1) st64(k.rs, WS_PST + (tok >> 4) * 2048 + (ct * 2 + 1) * 128 + t * 8, (u32x2){0u, 0u});
        hv[0] = hq[0]; hv[1] = hq[1]; hv[2] = hq[2];
        const lds_ptr stash = k.lds + RED_LDS + tt * 5120 + lane * 8;
        *(LAS u32x2*)(stash + 3072) = hq[3]; *(LAS u32x2*)(stash + 4096) = hq[4];
    }
}
DI void pair_reduce(lds_ptr red0, int tt, int kh, int lane, const f32x4 (&acc)[5], f32x4 (&own)[3]) {
    const lds_ptr red = red0 + tt * 5120 + lane * 16;
    if (kh == 0) { *(LAS f32x4*)(red + 3072) = acc[3]; *(LAS f32x4*)(red + 4096) = acc[4]; }
    else { *(LAS f32x4*)(red) = acc[0]; *(LAS f32x4*)(red + 1024) = acc[1]; *(LAS f32x4*)(red + 2048) = acc[2]; }
    BAR_L();
    if (kh == 0) {
#pragma unroll
        for (int u = 0; u < 3; ++u) own[u] = acc[u] + *(const LAS f32x4*)(red + u * 1024);
    } else {
        own[0] = acc[3] + *(const LAS f32x4*)(red + 3072); own[1] = acc[4] + *(const LAS f32x4*)(red + 4096); own[2] = own[1];
    }
}
template <int N, int NP>
DI void w2_stage(const Ctx& k, int ct, int b, int s0, int w0) {
    const int nw = 8 - w0;
#pragma unroll
    for (int j = 0; j < NP; ++j) {
        int q = k.w - w0 + nw * j;
        if (q >= 10 * N) q -= nw;
        const int hf = q >= 5 * N, rem = q - hf * 5 * N, n = rem / 5;
        glds16<true>(k.lane * 16, k.ws + WS_W2 + (ct * 54 + hf * 27 + s0) * 5120 + rem * 1024, k.lds0 + n * 20480 + b * 10240 + hf * 5120 + (rem - 5 * n) * 1024);
    }
}
DI void p5_phase(const Ctx& k, int rb64, int ct, u32x2 (&hv)[3]) {
    const int lane = k.lane, tt = k.w & 3, kh = k.w >> 2, t = lane & 15, g = lane >> 4;
    const unsigned ap = WS_HH + (rb64 * 4 + tt) * HH_TILE + kh * 27 * 1024 + lane * 16;
    constexpr int WIN = 6;
    u32x4 a[27];
#pragma unroll
    for (int i = 0; i < WIN; ++i) ald128(a[i], k.rsw, ap, i * 1024);
    if (kh) {
        const lds_ptr stash = k.lds + RED_LDS + tt * 5120 + lane * 8;
        hv[0] = *(const LAS u32x2*)(stash + 3072); hv[1] = *(const LAS u32x2*)(stash + 4096); hv[2] = hv[1];
    }
    lds_ptr wb[3] = {k.lds + lane * 16 + kh * 5120, k.lds + lane * 16 + kh * 5120 + 61440, k.lds + lane * 16 + kh * 5120 + 122880};
    PIN(wb[0]); PIN(wb[1]); PIN(wb[2]);
    auto wread = [&](int n, int cf) {
        const int m = n % 7;
        return as_frag(lds_r128(wb[m / 3] + (m % 3) * 20480 + ((n / 7) & 1) * 10240 + cf * 1024));
    };
    bf16x8 wf[2][5];
#pragma unroll
    for (int cf = 0; cf < 5; ++cf) wf[0][cf] = wread(0, cf);
    f32x4 acc[5];
#pragma unroll
    for (int cf = 0; cf < 5; ++cf) { acc[cf] = (f32x4){0, 0, 0, 0}; PIN(acc[cf]); }
    mfma_fence();
    PRIO(1);
#define P5_STEP(i, NW) do { \
        if ((i) == 7) { BAR_L(); w2_stage<7, 9>(k, ct, 0, 14, 0); } \
        if ((i) == 13) { WAIT_BAR(6); w2_stage<6, 8>(k, ct, 1, 21, 0); } \
        if ((i) == 20) { WAIT_BAR(6); } \
        if ((i) + WIN < 27) ald128(a[(i) + WIN], k.rsw, ap, ((i) + WIN) * 1024); \
        ald_wait<NW>(a[i]); SBAR(); \
        _Pragma("unroll") for (int cf = 0; cf < 5; ++cf) { \
            mfma16_acc(acc[cf], wf[(i) & 1][cf], as_frag(a[i])); SBAR(); \
            if ((i) + 1 < 27) wf[((i) + 1) & 1][cf] = wread((i) + 1, cf); \
            if (cf < 4) PIN(wf[(i) & 1][cf + 1]); else if ((i) + 1 < 27) PIN(wf[((i) + 1) & 1][0]); \
            SBAR(); } } while (0)
    P5_STEP(0, 6); P5_STEP(1, 6); P5_STEP(2, 6); P5_STEP(3, 6); P5_STEP(4, 6); P5_STEP(5, 6); P5_STEP(6, 6);
    P5_STEP(7, 15); P5_STEP(8, 15); P5_STEP(9, 15); P5_STEP(10, 15); P5_STEP(11, 15); P5_STEP(12, 15);
    P5_STEP(13, 14); P5_STEP(14, 14); P5_STEP(15, 14); P5_STEP(16, 14); P5_STEP(17, 14); P5_STEP(18, 14); P5_STEP(19, 6);
    P5_STEP(20, 6); P5_STEP(21, 5); P5_STEP(22, 4); P5_STEP(23, 3); P5_STEP(24, 2); P5_STEP(25, 1); P5_STEP(26, 0);
#undef P5_STEP
    PRIO(0);
    mfma_fence();
    STAMP(16);
    f32x4 own[3];
    pair_reduce(k.lds + RED_LDS, tt, kh, lane, acc, own);
    const int tok = rb64 * 64 + tt * 16 + t;
#pragma unroll
    for (int u = 0; u < 3; ++u) {
        if (u < 2 || kh == 0) {
            const int col = ct * 80 + own_cf(kh, u) * 16 + 4 * g;
            *(u32x2*)(k.out + (size_t)tok * D + col) = (u32x2){cvtpk(own[u][0] + bflo(hv[u].x), own[u][1] + bfhi(hv[u].x)), cvtpk(own[u][2] + bflo(hv[u].y), own[u][3] + bfhi(hv[u].y))};
        }
    }
}

constexpr int P4_CS = 143360;
DI void w13_prefetch(const Ctx& k, int f0, int f) {
    const int p0 = f * 20;
#pragma unroll 4
    for (int j = 0; j < 20; ++j) glds16<true>(k.lane * 16, k.ws + WS_W13 + f0 * 20480 + (p0 + j) * 1024, k.lds0 + (p0 + j) * 1024);
}
DI void p4_colsum(const Ctx& k, int f) {
    const int t = k.lane & 15, g = k.lane >> 4;
    u32x4 wv[20];
#pragma unroll
    for (int s = 0; s < 20; ++s) wv[s] = lds_r128(k.lds + (f * 20 + s) * 1024 + k.lane * 16);
    f32x4 c0 = {0, 0, 0, 0}, c1 = {0, 0, 0, 0};
    const bf16x8 ones = {0x3F80, 0x3F80, 0x3F80, 0x3F80, 0x3F80, 0x3F80, 0x3F80, 0x3F80};
#pragma unroll
    for (int s = 0; s < 20; s += 2) { c0 = mfma16(as_frag(wv[s]), ones, c0); c1 = mfma16(as_frag(wv[s + 1]), ones, c1); }
    if (t == 0) *(LAS f32x4*)(k.lds + P4_CS + f * 64 + g * 16) = c0 + c1;
}
DI void p4_phase(const Ctx& k, int rb, int f0, int nf, int ct5) {
    const int lane = k.lane, w = k.w, t = lane & 15, g = lane >> 4;
    const lds_ptr lds = k.lds;
    const unsigned ap = WS_H + (rb * 16 + w * 2) * OH_TILE + lane * 16;
    u32x4 a0[20], a1[20];
#define P4_LD(s) do { a0[s] = ld128(k.rs, ap + (s) * 1024); a1[s] = ld128(k.rs, ap + OH_TILE + (s) * 1024); } while (0)
    u32x2 pv[2][4];
#pragma unroll
    for (int i = 0; i < 2; ++i)
#pragma unroll
        for (int j = 0; j < 4; ++j) pv[i][j] = ld64(k.rs, WS_PST + (rb * 16 + w * 2 + i) * 2048 + (4 * g + j) * 128 + t * 8);
    P4_LD(0); P4_LD(1); P4_LD(2); P4_LD(3);
    lds_ptr wb[3] = {lds + lane * 16, lds + lane * 16 + 61440, lds + lane * 16 + 122880};
    PIN(wb[1]); PIN(wb[2]);
    bf16x8 wf[2][7];
#pragma unroll
    for (int f = 0; f < 7; ++f) wf[0][f] = as_frag(lds_r128(wb[f / 3] + (f % 3) * 20480));
    f32x4 acc[2][7];
#pragma unroll
    for (int f = 0; f < 7; ++f) { acc[0][f] = (f32x4){0, 0, 0, 0}; acc[1][f] = (f32x4){0, 0, 0, 0}; PIN(acc[0][f]); PIN(acc[1][f]); }
    float mean[2], rstd[2], vpe[2];
    mfma_fence();
    STAMP(13);
    PRIO(1);
#pragma unroll
    for (int s = 0; s < 20; ++s) {
        if (s + 4 < 20) P4_LD(s + 4);
        SBAR();
        if (s == 16) { __builtin_amdgcn_s_barrier(); w2_stage<7, 9>(k, ct5, 0, 0, 0); SBAR(); }
        if (s == 4) {
#pragma unroll
            for (int i = 0; i < 2; ++i) {
                float sm = (__uint_as_float(pv[i][0].x) + __uint_as_float(pv[i][1].x)) + (__uint_as_float(pv[i][2].x) + __uint_as_float(pv[i][3].x));
                float q = (__uint_as_float(pv[i][0].y) + __uint_as_float(pv[i][1].y)) + (__uint_as_float(pv[i][2].y) + __uint_as_float(pv[i][3].y));
                sm = sum_x32(sum_x16(sm)); q = sum_x32(sum_x16(q));
                mean[i] = sm * (1.0f / D);
                vpe[i] = fmaxf(q * (1.0f / D) - mean[i] * mean[i], 0.f) + LN_EPS;
                rstd[i] = __builtin_amdgcn_rsqf(vpe[i]);
            }
            PIN(mean[0]); PIN(mean[1]); PIN(rstd[0]); PIN(rstd[1]); PIN(vpe[0]); PIN(vpe[1]);
            SBAR();
        }
#pragma unroll
        for (int f = 0; f < 7; ++f) {
            mfma16_acc(acc[0][f], wf[s & 1][f], as_frag(a0[s])); SBAR();
            if (s + 1 < 20) wf[(s + 1) & 1][f] = as_frag(lds_r128(wb[f / 3] + (f % 3) * 20480 + (s + 1) * 1024));
            if (f < 6) PIN(wf[s & 1][f + 1]); else if (s + 1 < 20) PIN(wf[(s + 1) & 1][0]);
            SBAR();
            mfma16_acc(acc[1][f], wf[s & 1][f], as_frag(a1[s])); SBAR();
        }
    }
#undef P4_LD
    PRIO(0);
    mfma_fence();
    STAMP(14);
    f32x4 cs[7];
#pragma unroll
    for (int f = 0; f < 7; ++f) cs[f] = *(const LAS f32x4*)(lds + P4_CS + f * 64 + g * 16);
#pragma unroll
    for (int i = 0; i < 2; ++i) {
        const float ce = -LOG2E * rstd[i], ir2 = vpe[i], nm = -mean[i];
        const f32x2 ce2 = {ce, ce}, ir22 = {ir2, ir2}, nm2 = {nm, nm};
        unsigned d[8];
#pragma unroll
        for (int f = 0; f < 7; ++f) {
            const f32x2 ga = __builtin_elementwise_fma(nm2, __builtin_shufflevector(cs[f], cs[f], 0, 1), __builtin_shufflevector(acc[i][f], acc[i][f], 0, 1));
            const f32x2 ua = __builtin_elementwise_fma(nm2, __builtin_shufflevector(cs[f], cs[f], 2, 3), __builtin_shufflevector(acc[i][f], acc[i][f], 2, 3));
            const f32x2 t2 = ga * ce2;
            const f32x2 e2 = {EX(t2.x), EX(t2.y)};
            const f32x2 d2 = __builtin_elementwise_fma(e2, ir22, ir22);
            const f32x2 r2 = {__builtin_amdgcn_rcpf(d2.x), __builtin_amdgcn_rcpf(d2.y)};
            const f32x2 o2 = (ga * ua) * r2;
            d[f] = cvtpk(o2.x, o2.y);
        }
        d[7] = 0;
        const unsigned dst = WS_HH + (rb * 16 + w * 2 + i) * HH_TILE + f0 * 256 + lane * 16;
#pragma unroll
        for (int u = 0; u < 2; ++u) {
            auto a = __builtin_amdgcn_permlane32_swap(d[4 * u], d[4 * u + 2], false, false);
            auto b = __builtin_amdgcn_permlane32_swap(d[4 * u + 1], d[4 * u + 3], false, false);
            auto e = __builtin_amdgcn_permlane16_swap(a[0], b[0], false, false);
            auto f = __builtin_amdgcn_permlane16_swap(a[1], b[1], false, false);
            if (4 * u + g < nf) st128(k.rs, dst + u * 1024, (u32x4){e[0], e[1], f[0], f[1]});
        }
    }
}

constexpr int LDS_BYTES = 163840;
__global__ __launch_bounds__(512, 2) void fused_block(const uint16_t* __restrict__ x, const uint16_t* __restrict__ wqkv, const uint16_t* __restrict__ wo,
                                                      const uint16_t* __restrict__ w1, const uint16_t* __restrict__ w3, const uint16_t* __restrict__ w2,
                                                      char* __restrict__ ws, uint16_t* __restrict__ out) {
    extern __shared__ __attribute__((aligned(16))) char smem[];
    Ctx k;
    k.x = x; k.wqkv = wqkv; k.wo = wo; k.w1 = w1; k.w3 = w3; k.w2 = w2; k.ws = ws; k.out = out;
    k.rs = __builtin_amdgcn_make_buffer_rsrc(ws, 0, WS_END + 20480, 0x00020000);
    k.lds = (lds_ptr)smem; k.lds0 = (unsigned)(uintptr_t)smem;
    k.c = blockIdx.x; k.xcd = k.c & 7; k.lb = k.c >> 3;
    k.tid = threadIdx.x; k.lane = k.tid & 63; k.w = rfl(k.tid >> 6);
    k.rsw = (u32x4){(unsigned)(uintptr_t)ws, (unsigned)((uintptr_t)ws >> 32) & 0xffffu, WS_END + 20480, 0x00020000u};
    k.tag = 0;
    k.sc = 0; k.sr = 0;
    STAMP(0);
    startup_load(k);
    const int rb1 = (k.xcd >> 2) * 8 + (k.lb & 7), cb1 = (k.xcd & 3) * 4 + (k.lb >> 3);
    if (cb1 < 15) {
        if (cb1 >= 10) p1_body<true>(k, rb1, cb1); else p1_body<false>(k, rb1, cb1);
        STAMP(3);
        WAIT_BAR(0);
        if (k.tid == 0) st_flag(ws + WS_F1 + (rb1 * 16 + cb1) * FLAG_STRIDE, k.tag);
    } else {
        WAIT_VM(0);
        startup_tag(k);
        ln1_stats(k, rb1);
        if (rb1 < 15) colsum_qkv(k, rb1);
        WAIT_BAR(0);
    }
    STAMP(4);
    if (k.w) pretile_load(k);
    const int h = k.xcd * 4 + (k.lb & 3), qb = k.lb >> 2;
    const int rb64 = k.xcd * 4 + (k.lb >> 3), ct = k.lb & 7;
    const int rb4 = (k.xcd >> 1) * 2 + (k.lb >> 4), cb4 = (k.xcd & 1) * 16 + (k.lb & 15);
    const int f0 = cb4 < 24 ? cb4 * 7 : 168 + (cb4 - 24) * 6, nf = cb4 < 24 ? 7 : 6;
    AttnCtx ac;
    attn_setup(k, ac, h);
    if (k.w == 0) {
        const int c0 = (20 * h) >> 7, c1 = (20 * h + 19) >> 7, j = k.lane & 15, sel = k.lane >> 4;
        const int cbk = (sel & 1) ? c1 : c0;
        const unsigned o0 = WS_F1 + (j * 16 + (sel < 2 ? 5 : 10) + cbk) * FLAG_STRIDE;
        const unsigned o1 = WS_F1 + ((2 * qb + (k.lane & 1)) * 16 + ((k.lane & 2) ? c1 : c0)) * FLAG_STRIDE;
        poll_flags2(k, o0, o1);
    }
    WAIT_BAR(0);
    STAMP(5);
    p2_phase(k, ac, h, qb, ct);
    STAMP(8);
    WAIT_BAR(0);
    if (k.tid == 0) st_flag(ws + WS_F2 + (qb * 32 + h) * FLAG_STRIDE, k.tag);
    P3Pre pre;
    p3_pre(k, rb64, ct, pre);
    if (k.w == 0) poll_flags(k, WS_F2 + (k.xcd * 32 + (k.lane & 31)) * FLAG_STRIDE);
    else if (k.w <= 3) w13_prefetch(k, f0, k.w - 1);
    BAR_L();
    if (k.c == 0 && k.tid == 0) st_flag(ws + WS_EPOCH, k.tag);
    STAMP(9);
    u32x2 hv[3];
    p3_phase(k, rb64, ct, f0, pre, hv);
    STAMP(11);
    if (k.w < 4) WAIT_VM(0);
    BAR_L();
    if (k.tid == 0) st_flag(ws + WS_F3 + (k.xcd * 32 + k.lb) * FLAG_STRIDE, k.tag);
    if (k.w == 0) poll_flags(k, WS_F3 + (rb4 * 32 + (k.lane & 31)) * FLAG_STRIDE);
    else {
        WAIT_VM(0);
        p4_colsum(k, k.w - 1);
    }
    WAIT_BAR(0);
    STAMP(12);
    p4_phase(k, rb4, f0, nf, ct);
    STAMP(15);
    WAIT_BAR(0);
    if (k.tid == 0) st_flag(ws + WS_F4 + (rb4 * 32 + cb4) * FLAG_STRIDE, k.tag);
    if (k.w == 0) poll_flags(k, WS_F4 + ((rb64 >> 2) * 32 + (k.lane & 31)) * FLAG_STRIDE);
    else {
        w2_stage<7, 10>(k, ct, 1, 7, 1);
        const int li = (k.w - 1) * 64 + k.lane;
        if (k.w <= 5) {
            const int ln = (k.lb >> 3) * 260 + (li < 260 ? li : li - 64), hf = ln >= 520;
            const unsigned warm = ld32(k.rs, WS_W2 + (ct * 54 + hf * 27 + 14) * 5120 + (ln - hf * 520) * 128);
            asm volatile("" :: "v"(warm));
        }
    }
    WAIT_BAR(0);
    STAMP(17);
    p5_phase(k, rb64, ct, hv);
    STAMP(18);
    STAMP_FLUSH();
}

extern "C" void kernel_launch(void* const* d_in, const int* in_sizes, int n_in, void* d_out, int out_size, void* d_ws, size_t ws_size, hipStream_t stream) {
    (void)hipFuncSetAttribute((const void*)fused_block, hipFuncAttributeMaxDynamicSharedMemorySize, LDS_BYTES);
    fused_block<<<256, 512, LDS_BYTES, stream>>>((const uint16_t*)d_in[0], (const uint16_t*)d_in[1], (const uint16_t*)d_in[2], (const uint16_t*)d_in[4],
                                                 (const uint16_t*)d_in[5], (const uint16_t*)d_in[6], (char*)d_ws, (uint16_t*)d_out);
}
```

```cpp
#include <hip/hip_runtime.h>
#include <hip/hip_bf16.h>
#include <stdint.h>

#define DI __device__ __forceinline__
#define LAS __attribute__((address_space(3)))
#define GAS __attribute__((address_space(1)))
typedef short bf16x8 __attribute__((ext_vector_type(8)));
typedef short s16x4 __attribute__((ext_vector_type(4)));
typedef float f32x2 __attribute__((ext_vector_type(2)));
typedef float f32x4 __attribute__((ext_vector_type(4)));
typedef float f32x16 __attribute__((ext_vector_type(16)));
typedef unsigned u32x2 __attribute__((ext_vector_type(2)));
typedef unsigned u32x4 __attribute__((ext_vector_type(4)));
typedef unsigned long long u64;
typedef __bf16 bf16x2_t __attribute__((ext_vector_type(2)));
typedef short v4i16_t __attribute__((ext_vector_type(4)));
typedef LAS char* lds_ptr;
typedef __amdgpu_buffer_rsrc_t rsrc_t;

constexpr int S = 2048, D = 640, H = 32, DH = 20, F = 1728, NQKV = 3 * D;
constexpr float LN_EPS = 1e-5f;
constexpr float LOG2E = 1.4426950408889634f;
constexpr float QSCALE = 0.22360679774997896f * LOG2E;

constexpr unsigned WS_EPOCH = 0;
constexpr unsigned FLAG_STRIDE = 64, FLAG_ARR = 256 * FLAG_STRIDE;
constexpr unsigned WS_F1 = 4096, WS_FP = WS_F1 + FLAG_ARR, WS_F2 = WS_FP + FLAG_ARR, WS_F3 = WS_F2 + FLAG_ARR;
constexpr unsigned WS_F4A = WS_F3 + FLAG_ARR, WS_F4B = WS_F4A + 4 * FLAG_ARR;
constexpr unsigned WS_ST1 = WS_F4B + 4 * FLAG_ARR;
constexpr unsigned WS_CSQ = WS_ST1 + 32768;
static_assert(WS_CSQ + 1920 * 8 <= 262144, "control region");
constexpr unsigned WS_PST = 262144;
constexpr unsigned WS_Q = 524288;
constexpr unsigned IMG = S * D * 2;
constexpr unsigned QK_TILE = 32 * 1280, QK_IMG = 64 * QK_TILE;
constexpr unsigned WS_K = WS_Q + QK_IMG;
constexpr unsigned WS_V = WS_K + QK_IMG;
constexpr unsigned OH_TILE = 21504, HH_TILE = 56320;
constexpr unsigned WS_O = WS_V + IMG;
constexpr unsigned WS_H = WS_O + 128 * OH_TILE;
constexpr unsigned WS_HH = WS_H + 128 * OH_TILE;
constexpr unsigned WS_WO = WS_HH + 128 * HH_TILE;
constexpr unsigned WS_W13 = WS_WO + D * D * 2;
constexpr unsigned WS_W2 = WS_W13 + D * F * 4;
constexpr unsigned WS_END = WS_W2 + F * D * 2;

DI unsigned cvtpk(float lo, float hi) { f32x2 v = {lo, hi}; bf16x2_t b = __builtin_convertvector(v, bf16x2_t); return __builtin_bit_cast(unsigned, b); }
DI float bflo(unsigned u) { return __uint_as_float(u << 16); }
DI float bfhi(unsigned u) { return __uint_as_float(u & 0xffff0000u); }
DI f32x4 mfma16(bf16x8 a, bf16x8 b, f32x4 c) { return __builtin_amdgcn_mfma_f32_16x16x32_bf16(a, b, c, 0, 0, 0); }
DI void mfma16_acc(f32x4& c, bf16x8 a, bf16x8 b) { asm volatile("v_mfma_f32_16x16x32_bf16 %0, %1, %2, %0" : "+v"(c) : "v"(a), "v"(b)); }
DI void mfma_fence() { __builtin_amdgcn_sched_barrier(0); asm volatile("s_nop 7\n\ts_nop 1" ::: "memory"); __builtin_amdgcn_sched_barrier(0); }
DI f32x16 mfma32(bf16x8 a, bf16x8 b, f32x16 c) { return __builtin_amdgcn_mfma_f32_32x32x16_bf16(a, b, c, 0, 0, 0); }
DI int rfl(int x) { return __builtin_amdgcn_readfirstlane(x); }
template <bool SC1>
DI void glds16(unsigned voff, const void* sbase, unsigned ldsdst) {
    unsigned keep;
    if (SC1) asm volatile("s_mov_b32 %0, m0\n\ts_mov_b32 m0, %3\n\ts_nop 0\n\tglobal_load_lds_dwordx4 %1, %2 sc1\n\ts_mov_b32 m0, %0" : "=&s"(keep) : "v"(voff), "s"(sbase), "s"(ldsdst) : "memory");
    else asm volatile("s_mov_b32 %0, m0\n\ts_mov_b32 m0, %3\n\ts_nop 0\n\tglobal_load_lds_dwordx4 %1, %2\n\ts_mov_b32 m0, %0" : "=&s"(keep) : "v"(voff), "s"(sbase), "s"(ldsdst) : "memory");
}
DI void glds4(const void* gptr, unsigned ldsdst) {
    unsigned keep;
    asm volatile("s_mov_b32 %0, m0\n\ts_mov_b32 m0, %2\n\ts_nop 0\n\tglobal_load_lds_dword %1, off\n\ts_mov_b32 m0, %0" : "=&s"(keep) : "v"(gptr), "s"(ldsdst) : "memory");
}
#define WAIT_VM(n) asm volatile("s_waitcnt vmcnt(" #n ")" ::: "memory")
#define WAIT_BAR(n) asm volatile("s_waitcnt vmcnt(" #n ") lgkmcnt(0)\n\ts_barrier" ::: "memory")
#define BAR_L() asm volatile("s_waitcnt lgkmcnt(0)\n\ts_barrier" ::: "memory")
#define SBAR() __builtin_amdgcn_sched_barrier(0)
#define PIN(x) asm volatile("" : "+v"(x))
#define PRIO(n) __builtin_amdgcn_s_setprio(n)
#define EX(v) __builtin_amdgcn_exp2f(v)
DI u32x4 lds_r128(lds_ptr p) { return *(const LAS u32x4*)p; }
DI s16x4 lds_tr(lds_ptr p) { return __builtin_bit_cast(s16x4, __builtin_amdgcn_ds_read_tr16_b64_v4i16((LAS v4i16_t*)p)); }
DI bf16x8 as_frag(u32x4 v) { return __builtin_bit_cast(bf16x8, v); }
DI bf16x8 cat_frag(s16x4 a, s16x4 b) { return (bf16x8){a[0], a[1], a[2], a[3], b[0], b[1], b[2], b[3]}; }
DI float swap32_max(float m) { auto r = __builtin_amdgcn_permlane32_swap(__float_as_uint(m), __float_as_uint(m), false, false); return fmaxf(__uint_as_float(r[0]), __uint_as_float(r[1])); }
DI float sum_x16(float v) { auto r = __builtin_amdgcn_permlane16_swap(__float_as_uint(v), __float_as_uint(v), false, false); return __uint_as_float(r[0]) + __uint_as_float(r[1]); }
DI float sum_x32(float v) { auto r = __builtin_amdgcn_permlane32_swap(__float_as_uint(v), __float_as_uint(v), false, false); return __uint_as_float(r[0]) + __uint_as_float(r[1]); }
DI u32x4 ld128(rsrc_t r, unsigned off) { return __builtin_amdgcn_raw_buffer_load_b128(r, off, 0, 16); }
DI u32x2 ld64(rsrc_t r, unsigned off) { return __builtin_amdgcn_raw_buffer_load_b64(r, off, 0, 16); }
DI unsigned ld32(rsrc_t r, unsigned off) { return __builtin_amdgcn_raw_buffer_load_b32(r, off, 0, 16); }
DI void st128(rsrc_t r, unsigned off, u32x4 v) { __builtin_amdgcn_raw_buffer_store_b128(v, r, off, 0, 16); }
DI void st64(rsrc_t r, unsigned off, u32x2 v) { __builtin_amdgcn_raw_buffer_store_b64(v, r, off, 0, 16); }

DI void ald128(u32x4& v, u32x4 rsw, unsigned voff, unsigned soff) { asm volatile("buffer_load_dwordx4 %0, %1, %2, %3 offen sc1" : "=&v"(v) : "v"(voff), "s"(rsw), "s"(soff) : "memory"); }
template <int N> DI void ald_wait(u32x4& v) { asm volatile("s_waitcnt vmcnt(%1)" : "+v"(v) : "i"(N) : "memory"); }
DI void ald64(u32x2& v, u32x4 rsw, unsigned voff) { asm volatile("buffer_load_dwordx2 %0, %1, %2, 0 offen sc1" : "=&v"(v) : "v"(voff), "s"(rsw) : "memory"); }
DI void touch_lines(unsigned& dummy, u32x4 rsw, unsigned voff) { asm volatile("buffer_load_dword %0, %1, %2, 0 offen sc1" : "=&v"(dummy) : "v"(voff), "s"(rsw) : "memory"); }
struct Ctx {
    const uint16_t *x, *wqkv, *wo, *w1, *w3, *w2;
    char* ws; uint16_t* out;
    rsrc_t rs; u32x4 rsw; u64 tag; lds_ptr lds; unsigned lds0;
    int c, xcd, lb, tid, lane, w;
    mutable unsigned sc, sr;
};

#define STAMP(i)
#define STAMP_FLUSH()

DI u64 ld_flag(const char* p) { return __hip_atomic_load((const GAS u64*)p, __ATOMIC_RELAXED, __HIP_MEMORY_SCOPE_AGENT); }
DI void st_flag(char* p, u64 v) { __hip_atomic_store((GAS u64*)p, v, __ATOMIC_RELAXED, __HIP_MEMORY_SCOPE_AGENT); }
constexpr unsigned SPIN_CAP = 1u << 18;
DI void poll_flags(const Ctx& k, unsigned off) {
    u64 a = ld_flag(k.ws + off);
    __builtin_amdgcn_s_sleep(8);
    for (unsigned spins = 0; spins < SPIN_CAP; ++spins) {
        const u64 b = ld_flag(k.ws + off);
        if (__all(a == k.tag)) break;
        a = ld_flag(k.ws + off);
        if (__all(b == k.tag)) break;
    }
}
DI void poll_flags2(const Ctx& k, unsigned off0, unsigned off1) {
    u64 a0 = ld_flag(k.ws + off0), a1 = ld_flag(k.ws + off1);
    __builtin_amdgcn_s_sleep(8);
    for (unsigned spins = 0; spins < SPIN_CAP; ++spins) {
        const u64 b0 = ld_flag(k.ws + off0), b1 = ld_flag(k.ws + off1);
        if (__all(a0 == k.tag && a1 == k.tag)) break;
        a0 = ld_flag(k.ws + off0); a1 = ld_flag(k.ws + off1);
        if (__all(b0 == k.tag && b1 == k.tag)) break;
    }
}
DI void poll_all(const Ctx& k, unsigned arr) {
    for (unsigned spins = 0; spins < SPIN_CAP; ++spins) {
        const unsigned o = arr + k.lane * FLAG_STRIDE;
        const u64 a = ld_flag(k.ws + o), b = ld_flag(k.ws + o + 64 * FLAG_STRIDE), c = ld_flag(k.ws + o + 128 * FLAG_STRIDE), d = ld_flag(k.ws + o + 192 * FLAG_STRIDE);
        if (__all(a == k.tag && b == k.tag && c == k.tag && d == k.tag)) break;
        __builtin_amdgcn_s_sleep(1);
    }
}

constexpr int SU_LDS = 131072;
DI void startup_load(const Ctx& k) { glds4(k.ws + WS_EPOCH + (k.lane & 1) * 4, k.lds0 + SU_LDS + k.w * 256); }
DI void startup_tag(Ctx& k) {
    const u32x2 e = *(const LAS u32x2*)(k.lds + SU_LDS + k.w * 256);
    k.tag = (((u64)(unsigned)rfl(e.y) << 32) | (unsigned)rfl(e.x)) + 1;
}
DI void ln1_stats(const Ctx& k, int j) {
    u32x4 v[2][10];
#pragma unroll
    for (int p = 0; p < 2; ++p)
#pragma unroll
        for (int i = 0; i < 10; ++i) v[p][i] = *(const u32x4*)(k.x + (size_t)(128 * j + 16 * k.w + 8 * p + (k.lane >> 3)) * D + ((k.lane & 7) + 8 * i) * 8);
#pragma unroll
    for (int p = 0; p < 2; ++p) {
        float s = 0.f, q = 0.f;
#pragma unroll
        for (int i = 0; i < 10; ++i)
#pragma unroll
            for (int e = 0; e < 4; ++e) { const float a = bflo(v[p][i][e]), b = bfhi(v[p][i][e]); s += a + b; q = fmaf(a, a, fmaf(b, b, q)); }
#pragma unroll
        for (int o = 4; o >= 1; o >>= 1) { s += __shfl_xor(s, o); q += __shfl_xor(q, o); }
        const float mean = s * (1.0f / D), var = fmaxf(q * (1.0f / D) - mean * mean, 0.f);
        const float val = (k.lane & 1) ? 1.0f / sqrtf(var + LN_EPS) : mean;
        const int row = 128 * j + 16 * k.w + 8 * p + (k.lane >> 3);
        if ((k.lane & 7) < 2) st64(k.rs, WS_ST1 + row * 16 + (k.lane & 1) * 8, (u32x2){__float_as_uint(val), (unsigned)k.tag});
    }
}
DI void colsum_qkv(const Ctx& k, int cb) {
    float a[8] = {0, 0, 0, 0, 0, 0, 0, 0};
    u32x4 v[20];
#pragma unroll
    for (int i = 0; i < 20; ++i) v[i] = *(const u32x4*)(k.wqkv + (size_t)(k.w * 80 + i * 4 + (k.lane >> 4)) * NQKV + cb * 128 + (k.lane & 15) * 8);
#pragma unroll
    for (int i = 0; i < 20; ++i)
#pragma unroll
        for (int j = 0; j < 4; ++j) { a[2 * j] += bflo(v[i][j]); a[2 * j + 1] += bfhi(v[i][j]); }
    LAS float* red = (LAS float*)k.lds;
#pragma unroll
    for (int j = 0; j < 8; ++j) { a[j] += __shfl_xor(a[j], 16); a[j] += __shfl_xor(a[j], 32); }
    if (k.lane < 16) {
#pragma unroll
        for (int j = 0; j < 8; ++j) red[k.w * 128 + k.lane * 8 + j] = a[j];
    }
    BAR_L();
    if (k.tid < 128) {
        float t = 0.f;
#pragma unroll
        for (int i = 0; i < 8; ++i) t += red[i * 128 + k.tid];
        st64(k.rs, WS_CSQ + (cb * 128 + k.tid) * 8, (u32x2){__float_as_uint(t), (unsigned)k.tag});
    }
}

DI int p4_f0(int cb) { return cb < 24 ? cb * 7 : 168 + (cb - 24) * 6; }
DI int hh_f8(int sigma, int gp) {
    const int kh = sigma >= 27, i = sigma - 27 * kh;
    if (i < 16) return p4_f0(16 * kh + i) + gp;
    const int l = 4 * (11 * kh + i - 16) + gp, cb = l < 72 ? l / 3 : 24 + (l - 72) / 2;
    return p4_f0(cb) + 4 + (l < 72 ? l % 3 : (l - 72) % 2);
}
constexpr int PT_LDS = 131072;
DI int pt_off(int i) { return PT_LDS + (i < 3 ? i * 8192 : 24576 + (i - 3) * 4096); }
DI void pretile_load(const Ctx& k) {
    const int wv = k.w - 1;
#pragma unroll
    for (int i = 0; i < 5; ++i) {
        const int u = k.c + 256 * i;
        if (u < 540) {
            const int s = u % 20, fb = u / 20;
            const uint16_t* src = ((k.lane & 1) ? k.w3 : k.w1) + fb * 64 + (k.lane >> 1) * 2 + (size_t)32 * s * F;
            for (int r = wv; r < 32; r += 7) glds4(src + (size_t)r * F, k.lds0 + pt_off(i) + r * 256);
        } else {
            const int pc = (i & 1) ? wv - 3 : wv;
            if (pc >= 0 && pc < 4) {
                const bool is2 = u < 1080;
                const int uu = is2 ? u - 540 : u - 1080, s = uu / 10, nb = uu % 10;
                const int r = (is2 ? 8 * hh_f8(s, pc) : 32 * s + 8 * pc) + (k.lane >> 3);
                glds16<false>(r * (D * 2) + nb * 128 + (k.lane & 7) * 16, is2 ? k.w2 : k.wo, k.lds0 + pt_off(i) + pc * 1024);
            }
        }
    }
}
struct PreTile { u32x4 v[5]; };
DI void pretile_read(const Ctx& k, PreTile& p) {
    const int g = k.lane >> 4, q = (k.lane >> 2) & 3, pp = k.lane & 3;
#pragma unroll
    for (int i = 0; i < 5; ++i) {
        const int u = k.c + 256 * i;
        const bool wide = u < 540;
        const int rs = wide ? 256 : 128, f = wide ? k.w : (k.w & 3);
        const lds_ptr a = k.lds + pt_off(i) + (8 * g + q) * rs + f * 32 + pp * 8;
        p.v[i] = __builtin_bit_cast(u32x4, cat_frag(lds_tr(a), lds_tr(a + 4 * rs)));
    }
}
DI void pretile_store(const Ctx& k, const PreTile& p, int i) {
    const int u = k.c + 256 * i;
    unsigned dst;
    if (u < 540) dst = WS_W13 + (((u / 20) * 8 + k.w) * 20 + u % 20) * 1024;
    else {
        const bool is2 = u < 1080;
        const int uu = is2 ? u - 540 : u - 1080, s = uu / 10, cfg = (uu % 10) * 4 + (k.w & 3), ct = cfg / 5, cf = cfg % 5;
        dst = is2 ? WS_W2 + ((ct * 54 + s) * 5 + cf) * 1024 : WS_WO + ((ct * 20 + s) * 5 + cf) * 1024;
    }
    st128(k.rs, dst + k.lane * 16, p.v[i]);
}

constexpr int P1_SLOT = 32768;
struct P1Frag { bf16x8 a[4], b[2]; };
template <bool ISV> DI void p1_mma(f32x4 (&acc)[4][2], const P1Frag& f, int m, int n) { if (ISV) mfma16_acc(acc[m][n], f.a[m], f.b[n]); else mfma16_acc(acc[m][n], f.b[n], f.a[m]); }
template <bool ISV>
DI void p1_body(Ctx& k, int rb, int cb) {
    const int lane = k.lane, w = k.w, th = w >> 2, cg = w & 3, t = lane & 15, g = lane >> 4;
    unsigned voffA[2], voffW[2];
#pragma unroll
    for (int i = 0; i < 2; ++i) {
        const int ra = 8 * (2 * w + i) + (lane >> 3), ca = (lane & 7) ^ ((ra >> 1) & 7);
        voffA[i] = ra * (D * 2) + ca * 16;
        const int rw = 4 * (2 * w + i) + (lane >> 4), fw = (rw & 3) | (((rw >> 3) & 1) << 2), cw = (lane & 15) ^ (fw << 1);
        voffW[i] = rw * (NQKV * 2) + cw * 16;
    }
    const char* gA = (const char*)k.x + (size_t)rb * 128 * D * 2;
    const char* gW = (const char*)k.wqkv + cb * 256;
    auto stage = [&](int kt) {
        const unsigned sl = k.lds0 + (kt & 3) * P1_SLOT + w * 2048;
#pragma unroll
        for (int i = 0; i < 2; ++i) glds16<false>(voffA[i], gA + kt * 128, sl + i * 1024);
#pragma unroll
        for (int i = 0; i < 2; ++i) glds16<false>(voffW[i], gW + (size_t)kt * 64 * NQKV * 2, sl + 16384 + i * 1024);
    };
    stage(0); stage(1); stage(2); stage(3);
    int aoff[2], woff[2];
    aoff[0] = (th * 64 + t) * 128 + ((g ^ (t >> 1)) << 4); aoff[1] = aoff[0] ^ 64;
    const int q4 = (lane >> 2) & 3, p = lane & 3, fw = q4 | ((g & 1) << 2);
#pragma unroll
    for (int n = 0; n < 2; ++n) woff[n] = 16384 + (8 * g + q4) * 256 + (((2 * cg + n) ^ fw) << 5) + p * 8;
    f32x4 acc[4][2];
#pragma unroll
    for (int m = 0; m < 4; ++m) { acc[m][0] = (f32x4){0, 0, 0, 0}; acc[m][1] = (f32x4){0, 0, 0, 0}; PIN(acc[m][0]); PIN(acc[m][1]); }
    mfma_fence();
    P1Frag f0, f1;
#define P1_RA(F_, sl, ks, m) F_.a[m] = as_frag(lds_r128(sl + aoff[ks] + (m) * 2048))
#define P1_RB(F_, sl, ks, n) F_.b[n] = cat_frag(lds_tr(sl + woff[n] + (ks) * 8192), lds_tr(sl + woff[n] + (ks) * 8192 + 1024))
#define P1_STEP(RD, FN, sl, ks, FC) do { \
        p1_mma<ISV>(acc, FC, 0, 0); SBAR(); if (RD) P1_RB(FN, sl, ks, 0); PIN(FC.b[1]); SBAR(); \
        p1_mma<ISV>(acc, FC, 0, 1); SBAR(); if (RD) P1_RA(FN, sl, ks, 0); PIN(FC.a[1]); SBAR(); \
        p1_mma<ISV>(acc, FC, 1, 0); SBAR(); if (RD) P1_RA(FN, sl, ks, 1); SBAR(); \
        p1_mma<ISV>(acc, FC, 1, 1); SBAR(); if (RD) P1_RB(FN, sl, ks, 1); PIN(FC.a[2]); SBAR(); \
        p1_mma<ISV>(acc, FC, 2, 0); SBAR(); if (RD) P1_RA(FN, sl, ks, 2); SBAR(); \
        p1_mma<ISV>(acc, FC, 2, 1); SBAR(); if (RD) P1_RA(FN, sl, ks, 3); PIN(FC.a[3]); SBAR(); \
        p1_mma<ISV>(acc, FC, 3, 0); SBAR(); \
        p1_mma<ISV>(acc, FC, 3, 1); SBAR(); } while (0)
    WAIT_BAR(12);
    STAMP(1);
    startup_tag(k);
    { const lds_ptr sl = k.lds; P1_RB(f0, sl, 0, 0); P1_RB(f0, sl, 0, 1); P1_RA(f0, sl, 0, 0); P1_RA(f0, sl, 0, 1); P1_RA(f0, sl, 0, 2); P1_RA(f0, sl, 0, 3); SBAR(); }
    u32x2 gr = {0, 0};
    PRIO(1);
    const unsigned groff = k.tid < 256 ? WS_ST1 + (rb * 128) * 16 + k.tid * 8 : WS_CSQ + (cb * 128 + (k.tid & 127)) * 8;
#pragma unroll
    for (int kt = 0; kt < 10; ++kt) {
        const lds_ptr sl = k.lds + (kt & 3) * P1_SLOT, sn = k.lds + ((kt + 1) & 3) * P1_SLOT;
        P1_STEP(true, f1, sl, 1, f0);
        if (kt < 9) {
            if (kt < 7) WAIT_BAR(8); else if (kt == 7) WAIT_BAR(4); else WAIT_BAR(0);
            if (kt == 6) gr = ld64(k.rs, groff);
            if (kt + 4 < 10) stage(kt + 4);
            SBAR();
        }
        P1_STEP(kt < 9, f0, sn, 0, f1);
    }
#undef P1_RA
#undef P1_RB
#undef P1_STEP
    PRIO(0);
    mfma_fence();
    STAMP(2);
    for (unsigned spins = 0; spins < SPIN_CAP; ++spins) {
        if (__all(gr.y == (unsigned)k.tag)) break;
        gr = ld64(k.rs, groff);
    }
    LAS float* sst = (LAS float*)k.lds;
    if (k.tid < 384) sst[k.tid] = __uint_as_float(gr.x);
    BAR_L();
    const LAS f32x2* st = (const LAS f32x2*)sst + th * 64;
    const LAS float* cs = sst + 256 + cg * 32;
    if (!ISV) {
        const float osc = cb < 5 ? QSCALE : 1.0f;
        const unsigned img = cb < 5 ? WS_Q : WS_K;
        const int cbs = cb % 5;
        unsigned dst[2]; f32x4 c4[2];
#pragma unroll
        for (int n = 0; n < 2; ++n) {
            const int cgi = cbs * 32 + cg * 8 + n * 4 + g, hd = cgi / 5, gi = cgi - 5 * hd;
            const int blk = gi == 4 ? 4 : ((gi & 1) << 1) | (gi >> 1);
            dst[n] = img + (rb * 4 + th * 2) * QK_TILE + hd * 1280 + blk * 256 + t * 8;
            c4[n] = *(const LAS f32x4*)(cs + n * 16 + 4 * g);
        }
#pragma unroll
        for (int m = 0; m < 4; ++m) {
            const f32x2 sm = st[m * 16 + t];
            const float r = sm.y * osc;
#pragma unroll
            for (int n = 0; n < 2; ++n) {
                const f32x4 y = (acc[m][n] - c4[n] * sm.x) * r;
                const unsigned o = dst[n] + (m >> 1) * QK_TILE + (m & 1) * 128;
                st64(k.rs, o, (u32x2){cvtpk(y[0], y[1]), cvtpk(y[2], y[3])});
            }
        }
    } else {
        const int pos = ((g & 1) << 1) | (g >> 1);
        const unsigned dst = WS_V + (rb * 8 + th * 4) * 20480 + ((cb - 10) * 128 + cg * 32 + t) * 32 + pos * 8;
        const float c1[2] = {cs[t], cs[16 + t]};
#pragma unroll
        for (int m = 0; m < 4; ++m) {
            f32x2 sm[4];
#pragma unroll
            for (int i = 0; i < 4; ++i) sm[i] = st[m * 16 + 4 * g + i];
#pragma unroll
            for (int n = 0; n < 2; ++n) {
                float y[4];
#pragma unroll
                for (int i = 0; i < 4; ++i) y[i] = (acc[m][n][i] - c1[n] * sm[i].x) * sm[i].y;
                st64(k.rs, dst + m * 20480 + n * 512, (u32x2){cvtpk(y[0], y[1]), cvtpk(y[2], y[3])});
            }
        }
    }
}

constexpr int AT_KT = 8192, AT_KB = 10240, AT_HALF = AT_KB + 10240, AT_BUF = 2 * AT_HALF;
constexpr int AT_ONES = AT_BUF, AT_ONES_BYTES = 10752, AT_KC = AT_ONES + AT_ONES_BYTES, AT_KC_BYTES = 2048, AT_FLAG = AT_KC + AT_KC_BYTES, WO_LDS = 61440, AT_RED = 0;
DI int at_slot(int i) { return i == 0 ? 0 : (i == 1 ? 61440 : 61440 + AT_BUF); }
struct AttnCtx {
    unsigned voff[5]; const char* gsrc; unsigned ldst, sstride;
    u32x4 qf0[2]; u32x2 qf1[2];
    int kofs, kt, vlane; bool vdata, hi;
};
DI void attn_stage(const AttnCtx& c, int sidx, int slot) {
#pragma unroll
    for (int i = 0; i < 5; ++i) glds16<true>(c.voff[i], c.gsrc + sidx * c.sstride, c.ldst + at_slot(slot) + i * 1024);
}
DI void attn_setup(const Ctx& k, AttnCtx& c, int h) {
    const int lane = k.lane, w = k.w, kh = w >> 2, r = lane & 31, hi = lane >> 5;
    const bool stV = (w >> 1) & 1;
#pragma unroll
    for (int i = 0; i < 5; ++i) {
        const int pc = 5 * (w & 1) + i;
        if (!stV) c.voff[i] = pc < 8 ? pc * QK_TILE + lane * 16 : ((pc - 8) * 4 + (lane >> 4)) * QK_TILE + 1024 + (lane & 15) * 16;
        else { const int ci = 64 * pc + lane, st = ci / 40, wi = ci % 40; c.voff[i] = st * 20480 + ((wi % 20) * 2 + wi / 20) * 16; }
    }
    c.gsrc = k.ws + (stV ? WS_V + h * 640 + kh * 64 * 20480 : WS_K + h * 1280 + kh * 32 * QK_TILE);
    c.sstride = stV ? 16 * 20480 : 8 * QK_TILE;
    c.ldst = k.lds0 + kh * AT_HALF + (stV ? AT_KB : 0) + (w & 1) * 5120;
    c.kofs = kh * AT_HALF + hi * 256 + r * 8;
    c.hi = hi;
    c.kt = hi ? AT_KC + r * 8 : kh * AT_HALF + AT_KT + r * 8;
    c.vdata = r < 20;
    c.vlane = c.vdata ? kh * AT_HALF + AT_KB + hi * 320 + r * 16 : AT_ONES + (r == 20 ? 128 : r < 28 ? 144 : hi ? 16 : 208);
    for (int i = k.tid; i < AT_ONES_BYTES / 4; i += 512) ((LAS unsigned*)(k.lds + AT_ONES))[i] = (i & 4) ? 0u : 0x3F803F80u;
    ((LAS unsigned*)(k.lds + AT_KC))[k.tid] = (k.tid & 1) ? 0u : 0x00003F80u;
    if (k.tid == 0) *(LAS unsigned*)(k.lds + AT_FLAG) = 0;
}
DI void attn_first_loads(const Ctx& k, AttnCtx& c, int h, int qb) {
    const int qg = k.w & 3, r = k.lane & 31, hi = k.lane >> 5;
    attn_stage(c, 0, 0);
#pragma unroll
    for (int i = 0; i < 2; ++i) {
        const unsigned qp = WS_Q + (qb * 8 + qg * 2 + i) * QK_TILE + h * 1280 + r * 8;
        const u32x2 a = ld64(k.rs, qp + hi * 256), b = ld64(k.rs, qp + hi * 256 + 512), e = ld64(k.rs, qp + 1024);
        c.qf0[i] = (u32x4){a.x, a.y, b.x, b.y};
        c.qf1[i] = hi ? (u32x2){0, 0} : e;
    }
    attn_stage(c, 1, 1);
}
DI bf16x8 kfrag0(lds_ptr p) { const u32x2 a = *(const LAS u32x2*)p, b = *(const LAS u32x2*)(p + 512); return as_frag((u32x4){a.x, a.y, b.x, b.y}); }
DI s16x4 kfrag1(lds_ptr p) { return __builtin_bit_cast(s16x4, *(const LAS u32x2*)p); }
DI s16x4 as_frag4(u32x2 v) { return __builtin_bit_cast(s16x4, v); }
DI f32x16 mfma32x8(s16x4 a, s16x4 b, f32x16 c) { return __builtin_amdgcn_mfma_f32_32x32x8bf16_1k(a, b, c, 0, 0, 0); }
DI void wo_prefetch(const Ctx& k, int ct) {
#pragma unroll 5
    for (int j = 0; j < 15; ++j) { int pc = k.w - 1 + 7 * j; if (pc >= 100) pc -= 7; glds16<true>(k.lane * 16, k.ws + WS_WO + ct * 102400 + pc * 1024, k.lds0 + WO_LDS + pc * 1024); }
}
DI void attn_fast(const Ctx& k, AttnCtx& c, const PreTile& pt, f32x16& oA, f32x16& oB, int ct3) {
    const lds_ptr lds = k.lds;
    const int hi = k.lane >> 5, kh = k.w >> 2;
    {
        const bf16x8 k0 = kfrag0(lds + c.kofs - kh * AT_HALF); const s16x4 k1 = kfrag1(lds + (hi ? c.kt : c.kt - kh * AT_HALF));
#pragma unroll
        for (int i = 0; i < 2; ++i) {
            f32x16 s = mfma32(k0, as_frag(c.qf0[i]), (f32x16){});
            s = mfma32x8(k1, as_frag4(c.qf1[i]), s);
            float m = s[0];
#pragma unroll
            for (int j = 1; j < 16; ++j) m = fmaxf(m, s[j]);
            m = swap32_max(m);
            if (hi) c.qf1[i].x = cvtpk(-m, 0.f) & 0xffffu;
        }
    }
    const bf16x8 qA0 = as_frag(c.qf0[0]), qB0 = as_frag(c.qf0[1]); const s16x4 qA1 = as_frag4(c.qf1[0]), qB1 = as_frag4(c.qf1[1]);
    f32x16 sA, sB, nA, nB;
    oA = (f32x16){}; oB = (f32x16){};
    bf16x8 kf0 = kfrag0(lds + c.kofs); s16x4 kf1 = kfrag1(lds + c.kt);
    sA = mfma32(kf0, qA0, (f32x16){}); sB = mfma32(kf0, qB0, (f32x16){});
    sA = mfma32x8(kf1, qA1, sA); sB = mfma32x8(kf1, qB1, sB);
    kf0 = kfrag0(lds + c.kofs + 1024); kf1 = kfrag1(lds + c.kt + 256);
    u32x4 pA0, pA1, pB0, pB1 = {0, 0, 0, 0};
    bf16x8 vf0, vf1 = {0, 0, 0, 0, 0, 0, 0, 0};
    u32x2 fa = {0, 0}, fb = fa, fc = fa, fd = fa;
#define GAP(MF, X, i, P, j) do { MF; SBAR(); X[i] = EX(X[i]); X[i + 1] = EX(X[i + 1]); X[i + 2] = EX(X[i + 2]); X[i + 3] = EX(X[i + 3]); \
        P[j] = cvtpk(X[i], X[i + 1]); P[j + 1] = cvtpk(X[i + 2], X[i + 3]); PIN(P); SBAR(); } while (0)
#define STEP(SA, SB, NA, NB, VP, KP, KT, HK) do { \
        GAP(NA = mfma32(kf0, qA0, (f32x16){}), SA, 0, pA0, 0); vf0 = as_frag(lds_r128(VP)); HK(0); SBAR(); \
        GAP(oB = mfma32(vf1, as_frag(pB1), oB), SA, 4, pA0, 2); vf1 = as_frag(lds_r128((VP) + 640)); HK(1); SBAR(); \
        GAP(NB = mfma32(kf0, qB0, (f32x16){}), SA, 8, pA1, 0); HK(2); SBAR(); \
        GAP(NA = mfma32x8(kf1, qA1, NA), SA, 12, pA1, 2); HK(3); SBAR(); \
        GAP(NB = mfma32x8(kf1, qB1, NB), SB, 0, pB0, 0); kf0 = kfrag0(KP); kf1 = kfrag1(KT); HK(4); SBAR(); \
        GAP(oA = mfma32(vf0, as_frag(pA0), oA), SB, 4, pB0, 2); HK(5); SBAR(); \
        GAP(oA = mfma32(vf1, as_frag(pA1), oA), SB, 8, pB1, 0); HK(6); SBAR(); \
        GAP(oB = mfma32(vf0, as_frag(pB0), oB), SB, 12, pB1, 2); HK(7); SBAR(); \
    } while (0)
#define HK_NONE(j)
#define STAGE(sidx, slot, nslot, PT, NST, HA, HB) do { \
        const int sb = at_slot(slot), sn = at_slot(nslot); \
        const lds_ptr vb = lds + (c.vdata ? c.vlane + sb : c.vlane); \
        lds_ptr k0 = lds + c.kofs + sb, k0n = lds + c.kofs + sn, k1 = lds + (hi ? c.kt : c.kt + sb), k1n = lds + (hi ? c.kt : c.kt + sn); \
        PIN(k0); PIN(k0n); PIN(k1); PIN(k1n); \
        STEP(sA, sB, nA, nB, vb, k0 + 2 * 1024, k1 + 2 * 256, HK_NONE); PT(0); \
        STEP(nA, nB, sA, sB, vb + 1280, k0 + 3 * 1024, k1 + 3 * 256, HK_NONE); PT(1); \
        STEP(sA, sB, nA, nB, vb + 2 * 1280, k0 + 4 * 1024, k1 + 4 * 256, HK_NONE); PT(2); \
        STEP(nA, nB, sA, sB, vb + 3 * 1280, k0 + 5 * 1024, k1 + 5 * 256, HK_NONE); PT(3); \
        STEP(sA, sB, nA, nB, vb + 4 * 1280, k0 + 6 * 1024, k1 + 6 * 256, HK_NONE); PT(4); \
        STEP(nA, nB, sA, sB, vb + 5 * 1280, k0 + 7 * 1024, k1 + 7 * 256, HK_NONE); \
        if ((sidx) == 3 && k.w == 0) {     \
            const unsigned tl = (unsigned)k.tag, th = (unsigned)(k.tag >> 32); \
            if (!__all(fa.x == tl && fa.y == th && fb.x == tl && fb.y == th && fc.x == tl && fc.y == th && fd.x == tl && fd.y == th)) poll_all(k, WS_FP); \
        } \
        WAIT_BAR(NST); \
        if ((sidx) < 2) attn_stage(c, (sidx) + 2, (nslot) == 2 ? 0 : (nslot) + 1); \
        if ((sidx) == 1 && k.tid == 0) st_flag(k.ws + WS_FP + k.c * FLAG_STRIDE, k.tag);     \
        if ((sidx) == 2 && k.w == 0) {     \
            const unsigned o = WS_FP + k.lane * FLAG_STRIDE; \
            fa = ld64(k.rs, o); fb = ld64(k.rs, o + 64 * FLAG_STRIDE); fc = ld64(k.rs, o + 128 * FLAG_STRIDE); fd = ld64(k.rs, o + 192 * FLAG_STRIDE); \
        } \
        SBAR(); \
        STEP(sA, sB, nA, nB, vb + 6 * 1280, k0n, k1n, HA); \
        STEP(nA, nB, sA, sB, vb + 7 * 1280, k0n + 1024, k1n + 256, HB); \
    } while (0)
#define PT_STORE(j) do { pretile_store(k, pt, j); SBAR(); } while (0)
#define PT_NONE(j)
    const char* wo_src = k.ws + WS_WO + ct3 * 102400;
#define WO_PIECE(n) do { int pc_ = k.w + 8 * (n); if (pc_ >= 100) pc_ -= 8; glds16<true>(k.lane * 16, wo_src + pc_ * 1024, k.lds0 + WO_LDS + pc_ * 1024); } while (0)
#define HK_WO_A(j) WO_PIECE(j)
#define HK_WO_B(j) do { if ((j) < 5) WO_PIECE(8 + (j)); } while (0)
    PRIO(1);
    STAGE(0, 0, 1, PT_STORE, 5, HK_NONE, HK_NONE);
    STAGE(1, 1, 2, PT_NONE, 0, HK_NONE, HK_NONE);
    STAGE(2, 2, 0, PT_NONE, 0, HK_NONE, HK_NONE);
    STAGE(3, 0, 1, PT_NONE, 0, HK_WO_A, HK_WO_B);
#undef STAGE
#undef STEP
#undef PT_STORE
#undef PT_NONE
#undef WO_PIECE
#undef HK_WO_A
#undef HK_WO_B
#undef HK_NONE
    PRIO(0);
    oB = mfma32(vf1, as_frag(pB1), oB);
#undef GAP
}
DI void attn_safe(const Ctx& k, AttnCtx& c, f32x16 (&o)[2], float (&mrun)[2]) {
    const lds_ptr lds = k.lds;
    attn_stage(c, 0, 0);
    if (k.lane >> 5) { c.qf1[0].x = 0; c.qf1[1].x = 0; }
    o[0] = (f32x16){}; o[1] = (f32x16){};
    mrun[0] = -INFINITY; mrun[1] = -INFINITY;
    for (int sidx = 0; sidx < 4; ++sidx) {
        const int sb = at_slot(sidx & 1);
        WAIT_BAR(0);
        if (sidx < 3) attn_stage(c, sidx + 1, (sidx + 1) & 1);
        const lds_ptr vb = lds + (c.vdata ? c.vlane + sb : c.vlane);
        for (int kt = 0; kt < 8; ++kt) {
            const bf16x8 k0 = kfrag0(lds + c.kofs + sb + kt * 1024); const s16x4 k1 = kfrag1(lds + (c.hi ? c.kt : c.kt + sb) + kt * 256);
            const bf16x8 v0 = as_frag(lds_r128(vb + kt * 1280)), v1 = as_frag(lds_r128(vb + kt * 1280 + 640));
#pragma unroll
            for (int i = 0; i < 2; ++i) {
                f32x16 s = mfma32(k0, as_frag(c.qf0[i]), (f32x16){});
                s = mfma32x8(k1, as_frag4(c.qf1[i]), s);
                float m = s[0];
#pragma unroll
                for (int j = 1; j < 16; ++j) m = fmaxf(m, s[j]);
                m = fmaxf(swap32_max(m), mrun[i]);
                const float alpha = EX(mrun[i] - m);
                mrun[i] = m;
                unsigned pk[8];
#pragma unroll
                for (int j = 0; j < 16; ++j) o[i][j] *= alpha;
#pragma unroll
                for (int j = 0; j < 8; ++j) pk[j] = cvtpk(EX(s[2 * j] - m), EX(s[2 * j + 1] - m));
                o[i] = mfma32(v0, as_frag((u32x4){pk[0], pk[1], pk[2], pk[3]}), o[i]);
                o[i] = mfma32(v1, as_frag((u32x4){pk[4], pk[5], pk[6], pk[7]}), o[i]);
            }
        }
    }
}
DI void p2_phase(const Ctx& k, AttnCtx& c, int h, int qb, int ct3) {
    const int lane = k.lane, qg = k.w & 3, kh = k.w >> 2, r = lane & 31, hi = lane >> 5;
    const lds_ptr lds = k.lds;
    attn_first_loads(k, c, h, qb);
    PreTile pt;
    pretile_read(k, pt);
    WAIT_BAR(5);
    STAMP(6);
    f32x16 o[2];
    attn_fast(k, c, pt, o[0], o[1], ct3);
    STAMP(7);
    LAS unsigned* flag = (LAS unsigned*)(lds + AT_FLAG);
    if (__any(hi && !(o[0][8] <= 5.764607523034235e17f && o[1][8] <= 5.764607523034235e17f)) && lane == 0) *flag = 1;
    BAR_L();
    const lds_ptr red = lds + AT_RED + qg * 8192 + lane * 16;
    auto put = [&](const f32x16& v, int slot) {
#pragma unroll
        for (int u = 0; u < 3; ++u) *(LAS f32x4*)(red + slot * 4096 + u * 1024) = (f32x4){v[4 * u], v[4 * u + 1], v[4 * u + 2], v[4 * u + 3]};
    };
    auto get = [&](int slot, int u) { return *(const LAS f32x4*)(red + slot * 4096 + u * 1024); };
    f32x4 of[3];
    if (*flag == 0) {
        if (kh) put(o[0], 0); else put(o[1], 1);
        BAR_L();
#pragma unroll
        for (int u = 0; u < 3; ++u) {
            const f32x4 p = get(kh, u);
#pragma unroll
            for (int j = 0; j < 4; ++j) of[u][j] = p[j] + (kh ? o[1][4 * u + j] : o[0][4 * u + j]);
        }
    } else {
        float mref[2];
        WAIT_BAR(0);
        attn_safe(k, c, o, mref);
        BAR_L();
        if (kh) put(o[0], 0); else put(o[1], 1);
        *(LAS float*)(red + (kh ? 0 : 4096) + 3072) = kh ? mref[0] : mref[1];
        BAR_L();
        const float m0 = kh ? mref[1] : mref[0], m1 = *(const LAS float*)(red + kh * 4096 + 3072), m = fmaxf(m0, m1);
        const float a0 = EX(m0 - m), a1 = EX(m1 - m);
#pragma unroll
        for (int u = 0; u < 3; ++u) {
            const f32x4 p = get(kh, u);
#pragma unroll
            for (int j = 0; j < 4; ++j) of[u][j] = p[j] * a1 + (kh ? o[1][4 * u + j] : o[0][4 * u + j]) * a0;
        }
        BAR_L();
        if (k.w) wo_prefetch(k, ct3);
    }
    {
        auto lr = __builtin_amdgcn_permlane32_swap(__float_as_uint(of[2][0]), __float_as_uint(of[2][0]), false, false);
        const float inv = 1.0f / __uint_as_float(lr[1]);
        const int tok = qb * 256 + qg * 64 + kh * 32 + r;
        const unsigned base = WS_O + (tok >> 4) * OH_TILE + (tok & 15) * 16;
        unsigned pk[3][2];
#pragma unroll
        for (int u = 0; u < 3; ++u) { pk[u][0] = cvtpk(of[u][0] * inv, of[u][1] * inv); pk[u][1] = cvtpk(of[u][2] * inv, of[u][3] * inv); }
        auto oaddr = [&](int col) { return base + (col >> 5) * 1024 + ((col >> 3) & 3) * 256 + (col & 7) * 2; };
        if ((h & 1) == 0) {
            auto s0 = __builtin_amdgcn_permlane32_swap(pk[0][0], pk[1][0], false, false);
            auto s1 = __builtin_amdgcn_permlane32_swap(pk[0][1], pk[1][1], false, false);
            st128(k.rs, oaddr(h * 20 + 8 * hi), (u32x4){s0[0], s1[0], s0[1], s1[1]});
            if (!hi) st64(k.rs, oaddr(h * 20 + 16), (u32x2){pk[2][0], pk[2][1]});
        } else {
            auto s0 = __builtin_amdgcn_permlane32_swap(pk[0][0], pk[2][0], false, false);
            auto s1 = __builtin_amdgcn_permlane32_swap(pk[0][1], pk[2][1], false, false);
            st128(k.rs, oaddr(h * 20 + 4 + 8 * hi), hi ? (u32x4){pk[1][0], pk[1][1], s0[0], s1[0]} : (u32x4){s0[1], s1[1], pk[1][0], pk[1][1]});
            if (!hi) st64(k.rs, oaddr(h * 20), (u32x2){s0[0], s1[0]});
        }
    }
}

DI int own_cf(int kh, int u) { return kh ? (u < 2 ? 3 + u : 4) : u; }
constexpr int RED_LDS = 143360;
struct P3Pre { u32x2 xv[5]; };
DI void p3_pre(const Ctx& k, int rb64, int ct, P3Pre& p) {
    const int tt = k.w & 3, t = k.lane & 15, g = k.lane >> 4;
    if (k.w < 4) {
#pragma unroll
        for (int cf = 0; cf < 5; ++cf) p.xv[cf] = *(const u32x2*)(k.x + (size_t)(rb64 * 64 + tt * 16 + t) * D + ct * 80 + cf * 16 + 4 * g);
    }
}
DI void w13_prefetch(const Ctx& k, int f0, int f);
DI void p3_phase(const Ctx& k, int rb64, int ct, int f0, const P3Pre& pre, u32x2 (&hv)[3]) {
    const int lane = k.lane, tt = k.w & 3, kh = k.w >> 2, t = lane & 15, g = lane >> 4;
    const unsigned ap = WS_O + (rb64 * 4 + tt) * OH_TILE + kh * 10240 + lane * 16;
    const lds_ptr wl = k.lds + WO_LDS + kh * 51200 + lane * 16;
    constexpr int WIN = 5;
    u32x4 a[10];
#pragma unroll
    for (int i = 0; i < WIN; ++i) a[i] = ld128(k.rs, ap + i * 1024);
    bf16x8 wf[2][5];
#pragma unroll
    for (int cf = 0; cf < 5; ++cf) wf[0][cf] = as_frag(lds_r128(wl + cf * 1024));
    f32x4 acc[5];
#pragma unroll
    for (int cf = 0; cf < 5; ++cf) { acc[cf] = (f32x4){0, 0, 0, 0}; PIN(acc[cf]); }
    mfma_fence();
    PRIO(1);
#pragma unroll
    for (int i = 0; i < 10; ++i) {
        if (i + WIN < 10) { a[i + WIN] = ld128(k.rs, ap + (i + WIN) * 1024); SBAR(); }
#pragma unroll
        for (int cf = 0; cf < 5; ++cf) {
            mfma16_acc(acc[cf], wf[i & 1][cf], as_frag(a[i])); SBAR();
            if (i + 1 < 10) wf[(i + 1) & 1][cf] = as_frag(lds_r128(wl + (i + 1) * 5120 + cf * 1024));
            if (cf < 4) PIN(wf[i & 1][cf + 1]); else if (i + 1 < 10) PIN(wf[(i + 1) & 1][0]);
            SBAR();
        }
    }
    PRIO(0);
    mfma_fence();
    STAMP(10);
    BAR_L();
    const lds_ptr red = k.lds + RED_LDS + tt * 5120 + lane * 16;
    if (kh) {
#pragma unroll
        for (int cf = 0; cf < 5; ++cf) *(LAS f32x4*)(red + cf * 1024) = acc[cf];
        BAR_L();
        w13_prefetch(k, f0, k.w - 1);
    } else {
        BAR_L();
        const int tok = rb64 * 64 + tt * 16 + t;
        float s = 0.f, q = 0.f;
        u32x2 hq[5];
#pragma unroll
        for (int cf = 0; cf < 5; ++cf) {
            const f32x4 v = acc[cf] + *(const LAS f32x4*)(red + cf * 1024);
            hq[cf] = (u32x2){cvtpk(v[0] + bflo(pre.xv[cf].x), v[1] + bfhi(pre.xv[cf].x)), cvtpk(v[2] + bflo(pre.xv[cf].y), v[3] + bfhi(pre.xv[cf].y))};
            const int col = ct * 80 + cf * 16 + 4 * g;
            st64(k.rs, WS_H + (tok >> 4) * OH_TILE + (col >> 5) * 1024 + ((col >> 3) & 3) * 256 + t * 16 + (col & 7) * 2, hq[cf]);
            const float h0 = bflo(hq[cf].x), h1 = bfhi(hq[cf].x), h2 = bflo(hq[cf].y), h3 = bfhi(hq[cf].y);
            s += (h0 + h1) + (h2 + h3); q += (h0 * h0 + h1 * h1) + (h2 * h2 + h3 * h3);
        }
        s = sum_x32(sum_x16(s)); q = sum_x32(sum_x16(q));
        if (g == 0) st64(k.rs, WS_PST + (tok >> 4) * 2048 + (ct * 2) * 128 + t * 8, (u32x2){__float_as_uint(s), __float_as_uint(q)});
        if (g == 1) st64(k.rs, WS_PST + (tok >> 4) * 2048 + (ct * 2 + 1) * 128 + t * 8, (u32x2){0u, 0u});
        hv[0] = hq[0]; hv[1] = hq[1]; hv[2] = hq[2];
        const lds_ptr stash = k.lds + RED_LDS + tt * 5120 + lane * 8;
        *(LAS u32x2*)(stash + 3072) = hq[3]; *(LAS u32x2*)(stash + 4096) = hq[4];
    }
}
DI void pair_reduce(lds_ptr red0, int tt, int kh, int lane, const f32x4 (&acc)[5], f32x4 (&own)[3]) {
    const lds_ptr red = red0 + tt * 5120 + lane * 16;
    if (kh == 0) { *(LAS f32x4*)(red + 3072) = acc[3]; *(LAS f32x4*)(red + 4096) = acc[4]; }
    else { *(LAS f32x4*)(red) = acc[0]; *(LAS f32x4*)(red + 1024) = acc[1]; *(LAS f32x4*)(red + 2048) = acc[2]; }
    BAR_L();
    if (kh == 0) {
#pragma unroll
        for (int u = 0; u < 3; ++u) own[u] = acc[u] + *(const LAS f32x4*)(red + u * 1024);
    } else {
        own[0] = acc[3] + *(const LAS f32x4*)(red + 3072); own[1] = acc[4] + *(const LAS f32x4*)(red + 4096); own[2] = own[1];
    }
}
template <int N, int NP>
DI void w2_stage(const Ctx& k, int ct, int b, int s0) {
#pragma unroll
    for (int j = 0; j < NP; ++j) {
        int q = k.w + 8 * j;
        if (q >= 10 * N) q -= 8;
        const int hf = q >= 5 * N, rem = q - hf * 5 * N;
        glds16<true>(k.lane * 16, k.ws + WS_W2 + (ct * 54 + hf * 27 + s0) * 5120 + rem * 1024, k.lds0 + b * 71680 + hf * 35840 + rem * 1024);
    }
}
constexpr int CNT_LDS = RED_LDS + 448, DUMMY_LDS = RED_LDS + 3584;
DI void pair_signal(const Ctx& k, int cnt, unsigned flagoff) {
    LAS unsigned* p = (LAS unsigned*)(k.lds + (k.lane == 0 ? CNT_LDS + cnt * 4 : DUMMY_LDS + k.lane * 4));
    const unsigned old = __hip_atomic_fetch_add(p, 1u, __ATOMIC_RELAXED, __HIP_MEMORY_SCOPE_WORKGROUP);
    const bool last = rfl(old) == 1;
    st64(k.rs, last && k.lane == 0 ? flagoff : 0x80000000u, (u32x2){(unsigned)k.tag, (unsigned)(k.tag >> 32)});
}
DI void p5_phase(const Ctx& k, int rb64, int ct, u32x2 (&hv)[3], unsigned f4b_pub) {
    const int lane = k.lane, tt = k.w & 3, kh = k.w >> 2, t = lane & 15, g = lane >> 4;
    const unsigned ap = WS_HH + (rb64 * 4 + tt) * HH_TILE + kh * 27 * 1024 + lane * 16;
    const unsigned f4b = WS_F4B + (((rb64 >> 2) * 32 + (lane & 31)) * 4 + (rb64 & 3)) * FLAG_STRIDE;
    constexpr int WIN = 6;
    u32x4 a[27];
#pragma unroll
    for (int i = 0; i < WIN; ++i) ald128(a[i], k.rsw, ap, i * 1024);
    w2_stage<7, 9>(k, ct, 1, 7);
    if (k.w >= 4) {
        WAIT_VM(15);
        pair_signal(k, 4 + (k.w >> 1), f4b_pub);
    }
    if (kh) {
        const lds_ptr stash = k.lds + RED_LDS + tt * 5120 + lane * 8;
        hv[0] = *(const LAS u32x2*)(stash + 3072); hv[1] = *(const LAS u32x2*)(stash + 4096); hv[2] = hv[1];
    }
    lds_ptr wb[2] = {k.lds + lane * 16 + kh * 35840, k.lds + lane * 16 + kh * 35840 + 71680};
    PIN(wb[0]); PIN(wb[1]);
    auto wread = [&](int n, int cf) { return as_frag(lds_r128(wb[(n / 7) & 1] + (n % 7) * 5120 + cf * 1024)); };
    bf16x8 wf[2][5];
#pragma unroll
    for (int cf = 0; cf < 5; ++cf) wf[0][cf] = wread(0, cf);
    f32x4 acc[5];
#pragma unroll
    for (int cf = 0; cf < 5; ++cf) { acc[cf] = (f32x4){0, 0, 0, 0}; PIN(acc[cf]); }
    u32x2 fl;
    mfma_fence();
    PRIO(1);
#define P5_STEP(i, NW) do { \
        if ((i) == 6) { WAIT_BAR(6); }                                                      \
        if ((i) == 7) { BAR_L(); ald64(fl, k.rsw, f4b); w2_stage<7, 9>(k, ct, 0, 14); } \
        if ((i) == 10) {     \
            asm volatile("s_waitcnt vmcnt(12)" : "+v"(fl) :: "memory"); \
            if (!__all(fl.x == (unsigned)k.tag && fl.y == (unsigned)(k.tag >> 32))) poll_flags(k, f4b); \
        } \
        if ((i) == 13) { WAIT_BAR(6); w2_stage<6, 8>(k, ct, 1, 21); } \
        if ((i) == 20) { WAIT_BAR(6); } \
        if ((i) + WIN < 27) ald128(a[(i) + WIN], k.rsw, ap, ((i) + WIN) * 1024); \
        ald_wait<NW>(a[i]); SBAR(); \
        _Pragma("unroll") for (int cf = 0; cf < 5; ++cf) { \
            mfma16_acc(acc[cf], wf[(i) & 1][cf], as_frag(a[i])); SBAR(); \
            if ((i) + 1 < 27) wf[((i) + 1) & 1][cf] = wread((i) + 1, cf); \
            if (cf < 4) PIN(wf[(i) & 1][cf + 1]); else if ((i) + 1 < 27) PIN(wf[((i) + 1) & 1][0]); \
            SBAR(); } } while (0)
    P5_STEP(0, 15); P5_STEP(1, 15); P5_STEP(2, 15); P5_STEP(3, 15); P5_STEP(4, 15); P5_STEP(5, 15); P5_STEP(6, 6);
    P5_STEP(7, 15); P5_STEP(8, 15); P5_STEP(9, 15); P5_STEP(10, 15); P5_STEP(11, 15); P5_STEP(12, 15);
    P5_STEP(13, 14); P5_STEP(14, 14); P5_STEP(15, 14); P5_STEP(16, 14); P5_STEP(17, 14); P5_STEP(18, 14); P5_STEP(19, 6);
    P5_STEP(20, 6); P5_STEP(21, 5); P5_STEP(22, 4); P5_STEP(23, 3); P5_STEP(24, 2); P5_STEP(25, 1); P5_STEP(26, 0);
#undef P5_STEP
    PRIO(0);
    mfma_fence();
    STAMP(16);
    f32x4 own[3];
    pair_reduce(k.lds + RED_LDS, tt, kh, lane, acc, own);
    const int tok = rb64 * 64 + tt * 16 + t;
#pragma unroll
    for (int u = 0; u < 3; ++u) {
        if (u < 2 || kh == 0) {
            const int col = ct * 80 + own_cf(kh, u) * 16 + 4 * g;
            *(u32x2*)(k.out + (size_t)tok * D + col) = (u32x2){cvtpk(own[u][0] + bflo(hv[u].x), own[u][1] + bfhi(hv[u].x)), cvtpk(own[u][2] + bflo(hv[u].y), own[u][3] + bfhi(hv[u].y))};
        }
    }
}

constexpr int P4_CS = 143360;
DI void w13_prefetch(const Ctx& k, int f0, int f) {
    const int p0 = f * 20;
#pragma unroll 4
    for (int j = 0; j < 20; ++j) glds16<true>(k.lane * 16, k.ws + WS_W13 + f0 * 20480 + (p0 + j) * 1024, k.lds0 + (p0 + j) * 1024);
}
DI void p4_colsum(const Ctx& k, int f) {
    const int t = k.lane & 15, g = k.lane >> 4;
    u32x4 wv[20];
#pragma unroll
    for (int s = 0; s < 20; ++s) wv[s] = lds_r128(k.lds + (f * 20 + s) * 1024 + k.lane * 16);
    f32x4 c0 = {0, 0, 0, 0}, c1 = {0, 0, 0, 0};
    const bf16x8 ones = {0x3F80, 0x3F80, 0x3F80, 0x3F80, 0x3F80, 0x3F80, 0x3F80, 0x3F80};
#pragma unroll
    for (int s = 0; s < 20; s += 2) { c0 = mfma16(as_frag(wv[s]), ones, c0); c1 = mfma16(as_frag(wv[s + 1]), ones, c1); }
    if (t == 0) *(LAS f32x4*)(k.lds + P4_CS + f * 64 + g * 16) = c0 + c1;
}
template <int NF>
DI void p4_swiglu(const Ctx& k, const f32x4 (&acc)[2][NF], int fbase, const float (&mean)[2], const float (&rstd)[2], const float (&vpe)[2], unsigned (&d)[2][4]) {
    const int g = k.lane >> 4;
#pragma unroll
    for (int f = 0; f < NF; ++f) {
        const f32x4 cs = *(const LAS f32x4*)(k.lds + P4_CS + (fbase + f) * 64 + g * 16);
#pragma unroll
        for (int i = 0; i < 2; ++i) {
            const float ce = -LOG2E * rstd[i], nm = -mean[i];
            const f32x2 ce2 = {ce, ce}, ir22 = {vpe[i], vpe[i]}, nm2 = {nm, nm};
            const f32x2 ga = __builtin_elementwise_fma(nm2, __builtin_shufflevector(cs, cs, 0, 1), __builtin_shufflevector(acc[i][f], acc[i][f], 0, 1));
            const f32x2 ua = __builtin_elementwise_fma(nm2, __builtin_shufflevector(cs, cs, 2, 3), __builtin_shufflevector(acc[i][f], acc[i][f], 2, 3));
            const f32x2 t2 = ga * ce2;
            const f32x2 e2 = {EX(t2.x), EX(t2.y)};
            const f32x2 d2 = __builtin_elementwise_fma(e2, ir22, ir22);
            const f32x2 r2 = {__builtin_amdgcn_rcpf(d2.x), __builtin_amdgcn_rcpf(d2.y)};
            const f32x2 o2 = (ga * ua) * r2;
            d[i][f] = cvtpk(o2.x, o2.y);
        }
    }
}
DI u32x4 p4_transpose(const unsigned (&d)[4]) {
    auto a = __builtin_amdgcn_permlane32_swap(d[0], d[2], false, false);
    auto b = __builtin_amdgcn_permlane32_swap(d[1], d[3], false, false);
    auto e = __builtin_amdgcn_permlane16_swap(a[0], b[0], false, false);
    auto f = __builtin_amdgcn_permlane16_swap(a[1], b[1], false, false);
    return (u32x4){e[0], e[1], f[0], f[1]};
}
DI void p4_phase(const Ctx& k, int rb, int cb, int ct5) {
    const int lane = k.lane, w = k.w, t = lane & 15, g = lane >> 4;
    const lds_ptr lds = k.lds;
    const unsigned ap = WS_H + (rb * 16 + w * 2) * OH_TILE + lane * 16;
    u32x4 a0[20], a1[20];
#define P4_LD(s) do { a0[s] = ld128(k.rs, ap + (s) * 1024); a1[s] = ld128(k.rs, ap + OH_TILE + (s) * 1024); } while (0)
    u32x2 pv[2][4];
#pragma unroll
    for (int i = 0; i < 2; ++i)
#pragma unroll
        for (int j = 0; j < 4; ++j) pv[i][j] = ld64(k.rs, WS_PST + (rb * 16 + w * 2 + i) * 2048 + (4 * g + j) * 128 + t * 8);
    P4_LD(0); P4_LD(1); P4_LD(2); P4_LD(3);
    lds_ptr wb[3] = {lds + lane * 16, lds + lane * 16 + 61440, lds + lane * 16 + 122880};
    PIN(wb[1]); PIN(wb[2]);
    auto wread = [&](int f, int s) { return as_frag(lds_r128(wb[f / 3] + (f % 3) * 20480 + s * 1024)); };
    bf16x8 wf[2][4];
#pragma unroll
    for (int f = 0; f < 4; ++f) wf[0][f] = wread(f, 0);
    f32x4 acc[2][4];
#pragma unroll
    for (int f = 0; f < 4; ++f) { acc[0][f] = (f32x4){0, 0, 0, 0}; acc[1][f] = (f32x4){0, 0, 0, 0}; PIN(acc[0][f]); PIN(acc[1][f]); }
    float mean[2], rstd[2], vpe[2];
    mfma_fence();
    STAMP(13);
    PRIO(1);
#pragma unroll
    for (int s = 0; s < 20; ++s) {
        if (s + 4 < 20) P4_LD(s + 4);
        SBAR();
        if (s == 4) {
#pragma unroll
            for (int i = 0; i < 2; ++i) {
                float sm = (__uint_as_float(pv[i][0].x) + __uint_as_float(pv[i][1].x)) + (__uint_as_float(pv[i][2].x) + __uint_as_float(pv[i][3].x));
                float q = (__uint_as_float(pv[i][0].y) + __uint_as_float(pv[i][1].y)) + (__uint_as_float(pv[i][2].y) + __uint_as_float(pv[i][3].y));
                sm = sum_x32(sum_x16(sm)); q = sum_x32(sum_x16(q));
                mean[i] = sm * (1.0f / D);
                vpe[i] = fmaxf(q * (1.0f / D) - mean[i] * mean[i], 0.f) + LN_EPS;
                rstd[i] = __builtin_amdgcn_rsqf(vpe[i]);
            }
            PIN(mean[0]); PIN(mean[1]); PIN(rstd[0]); PIN(rstd[1]); PIN(vpe[0]); PIN(vpe[1]);
            SBAR();
        }
#pragma unroll
        for (int f = 0; f < 4; ++f) {
            mfma16_acc(acc[0][f], wf[s & 1][f], as_frag(a0[s])); SBAR();
            if (s + 1 < 20) wf[(s + 1) & 1][f] = wread(f, s + 1); else if (f < 3) wf[0][f] = wread(4 + f, 0);
            if (f < 3) PIN(wf[s & 1][f + 1]); else if (s + 1 < 20) PIN(wf[(s + 1) & 1][0]);
            SBAR();
            mfma16_acc(acc[1][f], wf[s & 1][f], as_frag(a1[s])); SBAR();
        }
    }
#undef P4_LD
    PRIO(0);
    mfma_fence();
    STAMP(33);
    const unsigned hhb = WS_HH + (rb * 16 + w * 2) * HH_TILE + lane * 16;
    auto epilogue1 = [&]() {
        unsigned d[2][4];
        p4_swiglu<4>(k, acc, 0, mean, rstd, vpe, d);
#pragma unroll
        for (int i = 0; i < 2; ++i) st128(k.rs, hhb + i * HH_TILE + ((cb >> 4) * 27 + (cb & 15)) * 1024, p4_transpose(d[i]));
    };
    {
        LAS unsigned* p = (LAS unsigned*)(lds + (lane == 0 ? CNT_LDS + 32 : DUMMY_LDS + lane * 4));
        (void)__hip_atomic_fetch_add(p, 1u, __ATOMIC_RELAXED, __HIP_MEMORY_SCOPE_WORKGROUP);
    }
    epilogue1();
    SBAR();
    unsigned touched;
    f32x4 acc2[2][3];
#pragma unroll
    for (int f = 0; f < 3; ++f) { acc2[0][f] = (f32x4){0, 0, 0, 0}; acc2[1][f] = (f32x4){0, 0, 0, 0}; PIN(acc2[0][f]); PIN(acc2[1][f]); }
    mfma_fence();
    PRIO(1);
#pragma unroll
    for (int s = 0; s < 20; ++s) {
        if (s == 17) { WAIT_VM(1); pair_signal(k, w >> 1, WS_F4A + ((rb * 32 + cb) * 4 + (w >> 1)) * FLAG_STRIDE); SBAR(); }
        if (s == 2) {
            touch_lines(touched, k.rsw, WS_W2 + ct5 * 276480 + ((k.lb >> 3) * 540 + w * 64 + lane) * 128);
            SBAR();
        }
#pragma unroll
        for (int f = 0; f < 3; ++f) {
            mfma16_acc(acc2[0][f], wf[s & 1][f], as_frag(a0[s])); SBAR();
            if (s + 1 < 20) wf[(s + 1) & 1][f] = wread(4 + f, s + 1);
            if (f < 2) PIN(wf[s & 1][f + 1]); else if (s + 1 < 20) PIN(wf[(s + 1) & 1][0]);
            SBAR();
            mfma16_acc(acc2[1][f], wf[s & 1][f], as_frag(a1[s])); SBAR();
        }
    }
    PRIO(0);
    mfma_fence();
    asm volatile("s_waitcnt vmcnt(0)" :: "v"(touched) : "memory");
    STAMP(14);
    {
        unsigned d[2][4];
        p4_swiglu<3>(k, acc2, 4, mean, rstd, vpe, *(unsigned (*)[2][4])&d);
        d[0][3] = 0; d[1][3] = 0;
        const int n2 = cb < 24 ? 3 : 2, l = (cb < 24 ? 3 * cb : 72 + 2 * (cb - 24)) + g, ls = l >> 2;
        const unsigned pos = ((ls >= 11 ? 32 + ls : 16 + ls) * 4 + (l & 3)) * 256 + t * 16;
#pragma unroll
        for (int i = 0; i < 2; ++i) st128(k.rs, g < n2 ? WS_HH + (rb * 16 + w * 2 + i) * HH_TILE + pos : 0x80000000u, p4_transpose(d[i]));
    }
    if (w < 4) {
        for (unsigned spins = 0; spins < SPIN_CAP; ++spins) {
            if (rfl(*(volatile LAS unsigned*)(lds + CNT_LDS + 32)) == 8) break;
            __builtin_amdgcn_s_sleep(1);
        }
#pragma unroll 6
        for (int j = 0; j < 18; ++j) {
            int q = w + 4 * j;
            if (q >= 70) q -= 4;
            const int hf = q >= 35, rem = q - hf * 35;
            glds16<true>(lane * 16, k.ws + WS_W2 + (ct5 * 54 + hf * 27) * 5120 + rem * 1024, k.lds0 + hf * 35840 + rem * 1024);
        }
        WAIT_VM(18);
        pair_signal(k, 4 + (w >> 1), WS_F4B + ((rb * 32 + cb) * 4 + (w >> 1)) * FLAG_STRIDE);
        WAIT_VM(0);
    }
}

constexpr int LDS_BYTES = 163840;
__global__ __launch_bounds__(512, 2) void fused_block(const uint16_t* __restrict__ x, const uint16_t* __restrict__ wqkv, const uint16_t* __restrict__ wo,
                                                      const uint16_t* __restrict__ w1, const uint16_t* __restrict__ w3, const uint16_t* __restrict__ w2,
                                                      char* __restrict__ ws, uint16_t* __restrict__ out) {
    extern __shared__ __attribute__((aligned(16))) char smem[];
    Ctx k;
    k.x = x; k.wqkv = wqkv; k.wo = wo; k.w1 = w1; k.w3 = w3; k.w2 = w2; k.ws = ws; k.out = out;
    k.rs = __builtin_amdgcn_make_buffer_rsrc(ws, 0, WS_END + 20480, 0x00020000);
    k.lds = (lds_ptr)smem; k.lds0 = (unsigned)(uintptr_t)smem;
    k.c = blockIdx.x; k.xcd = k.c & 7; k.lb = k.c >> 3;
    k.tid = threadIdx.x; k.lane = k.tid & 63; k.w = rfl(k.tid >> 6);
    k.rsw = (u32x4){(unsigned)(uintptr_t)ws, (unsigned)((uintptr_t)ws >> 32) & 0xffffu, WS_END + 20480, 0x00020000u};
    k.tag = 0;
    k.sc = 0; k.sr = 0;
    STAMP(0);
    startup_load(k);
    const int rb1 = (k.xcd >> 2) * 8 + (k.lb & 7), cb1 = (k.xcd & 3) * 4 + (k.lb >> 3);
    if (cb1 < 15) {
        if (cb1 >= 10) p1_body<true>(k, rb1, cb1); else p1_body<false>(k, rb1, cb1);
        STAMP(3);
        WAIT_BAR(0);
        if (k.tid == 0) st_flag(ws + WS_F1 + (rb1 * 16 + cb1) * FLAG_STRIDE, k.tag);
    } else {
        WAIT_VM(0);
        startup_tag(k);
        ln1_stats(k, rb1);
        if (rb1 < 15) colsum_qkv(k, rb1);
        WAIT_BAR(0);
    }
    STAMP(4);
    if (k.w) pretile_load(k);
    const int h = k.xcd * 4 + (k.lb & 3), qb = k.lb >> 2;
    const int rb64 = k.xcd * 4 + (k.lb >> 3), ct = k.lb & 7;
    const int rb4 = (k.xcd >> 1) * 2 + (k.lb >> 4), cb4 = (k.xcd & 1) * 16 + (k.lb & 15);
    const int f0 = p4_f0(cb4);
    AttnCtx ac;
    attn_setup(k, ac, h);
    if (k.w == 0) {
        const int c0 = (20 * h) >> 7, c1 = (20 * h + 19) >> 7, j = k.lane & 15, sel = k.lane >> 4;
        const int cbk = (sel & 1) ? c1 : c0;
        const unsigned o0 = WS_F1 + (j * 16 + (sel < 2 ? 5 : 10) + cbk) * FLAG_STRIDE;
        const unsigned o1 = WS_F1 + ((2 * qb + (k.lane & 1)) * 16 + ((k.lane & 2) ? c1 : c0)) * FLAG_STRIDE;
        poll_flags2(k, o0, o1);
    }
    WAIT_BAR(0);
    STAMP(5);
    p2_phase(k, ac, h, qb, ct);
    STAMP(8);
    WAIT_BAR(0);
    if (k.tid == 0) st_flag(ws + WS_F2 + (qb * 32 + h) * FLAG_STRIDE, k.tag);
    P3Pre pre;
    p3_pre(k, rb64, ct, pre);
    if (k.w == 0) poll_flags(k, WS_F2 + (k.xcd * 32 + (k.lane & 31)) * FLAG_STRIDE);
    else if (k.w <= 3) w13_prefetch(k, f0, k.w - 1);
    BAR_L();
    if (k.c == 0 && k.tid == 0) st_flag(ws + WS_EPOCH, k.tag);
    STAMP(9);
    u32x2 hv[3];
    p3_phase(k, rb64, ct, f0, pre, hv);
    STAMP(11);
    if (k.w < 4) WAIT_VM(0);
    BAR_L();
    if (k.tid == 0) st_flag(ws + WS_F3 + (k.xcd * 32 + k.lb) * FLAG_STRIDE, k.tag);
    if (k.w == 0) {
        if (k.lane < 9) *(LAS unsigned*)(k.lds + CNT_LDS + k.lane * 4) = 0;
        poll_flags(k, WS_F3 + (rb4 * 32 + (k.lane & 31)) * FLAG_STRIDE);
    } else {
        WAIT_VM(0);
        p4_colsum(k, k.w - 1);
    }
    WAIT_BAR(0);
    STAMP(12);
    p4_phase(k, rb4, cb4, ct);
    STAMP(15);
    if (k.w == 0) poll_flags(k, WS_F4A + (((rb64 >> 2) * 32 + (k.lane & 31)) * 4 + (rb64 & 3)) * FLAG_STRIDE);
    BAR_L();
    STAMP(17);
    p5_phase(k, rb64, ct, hv, WS_F4B + ((rb4 * 32 + cb4) * 4 + (k.w >> 1)) * FLAG_STRIDE);
    STAMP(18);
    STAMP_FLUSH();
}

extern "C" void kernel_launch(void* const* d_in, const int* in_sizes, int n_in, void* d_out, int out_size, void* d_ws, size_t ws_size, hipStream_t stream) {
    (void)hipFuncSetAttribute((const void*)fused_block, hipFuncAttributeMaxDynamicSharedMemorySize, LDS_BYTES);
    fused_block<<<256, 512, LDS_BYTES, stream>>>((const uint16_t*)d_in[0], (const uint16_t*)d_in[1], (const uint16_t*)d_in[2], (const uint16_t*)d_in[4],
                                                 (const uint16_t*)d_in[5], (const uint16_t*)d_in[6], (char*)d_ws, (uint16_t*)d_out);
}
```

```cpp
#include <hip/hip_runtime.h>
#include <hip/hip_bf16.h>
#include <stdint.h>

#define DI __device__ __forceinline__
#define LAS __attribute__((address_space(3)))
#define GAS __attribute__((address_space(1)))
typedef short bf16x8 __attribute__((ext_vector_type(8)));
typedef short s16x4 __attribute__((ext_vector_type(4)));
typedef float f32x2 __attribute__((ext_vector_type(2)));
typedef float f32x4 __attribute__((ext_vector_type(4)));
typedef float f32x16 __attribute__((ext_vector_type(16)));
typedef unsigned u32x2 __attribute__((ext_vector_type(2)));
typedef unsigned u32x4 __attribute__((ext_vector_type(4)));
typedef unsigned long long u64;
typedef __bf16 bf16x2_t __attribute__((ext_vector_type(2)));
typedef short v4i16_t __attribute__((ext_vector_type(4)));
typedef LAS char* lds_ptr;
typedef __amdgpu_buffer_rsrc_t rsrc_t;

constexpr int S = 2048, D = 640, H = 32, DH = 20, F = 1728, NQKV = 3 * D;
constexpr float LN_EPS = 1e-5f;
constexpr float LOG2E = 1.4426950408889634f;
constexpr float QSCALE = 0.22360679774997896f * LOG2E;

constexpr unsigned WS_EPOCH = 0;
constexpr unsigned FLAG_STRIDE = 64, FLAG_ARR = 256 * FLAG_STRIDE;
constexpr unsigned WS_F1 = 4096, WS_FP = WS_F1 + FLAG_ARR, WS_F2 = WS_FP + FLAG_ARR, WS_F3 = WS_F2 + FLAG_ARR;
constexpr unsigned WS_F4A = WS_F3 + FLAG_ARR, WS_F4B = WS_F4A + 4 * FLAG_ARR;
constexpr unsigned WS_ST1 = WS_F4B + 4 * FLAG_ARR;
constexpr unsigned WS_CSQ = WS_ST1 + 32768;
static_assert(WS_CSQ + 1920 * 8 <= 262144, "control region");
constexpr unsigned WS_PST = 262144;
constexpr unsigned WS_Q = 524288;
constexpr unsigned IMG = S * D * 2;
constexpr unsigned QK_TILE = 32 * 1280, QK_IMG = 64 * QK_TILE;
constexpr unsigned WS_K = WS_Q + QK_IMG;
constexpr unsigned WS_V = WS_K + QK_IMG;
constexpr unsigned OH_TILE = 21504, HH_TILE = 56320;
constexpr unsigned WS_O = WS_V + IMG;
constexpr unsigned WS_H = WS_O + 128 * OH_TILE;
constexpr unsigned WS_HH = WS_H + 128 * OH_TILE;
constexpr unsigned WS_WO = WS_HH + 128 * HH_TILE;
constexpr unsigned WS_W13 = WS_WO + D * D * 2;
constexpr unsigned WS_W2 = WS_W13 + D * F * 4;
constexpr unsigned WS_END = WS_W2 + F * D * 2;

DI unsigned cvtpk(float lo, float hi) { f32x2 v = {lo, hi}; bf16x2_t b = __builtin_convertvector(v, bf16x2_t); return __builtin_bit_cast(unsigned, b); }
DI float bflo(unsigned u) { return __uint_as_float(u << 16); }
DI float bfhi(unsigned u) { return __uint_as_float(u & 0xffff0000u); }
DI f32x4 mfma16(bf16x8 a, bf16x8 b, f32x4 c) { return __builtin_amdgcn_mfma_f32_16x16x32_bf16(a, b, c, 0, 0, 0); }
DI void mfma16_acc(f32x4& c, bf16x8 a, bf16x8 b) { asm volatile("v_mfma_f32_16x16x32_bf16 %0, %1, %2, %0" : "+v"(c) : "v"(a), "v"(b)); }
DI void mfma_fence() { __builtin_amdgcn_sched_barrier(0); asm volatile("s_nop 7\n\ts_nop 1" ::: "memory"); __builtin_amdgcn_sched_barrier(0); }
DI f32x16 mfma32(bf16x8 a, bf16x8 b, f32x16 c) { return __builtin_amdgcn_mfma_f32_32x32x16_bf16(a, b, c, 0, 0, 0); }
DI int rfl(int x) { return __builtin_amdgcn_readfirstlane(x); }
template <bool SC1>
DI void glds16(unsigned voff, const void* sbase, unsigned ldsdst) {
    unsigned keep;
    if (SC1) asm volatile("s_mov_b32 %0, m0\n\ts_mov_b32 m0, %3\n\ts_nop 0\n\tglobal_load_lds_dwordx4 %1, %2 sc1\n\ts_mov_b32 m0, %0" : "=&s"(keep) : "v"(voff), "s"(sbase), "s"(ldsdst) : "memory");
    else asm volatile("s_mov_b32 %0, m0\n\ts_mov_b32 m0, %3\n\ts_nop 0\n\tglobal_load_lds_dwordx4 %1, %2\n\ts_mov_b32 m0, %0" : "=&s"(keep) : "v"(voff), "s"(sbase), "s"(ldsdst) : "memory");
}
DI void glds4(const void* gptr, unsigned ldsdst) {
    unsigned keep;
    asm volatile("s_mov_b32 %0, m0\n\ts_mov_b32 m0, %2\n\ts_nop 0\n\tglobal_load_lds_dword %1, off\n\ts_mov_b32 m0, %0" : "=&s"(keep) : "v"(gptr), "s"(ldsdst) : "memory");
}
#define WAIT_VM(n) asm volatile("s_waitcnt vmcnt(" #n ")" ::: "memory")
#define WAIT_BAR(n) asm volatile("s_waitcnt vmcnt(" #n ") lgkmcnt(0)\n\ts_barrier" ::: "memory")
#define BAR_L() asm volatile("s_waitcnt lgkmcnt(0)\n\ts_barrier" ::: "memory")
#define SBAR() __builtin_amdgcn_sched_barrier(0)
#define PIN(x) asm volatile("" : "+v"(x))
#define PRIO(n) __builtin_amdgcn_s_setprio(n)
#define EX(v) __builtin_amdgcn_exp2f(v)
DI u32x4 lds_r128(lds_ptr p) { return *(const LAS u32x4*)p; }
DI s16x4 lds_tr(lds_ptr p) { return __builtin_bit_cast(s16x4, __builtin_amdgcn_ds_read_tr16_b64_v4i16((LAS v4i16_t*)p)); }
DI bf16x8 as_frag(u32x4 v) { return __builtin_bit_cast(bf16x8, v); }
DI bf16x8 cat_frag(s16x4 a, s16x4 b) { return (bf16x8){a[0], a[1], a[2], a[3], b[0], b[1], b[2], b[3]}; }
DI float swap32_max(float m) { auto r = __builtin_amdgcn_permlane32_swap(__float_as_uint(m), __float_as_uint(m), false, false); return fmaxf(__uint_as_float(r[0]), __uint_as_float(r[1])); }
DI float sum_x16(float v) { auto r = __builtin_amdgcn_permlane16_swap(__float_as_uint(v), __float_as_uint(v), false, false); return __uint_as_float(r[0]) + __uint_as_float(r[1]); }
DI float sum_x32(float v) { auto r = __builtin_amdgcn_permlane32_swap(__float_as_uint(v), __float_as_uint(v), false, false); return __uint_as_float(r[0]) + __uint_as_float(r[1]); }
DI u32x4 ld128(rsrc_t r, unsigned off) { return __builtin_amdgcn_raw_buffer_load_b128(r, off, 0, 16); }
DI u32x2 ld64(rsrc_t r, unsigned off) { return __builtin_amdgcn_raw_buffer_load_b64(r, off, 0, 16); }
DI unsigned ld32(rsrc_t r, unsigned off) { return __builtin_amdgcn_raw_buffer_load_b32(r, off, 0, 16); }
DI void st128(rsrc_t r, unsigned off, u32x4 v) { __builtin_amdgcn_raw_buffer_store_b128(v, r, off, 0, 16); }
DI void st64(rsrc_t r, unsigned off, u32x2 v) { __builtin_amdgcn_raw_buffer_store_b64(v, r, off, 0, 16); }

DI void ald128(u32x4& v, u32x4 rsw, unsigned voff, unsigned soff) { asm volatile("buffer_load_dwordx4 %0, %1, %2, %3 offen sc1" : "=&v"(v) : "v"(voff), "s"(rsw), "s"(soff) : "memory"); }
template <int N> DI void ald_wait(u32x4& v) { asm volatile("s_waitcnt vmcnt(%1)" : "+v"(v) : "i"(N) : "memory"); }
DI void ald64(u32x2& v, u32x4 rsw, unsigned voff) { asm volatile("buffer_load_dwordx2 %0, %1, %2, 0 offen sc1" : "=&v"(v) : "v"(voff), "s"(rsw) : "memory"); }
DI void touch_lines(unsigned& dummy, u32x4 rsw, unsigned voff) { asm volatile("buffer_load_dword %0, %1, %2, 0 offen sc1" : "=&v"(dummy) : "v"(voff), "s"(rsw) : "memory"); }
struct Ctx {
    const uint16_t *x, *wqkv, *wo, *w1, *w3, *w2;
    char* ws; uint16_t* out;
    rsrc_t rs; u32x4 rsw; u64 tag; lds_ptr lds; unsigned lds0;
    int c, xcd, lb, tid, lane, w;
    mutable unsigned sc, sr;
};

#define STAMP(i)
#define STAMP_FLUSH()

DI u64 ld_flag(const char* p) { return __hip_atomic_load((const GAS u64*)p, __ATOMIC_RELAXED, __HIP_MEMORY_SCOPE_AGENT); }
DI void st_flag(char* p, u64 v) { __hip_atomic_store((GAS u64*)p, v, __ATOMIC_RELAXED, __HIP_MEMORY_SCOPE_AGENT); }
constexpr unsigned SPIN_CAP = 1u << 18;
DI void poll_flags(const Ctx& k, unsigned off) {
    u64 a = ld_flag(k.ws + off);
    __builtin_amdgcn_s_sleep(8);
    for (unsigned spins = 0; spins < SPIN_CAP; ++spins) {
        const u64 b = ld_flag(k.ws + off);
        if (__all(a == k.tag)) break;
        a = ld_flag(k.ws + off);
        if (__all(b == k.tag)) break;
    }
}
DI void poll_flags2(const Ctx& k, unsigned off0, unsigned off1) {
    u64 a0 = ld_flag(k.ws + off0), a1 = ld_flag(k.ws + off1);
    __builtin_amdgcn_s_sleep(8);
    for (unsigned spins = 0; spins < SPIN_CAP; ++spins) {
        const u64 b0 = ld_flag(k.ws + off0), b1 = ld_flag(k.ws + off1);
        if (__all(a0 == k.tag && a1 == k.tag)) break;
        a0 = ld_flag(k.ws + off0); a1 = ld_flag(k.ws + off1);
        if (__all(b0 == k.tag && b1 == k.tag)) break;
    }
}
DI void poll_all(const Ctx& k, unsigned arr) {
    for (unsigned spins = 0; spins < SPIN_CAP; ++spins) {
        const unsigned o = arr + k.lane * FLAG_STRIDE;
        const u64 a = ld_flag(k.ws + o), b = ld_flag(k.ws + o + 64 * FLAG_STRIDE), c = ld_flag(k.ws + o + 128 * FLAG_STRIDE), d = ld_flag(k.ws + o + 192 * FLAG_STRIDE);
        if (__all(a == k.tag && b == k.tag && c == k.tag && d == k.tag)) break;
        __builtin_amdgcn_s_sleep(1);
    }
}

constexpr int SU_LDS = 131072;
DI void startup_load(const Ctx& k) { glds4(k.ws + WS_EPOCH + (k.lane & 1) * 4, k.lds0 + SU_LDS + k.w * 256); }
DI void startup_tag(Ctx& k) {
    const u32x2 e = *(const LAS u32x2*)(k.lds + SU_LDS + k.w * 256);
    k.tag = (((u64)(unsigned)rfl(e.y) << 32) | (unsigned)rfl(e.x)) + 1;
}
DI void ln1_stats(const Ctx& k, int j) {
    u32x4 v[2][10];
#pragma unroll
    for (int p = 0; p < 2; ++p)
#pragma unroll
        for (int i = 0; i < 10; ++i) v[p][i] = *(const u32x4*)(k.x + (size_t)(128 * j + 16 * k.w + 8 * p + (k.lane >> 3)) * D + ((k.lane & 7) + 8 * i) * 8);
#pragma unroll
    for (int p = 0; p < 2; ++p) {
        float s = 0.f, q = 0.f;
#pragma unroll
        for (int i = 0; i < 10; ++i)
#pragma unroll
            for (int e = 0; e < 4; ++e) { const float a = bflo(v[p][i][e]), b = bfhi(v[p][i][e]); s += a + b; q = fmaf(a, a, fmaf(b, b, q)); }
#pragma unroll
        for (int o = 4; o >= 1; o >>= 1) { s += __shfl_xor(s, o); q += __shfl_xor(q, o); }
        const float mean = s * (1.0f / D), var = fmaxf(q * (1.0f / D) - mean * mean, 0.f);
        const float val = (k.lane & 1) ? 1.0f / sqrtf(var + LN_EPS) : mean;
        const int row = 128 * j + 16 * k.w + 8 * p + (k.lane >> 3);
        if ((k.lane & 7) < 2) st64(k.rs, WS_ST1 + row * 16 + (k.lane & 1) * 8, (u32x2){__float_as_uint(val), (unsigned)k.tag});
    }
}
DI void colsum_qkv(const Ctx& k, int cb) {
    float a[8] = {0, 0, 0, 0, 0, 0, 0, 0};
    u32x4 v[20];
#pragma unroll
    for (int i = 0; i < 20; ++i) v[i] = *(const u32x4*)(k.wqkv + (size_t)(k.w * 80 + i * 4 + (k.lane >> 4)) * NQKV + cb * 128 + (k.lane & 15) * 8);
#pragma unroll
    for (int i = 0; i < 20; ++i)
#pragma unroll
        for (int j = 0; j < 4; ++j) { a[2 * j] += bflo(v[i][j]); a[2 * j + 1] += bfhi(v[i][j]); }
    LAS float* red = (LAS float*)k.lds;
#pragma unroll
    for (int j = 0; j < 8; ++j) { a[j] += __shfl_xor(a[j], 16); a[j] += __shfl_xor(a[j], 32); }
    if (k.lane < 16) {
#pragma unroll
        for (int j = 0; j < 8; ++j) red[k.w * 128 + k.lane * 8 + j] = a[j];
    }
    BAR_L();
    if (k.tid < 128) {
        float t = 0.f;
#pragma unroll
        for (int i = 0; i < 8; ++i) t += red[i * 128 + k.tid];
        st64(k.rs, WS_CSQ + (cb * 128 + k.tid) * 8, (u32x2){__float_as_uint(t), (unsigned)k.tag});
    }
}

DI int p4_f0(int cb) { return cb < 24 ? cb * 7 : 168 + (cb - 24) * 6; }
DI int hh_f8(int sigma, int gp) {
    const int kh = sigma >= 27, i = sigma - 27 * kh;
    if (i < 16) return p4_f0(16 * kh + i) + gp;
    const int l = 4 * (11 * kh + i - 16) + gp, cb = l < 72 ? l / 3 : 24 + (l - 72) / 2;
    return p4_f0(cb) + 4 + (l < 72 ? l % 3 : (l - 72) % 2);
}
constexpr int PT_LDS = 131072;
DI int pt_off(int i) { return PT_LDS + (i < 3 ? i * 8192 : 24576 + (i - 3) * 4096); }
DI void pretile_load(const Ctx& k) {
    const int wv = k.w - 1;
#pragma unroll
    for (int i = 0; i < 5; ++i) {
        const int u = k.c + 256 * i;
        if (u < 540) {
            const int s = u % 20, fb = u / 20;
            const uint16_t* src = ((k.lane & 1) ? k.w3 : k.w1) + fb * 64 + (k.lane >> 1) * 2 + (size_t)32 * s * F;
            for (int r = wv; r < 32; r += 7) glds4(src + (size_t)r * F, k.lds0 + pt_off(i) + r * 256);
        } else {
            const int pc = (i & 1) ? wv - 3 : wv;
            if (pc >= 0 && pc < 4) {
                const bool is2 = u < 1080;
                const int uu = is2 ? u - 540 : u - 1080, s = uu / 10, nb = uu % 10;
                const int r = (is2 ? 8 * hh_f8(s, pc) : 32 * s + 8 * pc) + (k.lane >> 3);
                glds16<false>(r * (D * 2) + nb * 128 + (k.lane & 7) * 16, is2 ? k.w2 : k.wo, k.lds0 + pt_off(i) + pc * 1024);
            }
        }
    }
}
struct PreTile { u32x4 v[5]; };
DI void pretile_read(const Ctx& k, PreTile& p) {
    const int g = k.lane >> 4, q = (k.lane >> 2) & 3, pp = k.lane & 3;
#pragma unroll
    for (int i = 0; i < 5; ++i) {
        const int u = k.c + 256 * i;
        const bool wide = u < 540;
        const int rs = wide ? 256 : 128, f = wide ? k.w : (k.w & 3);
        const lds_ptr a = k.lds + pt_off(i) + (8 * g + q) * rs + f * 32 + pp * 8;
        p.v[i] = __builtin_bit_cast(u32x4, cat_frag(lds_tr(a), lds_tr(a + 4 * rs)));
    }
}
DI void pretile_store(const Ctx& k, const PreTile& p, int i) {
    const int u = k.c + 256 * i;
    unsigned dst;
    if (u < 540) dst = WS_W13 + (((u / 20) * 8 + k.w) * 20 + u % 20) * 1024;
    else {
        const bool is2 = u < 1080;
        const int uu = is2 ? u - 540 : u - 1080, s = uu / 10, cfg = (uu % 10) * 4 + (k.w & 3), ct = cfg / 5, cf = cfg % 5;
        dst = is2 ? WS_W2 + ((ct * 54 + s) * 5 + cf) * 1024 : WS_WO + ((ct * 20 + s) * 5 + cf) * 1024;
    }
    st128(k.rs, dst + k.lane * 16, p.v[i]);
}

constexpr int P1_SLOT = 32768;
struct P1Frag { bf16x8 a[4], b[2]; };
template <bool ISV> DI void p1_mma(f32x4 (&acc)[4][2], const P1Frag& f, int m, int n) { if (ISV) mfma16_acc(acc[m][n], f.a[m], f.b[n]); else mfma16_acc(acc[m][n], f.b[n], f.a[m]); }
template <bool ISV>
DI void p1_body(Ctx& k, int rb, int cb) {
    const int lane = k.lane, w = k.w, th = w >> 2, cg = w & 3, t = lane & 15, g = lane >> 4;
    unsigned voffA[2], voffW[2];
#pragma unroll
    for (int i = 0; i < 2; ++i) {
        const int ra = 8 * (2 * w + i) + (lane >> 3), ca = (lane & 7) ^ ((ra >> 1) & 7);
        voffA[i] = ra * (D * 2) + ca * 16;
        const int rw = 4 * (2 * w + i) + (lane >> 4), fw = (rw & 3) | (((rw >> 3) & 1) << 2), cw = (lane & 15) ^ (fw << 1);
        voffW[i] = rw * (NQKV * 2) + cw * 16;
    }
    const char* gA = (const char*)k.x + (size_t)rb * 128 * D * 2;
    const char* gW = (const char*)k.wqkv + cb * 256;
    auto stage = [&](int kt) {
        const unsigned sl = k.lds0 + (kt & 3) * P1_SLOT + w * 2048;
#pragma unroll
        for (int i = 0; i < 2; ++i) glds16<false>(voffA[i], gA + kt * 128, sl + i * 1024);
#pragma unroll
        for (int i = 0; i < 2; ++i) glds16<false>(voffW[i], gW + (size_t)kt * 64 * NQKV * 2, sl + 16384 + i * 1024);
    };
    stage(0); stage(1); stage(2); stage(3);
    int aoff[2], woff[2];
    aoff[0] = (th * 64 + t) * 128 + ((g ^ (t >> 1)) << 4); aoff[1] = aoff[0] ^ 64;
    const int q4 = (lane >> 2) & 3, p = lane & 3, fw = q4 | ((g & 1) << 2);
#pragma unroll
    for (int n = 0; n < 2; ++n) woff[n] = 16384 + (8 * g + q4) * 256 + (((2 * cg + n) ^ fw) << 5) + p * 8;
    f32x4 acc[4][2];
#pragma unroll
    for (int m = 0; m < 4; ++m) { acc[m][0] = (f32x4){0, 0, 0, 0}; acc[m][1] = (f32x4){0, 0, 0, 0}; PIN(acc[m][0]); PIN(acc[m][1]); }
    mfma_fence();
    P1Frag f0, f1;
#define P1_RA(F_, sl, ks, m) F_.a[m] = as_frag(lds_r128(sl + aoff[ks] + (m) * 2048))
#define P1_RB(F_, sl, ks, n) F_.b[n] = cat_frag(lds_tr(sl + woff[n] + (ks) * 8192), lds_tr(sl + woff[n] + (ks) * 8192 + 1024))
#define P1_STEP(RD, FN, sl, ks, FC) do { \
        p1_mma<ISV>(acc, FC, 0, 0); SBAR(); if (RD) P1_RB(FN, sl, ks, 0); PIN(FC.b[1]); SBAR(); \
        p1_mma<ISV>(acc, FC, 0, 1); SBAR(); if (RD) P1_RA(FN, sl, ks, 0); PIN(FC.a[1]); SBAR(); \
        p1_mma<ISV>(acc, FC, 1, 0); SBAR(); if (RD) P1_RA(FN, sl, ks, 1); SBAR(); \
        p1_mma<ISV>(acc, FC, 1, 1); SBAR(); if (RD) P1_RB(FN, sl, ks, 1); PIN(FC.a[2]); SBAR(); \
        p1_mma<ISV>(acc, FC, 2, 0); SBAR(); if (RD) P1_RA(FN, sl, ks, 2); SBAR(); \
        p1_mma<ISV>(acc, FC, 2, 1); SBAR(); if (RD) P1_RA(FN, sl, ks, 3); PIN(FC.a[3]); SBAR(); \
        p1_mma<ISV>(acc, FC, 3, 0); SBAR(); \
        p1_mma<ISV>(acc, FC, 3, 1); SBAR(); } while (0)
    WAIT_BAR(12);
    STAMP(1);
    startup_tag(k);
    { const lds_ptr sl = k.lds; P1_RB(f0, sl, 0, 0); P1_RB(f0, sl, 0, 1); P1_RA(f0, sl, 0, 0); P1_RA(f0, sl, 0, 1); P1_RA(f0, sl, 0, 2); P1_RA(f0, sl, 0, 3); SBAR(); }
    u32x2 gr = {0, 0};
    PRIO(1);
    const unsigned groff = k.tid < 256 ? WS_ST1 + (rb * 128) * 16 + k.tid * 8 : WS_CSQ + (cb * 128 + (k.tid & 127)) * 8;
#pragma unroll
    for (int kt = 0; kt < 10; ++kt) {
        const lds_ptr sl = k.lds + (kt & 3) * P1_SLOT, sn = k.lds + ((kt + 1) & 3) * P1_SLOT;
        P1_STEP(true, f1, sl, 1, f0);
        if (kt < 9) {
            if (kt < 7) WAIT_BAR(8); else if (kt == 7) WAIT_BAR(4); else WAIT_BAR(0);
            if (kt == 6) gr = ld64(k.rs, groff);
            if (kt + 4 < 10) stage(kt + 4);
            SBAR();
        }
        P1_STEP(kt < 9, f0, sn, 0, f1);
    }
#undef P1_RA
#undef P1_RB
#undef P1_STEP
    PRIO(0);
    mfma_fence();
    STAMP(2);
    for (unsigned spins = 0; spins < SPIN_CAP; ++spins) {
        if (__all(gr.y == (unsigned)k.tag)) break;
        gr = ld64(k.rs, groff);
    }
    LAS float* sst = (LAS float*)k.lds;
    if (k.tid < 384) sst[k.tid] = __uint_as_float(gr.x);
    BAR_L();
    const LAS f32x2* st = (const LAS f32x2*)sst + th * 64;
    const LAS float* cs = sst + 256 + cg * 32;
    if (!ISV) {
        const float osc = cb < 5 ? QSCALE : 1.0f;
        const unsigned img = cb < 5 ? WS_Q : WS_K;
        const int cbs = cb % 5;
        unsigned dst[2]; f32x4 c4[2];
#pragma unroll
        for (int n = 0; n < 2; ++n) {
            const int cgi = cbs * 32 + cg * 8 + n * 4 + g, hd = cgi / 5, gi = cgi - 5 * hd;
            const int blk = gi == 4 ? 4 : ((gi & 1) << 1) | (gi >> 1);
            dst[n] = img + (rb * 4 + th * 2) * QK_TILE + hd * 1280 + blk * 256 + t * 8;
            c4[n] = *(const LAS f32x4*)(cs + n * 16 + 4 * g);
        }
#pragma unroll
        for (int m = 0; m < 4; ++m) {
            const f32x2 sm = st[m * 16 + t];
            const float r = sm.y * osc;
#pragma unroll
            for (int n = 0; n < 2; ++n) {
                const f32x4 y = (acc[m][n] - c4[n] * sm.x) * r;
                const unsigned o = dst[n] + (m >> 1) * QK_TILE + (m & 1) * 128;
                st64(k.rs, o, (u32x2){cvtpk(y[0], y[1]), cvtpk(y[2], y[3])});
            }
        }
    } else {
        const int pos = ((g & 1) << 1) | (g >> 1);
        const unsigned dst = WS_V + (rb * 8 + th * 4) * 20480 + ((cb - 10) * 128 + cg * 32 + t) * 32 + pos * 8;
        const float c1[2] = {cs[t], cs[16 + t]};
#pragma unroll
        for (int m = 0; m < 4; ++m) {
            f32x2 sm[4];
#pragma unroll
            for (int i = 0; i < 4; ++i) sm[i] = st[m * 16 + 4 * g + i];
#pragma unroll
            for (int n = 0; n < 2; ++n) {
                float y[4];
#pragma unroll
                for (int i = 0; i < 4; ++i) y[i] = (acc[m][n][i] - c1[n] * sm[i].x) * sm[i].y;
                st64(k.rs, dst + m * 20480 + n * 512, (u32x2){cvtpk(y[0], y[1]), cvtpk(y[2], y[3])});
            }
        }
    }
}

constexpr int AT_KT = 8192, AT_KB = 10240, AT_HALF = AT_KB + 10240, AT_BUF = 2 * AT_HALF;
constexpr int AT_ONES = AT_BUF, AT_ONES_BYTES = 10752, AT_KC = AT_ONES + AT_ONES_BYTES, AT_KC_BYTES = 2048, AT_FLAG = AT_KC + AT_KC_BYTES, WO_LDS = 61440, AT_RED = 0;
DI int at_slot(int i) { return i == 0 ? 0 : (i == 1 ? 61440 : 61440 + AT_BUF); }
struct AttnCtx {
    unsigned voff[5]; const char* gsrc; unsigned ldst, sstride;
    u32x4 qf0[2]; u32x2 qf1[2];
    int kofs, kt, vlane; bool vdata, hi;
};
DI void attn_stage(const AttnCtx& c, int sidx, int slot) {
#pragma unroll
    for (int i = 0; i < 5; ++i) glds16<true>(c.voff[i], c.gsrc + sidx * c.sstride, c.ldst + at_slot(slot) + i * 1024);
}
DI void attn_setup(const Ctx& k, AttnCtx& c, int h) {
    const int lane = k.lane, w = k.w, kh = w >> 2, r = lane & 31, hi = lane >> 5;
    const bool stV = (w >> 1) & 1;
#pragma unroll
    for (int i = 0; i < 5; ++i) {
        const int pc = 5 * (w & 1) + i;
        if (!stV) c.voff[i] = pc < 8 ? pc * QK_TILE + lane * 16 : ((pc - 8) * 4 + (lane >> 4)) * QK_TILE + 1024 + (lane & 15) * 16;
        else { const int ci = 64 * pc + lane, st = ci / 40, wi = ci % 40; c.voff[i] = st * 20480 + ((wi % 20) * 2 + wi / 20) * 16; }
    }
    c.gsrc = k.ws + (stV ? WS_V + h * 640 + kh * 64 * 20480 : WS_K + h * 1280 + kh * 32 * QK_TILE);
    c.sstride = stV ? 16 * 20480 : 8 * QK_TILE;
    c.ldst = k.lds0 + kh * AT_HALF + (stV ? AT_KB : 0) + (w & 1) * 5120;
    c.kofs = kh * AT_HALF + hi * 256 + r * 8;
    c.hi = hi;
    c.kt = hi ? AT_KC + r * 8 : kh * AT_HALF + AT_KT + r * 8;
    c.vdata = r < 20;
    c.vlane = c.vdata ? kh * AT_HALF + AT_KB + hi * 320 + r * 16 : AT_ONES + (r == 20 ? 128 : r < 28 ? 144 : hi ? 16 : 208);
    for (int i = k.tid; i < AT_ONES_BYTES / 4; i += 512) ((LAS unsigned*)(k.lds + AT_ONES))[i] = (i & 4) ? 0u : 0x3F803F80u;
    ((LAS unsigned*)(k.lds + AT_KC))[k.tid] = (k.tid & 1) ? 0u : 0x00003F80u;
    if (k.tid == 0) *(LAS unsigned*)(k.lds + AT_FLAG) = 0;
}
DI void attn_first_loads(const Ctx& k, AttnCtx& c, int h, int qb) {
    const int qg = k.w & 3, r = k.lane & 31, hi = k.lane >> 5;
    attn_stage(c, 0, 0);
#pragma unroll
    for (int i = 0; i < 2; ++i) {
        const unsigned qp = WS_Q + (qb * 8 + qg * 2 + i) * QK_TILE + h * 1280 + r * 8;
        const u32x2 a = ld64(k.rs, qp + hi * 256), b = ld64(k.rs, qp + hi * 256 + 512), e = ld64(k.rs, qp + 1024);
        c.qf0[i] = (u32x4){a.x, a.y, b.x, b.y};
        c.qf1[i] = hi ? (u32x2){0, 0} : e;
    }
    attn_stage(c, 1, 1);
}
DI bf16x8 kfrag0(lds_ptr p) { const u32x2 a = *(const LAS u32x2*)p, b = *(const LAS u32x2*)(p + 512); return as_frag((u32x4){a.x, a.y, b.x, b.y}); }
DI s16x4 kfrag1(lds_ptr p) { return __builtin_bit_cast(s16x4, *(const LAS u32x2*)p); }
DI s16x4 as_frag4(u32x2 v) { return __builtin_bit_cast(s16x4, v); }
DI f32x16 mfma32x8(s16x4 a, s16x4 b, f32x16 c) { return __builtin_amdgcn_mfma_f32_32x32x8bf16_1k(a, b, c, 0, 0, 0); }
DI void wo_prefetch(const Ctx& k, int ct) {
#pragma unroll 5
    for (int j = 0; j < 15; ++j) { int pc = k.w - 1 + 7 * j; if (pc >= 100) pc -= 7; glds16<true>(k.lane * 16, k.ws + WS_WO + ct * 102400 + pc * 1024, k.lds0 + WO_LDS + pc * 1024); }
}
DI void attn_fast(const Ctx& k, AttnCtx& c, const PreTile& pt, f32x16& oA, f32x16& oB, int ct3, bool& unsafe) {
    const lds_ptr lds = k.lds;
    const int hi = k.lane >> 5, kh = k.w >> 2;
    unsafe = false;
    {
        const bf16x8 k0 = kfrag0(lds + c.kofs - kh * AT_HALF); const s16x4 k1 = kfrag1(lds + (hi ? c.kt : c.kt - kh * AT_HALF));
#pragma unroll
        for (int i = 0; i < 2; ++i) {
            f32x16 s = mfma32(k0, as_frag(c.qf0[i]), (f32x16){});
            s = mfma32x8(k1, as_frag4(c.qf1[i]), s);
            float m = s[0];
#pragma unroll
            for (int j = 1; j < 16; ++j) m = fmaxf(m, s[j]);
            m = swap32_max(m);
            unsafe |= !(fabsf(m) <= 4096.f);
            if (hi) c.qf1[i].x = cvtpk(-m, 0.f) & 0xffffu;
        }
    }
    const bf16x8 qA0 = as_frag(c.qf0[0]), qB0 = as_frag(c.qf0[1]); const s16x4 qA1 = as_frag4(c.qf1[0]), qB1 = as_frag4(c.qf1[1]);
    f32x16 sA, sB, nA, nB;
    oA = (f32x16){}; oB = (f32x16){};
    bf16x8 kf0 = kfrag0(lds + c.kofs); s16x4 kf1 = kfrag1(lds + c.kt);
    sA = mfma32(kf0, qA0, (f32x16){}); sB = mfma32(kf0, qB0, (f32x16){});
    sA = mfma32x8(kf1, qA1, sA); sB = mfma32x8(kf1, qB1, sB);
    kf0 = kfrag0(lds + c.kofs + 1024); kf1 = kfrag1(lds + c.kt + 256);
    u32x4 pA0, pA1, pB0, pB1 = {0, 0, 0, 0};
    bf16x8 vf0, vf1 = {0, 0, 0, 0, 0, 0, 0, 0};
    u32x2 fa = {0, 0}, fb = fa, fc = fa, fd = fa;
#define GAP(MF, X, i, P, j) do { MF; SBAR(); X[i] = EX(X[i]); X[i + 1] = EX(X[i + 1]); X[i + 2] = EX(X[i + 2]); X[i + 3] = EX(X[i + 3]); \
        P[j] = cvtpk(X[i], X[i + 1]); P[j + 1] = cvtpk(X[i + 2], X[i + 3]); PIN(P); SBAR(); } while (0)
#define STEP(SA, SB, NA, NB, VP, KP, KT, HK) do { \
        GAP(NA = mfma32(kf0, qA0, (f32x16){}), SA, 0, pA0, 0); vf0 = as_frag(lds_r128(VP)); HK(0); SBAR(); \
        GAP(oB = mfma32(vf1, as_frag(pB1), oB), SA, 4, pA0, 2); vf1 = as_frag(lds_r128((VP) + 640)); HK(1); SBAR(); \
        GAP(NB = mfma32(kf0, qB0, (f32x16){}), SA, 8, pA1, 0); HK(2); SBAR(); \
        GAP(NA = mfma32x8(kf1, qA1, NA), SA, 12, pA1, 2); HK(3); SBAR(); \
        GAP(NB = mfma32x8(kf1, qB1, NB), SB, 0, pB0, 0); kf0 = kfrag0(KP); kf1 = kfrag1(KT); HK(4); SBAR(); \
        GAP(oA = mfma32(vf0, as_frag(pA0), oA), SB, 4, pB0, 2); HK(5); SBAR(); \
        GAP(oA = mfma32(vf1, as_frag(pA1), oA), SB, 8, pB1, 0); HK(6); SBAR(); \
        GAP(oB = mfma32(vf0, as_frag(pB0), oB), SB, 12, pB1, 2); HK(7); SBAR(); \
    } while (0)
#define HK_NONE(j)
#define STAGE(sidx, slot, nslot, PT, NST, HA, HB) do { \
        const int sb = at_slot(slot), sn = at_slot(nslot); \
        const lds_ptr vb = lds + (c.vdata ? c.vlane + sb : c.vlane); \
        lds_ptr k0 = lds + c.kofs + sb, k0n = lds + c.kofs + sn, k1 = lds + (hi ? c.kt : c.kt + sb), k1n = lds + (hi ? c.kt : c.kt + sn); \
        PIN(k0); PIN(k0n); PIN(k1); PIN(k1n); \
        STEP(sA, sB, nA, nB, vb, k0 + 2 * 1024, k1 + 2 * 256, HK_NONE); PT(0); \
        STEP(nA, nB, sA, sB, vb + 1280, k0 + 3 * 1024, k1 + 3 * 256, HK_NONE); PT(1); \
        STEP(sA, sB, nA, nB, vb + 2 * 1280, k0 + 4 * 1024, k1 + 4 * 256, HK_NONE); PT(2); \
        STEP(nA, nB, sA, sB, vb + 3 * 1280, k0 + 5 * 1024, k1 + 5 * 256, HK_NONE); PT(3); \
        STEP(sA, sB, nA, nB, vb + 4 * 1280, k0 + 6 * 1024, k1 + 6 * 256, HK_NONE); PT(4); \
        STEP(nA, nB, sA, sB, vb + 5 * 1280, k0 + 7 * 1024, k1 + 7 * 256, HK_NONE); \
        if ((sidx) == 3 && k.w == 0) {     \
            const unsigned tl = (unsigned)k.tag, th = (unsigned)(k.tag >> 32); \
            if (!__all(fa.x == tl && fa.y == th && fb.x == tl && fb.y == th && fc.x == tl && fc.y == th && fd.x == tl && fd.y == th)) poll_all(k, WS_FP); \
        } \
        WAIT_BAR(NST); \
        if ((sidx) < 2) attn_stage(c, (sidx) + 2, (nslot) == 2 ? 0 : (nslot) + 1); \
        if ((sidx) == 1 && k.tid == 0) st_flag(k.ws + WS_FP + k.c * FLAG_STRIDE, k.tag);     \
        if ((sidx) == 2 && k.w == 0) {     \
            const unsigned o = WS_FP + k.lane * FLAG_STRIDE; \
            fa = ld64(k.rs, o); fb = ld64(k.rs, o + 64 * FLAG_STRIDE); fc = ld64(k.rs, o + 128 * FLAG_STRIDE); fd = ld64(k.rs, o + 192 * FLAG_STRIDE); \
        } \
        SBAR(); \
        STEP(sA, sB, nA, nB, vb + 6 * 1280, k0n, k1n, HA); \
        STEP(nA, nB, sA, sB, vb + 7 * 1280, k0n + 1024, k1n + 256, HB); \
    } while (0)
#define PT_STORE(j) do { pretile_store(k, pt, j); SBAR(); } while (0)
#define PT_NONE(j)
    const char* wo_src = k.ws + WS_WO + ct3 * 102400;
#define WO_PIECE(n) do { int pc_ = k.w + 8 * (n); if (pc_ >= 100) pc_ -= 8; glds16<true>(k.lane * 16, wo_src + pc_ * 1024, k.lds0 + WO_LDS + pc_ * 1024); } while (0)
#define HK_WO_A(j) WO_PIECE(j)
#define HK_WO_B(j) do { if ((j) < 5) WO_PIECE(8 + (j)); } while (0)
    PRIO(1);
    STAGE(0, 0, 1, PT_STORE, 5, HK_NONE, HK_NONE);
    STAGE(1, 1, 2, PT_NONE, 0, HK_NONE, HK_NONE);
    STAGE(2, 2, 0, PT_NONE, 0, HK_NONE, HK_NONE);
    STAGE(3, 0, 1, PT_NONE, 0, HK_WO_A, HK_WO_B);
#undef STAGE
#undef STEP
#undef PT_STORE
#undef PT_NONE
#undef WO_PIECE
#undef HK_WO_A
#undef HK_WO_B
#undef HK_NONE
    PRIO(0);
    oB = mfma32(vf1, as_frag(pB1), oB);
#undef GAP
}
DI void attn_safe(const Ctx& k, AttnCtx& c, f32x16 (&o)[2], float (&mrun)[2]) {
    const lds_ptr lds = k.lds;
    attn_stage(c, 0, 0);
    if (k.lane >> 5) { c.qf1[0].x = 0; c.qf1[1].x = 0; }
    o[0] = (f32x16){}; o[1] = (f32x16){};
    mrun[0] = -INFINITY; mrun[1] = -INFINITY;
    for (int sidx = 0; sidx < 4; ++sidx) {
        const int sb = at_slot(sidx & 1);
        WAIT_BAR(0);
        if (sidx < 3) attn_stage(c, sidx + 1, (sidx + 1) & 1);
        const lds_ptr vb = lds + (c.vdata ? c.vlane + sb : c.vlane);
        for (int kt = 0; kt < 8; ++kt) {
            const bf16x8 k0 = kfrag0(lds + c.kofs + sb + kt * 1024); const s16x4 k1 = kfrag1(lds + (c.hi ? c.kt : c.kt + sb) + kt * 256);
            const bf16x8 v0 = as_frag(lds_r128(vb + kt * 1280)), v1 = as_frag(lds_r128(vb + kt * 1280 + 640));
#pragma unroll
            for (int i = 0; i < 2; ++i) {
                f32x16 s = mfma32(k0, as_frag(c.qf0[i]), (f32x16){});
                s = mfma32x8(k1, as_frag4(c.qf1[i]), s);
                float m = s[0];
#pragma unroll
                for (int j = 1; j < 16; ++j) m = fmaxf(m, s[j]);
                m = fmaxf(swap32_max(m), mrun[i]);
                const float alpha = EX(mrun[i] - m);
                mrun[i] = m;
                unsigned pk[8];
#pragma unroll
                for (int j = 0; j < 16; ++j) o[i][j] *= alpha;
#pragma unroll
                for (int j = 0; j < 8; ++j) pk[j] = cvtpk(EX(s[2 * j] - m), EX(s[2 * j + 1] - m));
                o[i] = mfma32(v0, as_frag((u32x4){pk[0], pk[1], pk[2], pk[3]}), o[i]);
                o[i] = mfma32(v1, as_frag((u32x4){pk[4], pk[5], pk[6], pk[7]}), o[i]);
            }
        }
    }
}
DI void p2_phase(const Ctx& k, AttnCtx& c, int h, int qb, int ct3) {
    const int lane = k.lane, qg = k.w & 3, kh = k.w >> 2, r = lane & 31, hi = lane >> 5;
    const lds_ptr lds = k.lds;
    attn_first_loads(k, c, h, qb);
    PreTile pt;
    pretile_read(k, pt);
    WAIT_BAR(5);
    STAMP(6);
    f32x16 o[2];
    bool unsafe;
    attn_fast(k, c, pt, o[0], o[1], ct3, unsafe);
    STAMP(7);
    LAS unsigned* flag = (LAS unsigned*)(lds + AT_FLAG);
    if (__any(unsafe || (hi && !(o[0][8] <= 5.764607523034235e17f && o[1][8] <= 5.764607523034235e17f))) && lane == 0) *flag = 1;
    BAR_L();
    const lds_ptr red = lds + AT_RED + qg * 8192 + lane * 16;
    auto put = [&](const f32x16& v, int slot) {
#pragma unroll
        for (int u = 0; u < 3; ++u) *(LAS f32x4*)(red + slot * 4096 + u * 1024) = (f32x4){v[4 * u], v[4 * u + 1], v[4 * u + 2], v[4 * u + 3]};
    };
    auto get = [&](int slot, int u) { return *(const LAS f32x4*)(red + slot * 4096 + u * 1024); };
    f32x4 of[3];
    if (*flag == 0) {
        if (kh) put(o[0], 0); else put(o[1], 1);
        BAR_L();
#pragma unroll
        for (int u = 0; u < 3; ++u) {
            const f32x4 p = get(kh, u);
#pragma unroll
            for (int j = 0; j < 4; ++j) of[u][j] = p[j] + (kh ? o[1][4 * u + j] : o[0][4 * u + j]);
        }
    } else {
        float mref[2];
        WAIT_BAR(0);
        attn_safe(k, c, o, mref);
        BAR_L();
        if (kh) put(o[0], 0); else put(o[1], 1);
        *(LAS float*)(red + (kh ? 0 : 4096) + 3072) = kh ? mref[0] : mref[1];
        BAR_L();
        const float m0 = kh ? mref[1] : mref[0], m1 = *(const LAS float*)(red + kh * 4096 + 3072), m = fmaxf(m0, m1);
        const float a0 = EX(m0 - m), a1 = EX(m1 - m);
#pragma unroll
        for (int u = 0; u < 3; ++u) {
            const f32x4 p = get(kh, u);
#pragma unroll
            for (int j = 0; j < 4; ++j) of[u][j] = p[j] * a1 + (kh ? o[1][4 * u + j] : o[0][4 * u + j]) * a0;
        }
        BAR_L();
        if (k.w) wo_prefetch(k, ct3);
    }
    {
        auto lr = __builtin_amdgcn_permlane32_swap(__float_as_uint(of[2][0]), __float_as_uint(of[2][0]), false, false);
        const float inv = 1.0f / __uint_as_float(lr[1]);
        const int tok = qb * 256 + qg * 64 + kh * 32 + r;
        const unsigned base = WS_O + (tok >> 4) * OH_TILE + (tok & 15) * 16;
        unsigned pk[3][2];
#pragma unroll
        for (int u = 0; u < 3; ++u) { pk[u][0] = cvtpk(of[u][0] * inv, of[u][1] * inv); pk[u][1] = cvtpk(of[u][2] * inv, of[u][3] * inv); }
        auto oaddr = [&](int col) { return base + (col >> 5) * 1024 + ((col >> 3) & 3) * 256 + (col & 7) * 2; };
        if ((h & 1) == 0) {
            auto s0 = __builtin_amdgcn_permlane32_swap(pk[0][0], pk[1][0], false, false);
            auto s1 = __builtin_amdgcn_permlane32_swap(pk[0][1], pk[1][1], false, false);
            st128(k.rs, oaddr(h * 20 + 8 * hi), (u32x4){s0[0], s1[0], s0[1], s1[1]});
            if (!hi) st64(k.rs, oaddr(h * 20 + 16), (u32x2){pk[2][0], pk[2][1]});
        } else {
            auto s0 = __builtin_amdgcn_permlane32_swap(pk[0][0], pk[2][0], false, false);
            auto s1 = __builtin_amdgcn_permlane32_swap(pk[0][1], pk[2][1], false, false);
            st128(k.rs, oaddr(h * 20 + 4 + 8 * hi), hi ? (u32x4){pk[1][0], pk[1][1], s0[0], s1[0]} : (u32x4){s0[1], s1[1], pk[1][0], pk[1][1]});
            if (!hi) st64(k.rs, oaddr(h * 20), (u32x2){s0[0], s1[0]});
        }
    }
}

DI int own_cf(int kh, int u) { return kh ? (u < 2 ? 3 + u : 4) : u; }
constexpr int RED_LDS = 143360;
struct P3Pre { u32x2 xv[5]; };
DI void p3_pre(const Ctx& k, int rb64, int ct, P3Pre& p) {
    const int tt = k.w & 3, t = k.lane & 15, g = k.lane >> 4;
    if (k.w < 4) {
#pragma unroll
        for (int cf = 0; cf < 5; ++cf) p.xv[cf] = *(const u32x2*)(k.x + (size_t)(rb64 * 64 + tt * 16 + t) * D + ct * 80 + cf * 16 + 4 * g);
    }
}
DI void w13_prefetch(const Ctx& k, int f0, int f);
DI void p3_phase(const Ctx& k, int rb64, int ct, int f0, const P3Pre& pre, u32x2 (&hv)[3]) {
    const int lane = k.lane, tt = k.w & 3, kh = k.w >> 2, t = lane & 15, g = lane >> 4;
    const unsigned ap = WS_O + (rb64 * 4 + tt) * OH_TILE + kh * 10240 + lane * 16;
    const lds_ptr wl = k.lds + WO_LDS + kh * 51200 + lane * 16;
    constexpr int WIN = 5;
    u32x4 a[10];
#pragma unroll
    for (int i = 0; i < WIN; ++i) a[i] = ld128(k.rs, ap + i * 1024);
    bf16x8 wf[2][5];
#pragma unroll
    for (int cf = 0; cf < 5; ++cf) wf[0][cf] = as_frag(lds_r128(wl + cf * 1024));
    f32x4 acc[5];
#pragma unroll
    for (int cf = 0; cf < 5; ++cf) { acc[cf] = (f32x4){0, 0, 0, 0}; PIN(acc[cf]); }
    mfma_fence();
    PRIO(1);
#pragma unroll
    for (int i = 0; i < 10; ++i) {
        if (i + WIN < 10) { a[i + WIN] = ld128(k.rs, ap + (i + WIN) * 1024); SBAR(); }
#pragma unroll
        for (int cf = 0; cf < 5; ++cf) {
            mfma16_acc(acc[cf], wf[i & 1][cf], as_frag(a[i])); SBAR();
            if (i + 1 < 10) wf[(i + 1) & 1][cf] = as_frag(lds_r128(wl + (i + 1) * 5120 + cf * 1024));
            if (cf < 4) PIN(wf[i & 1][cf + 1]); else if (i + 1 < 10) PIN(wf[(i + 1) & 1][0]);
            SBAR();
        }
    }
    PRIO(0);
    mfma_fence();
    STAMP(10);
    BAR_L();
    const lds_ptr red = k.lds + RED_LDS + tt * 5120 + lane * 16;
    if (kh) {
#pragma unroll
        for (int cf = 0; cf < 5; ++cf) *(LAS f32x4*)(red + cf * 1024) = acc[cf];
        BAR_L();
        w13_prefetch(k, f0, k.w - 1);
    } else {
        BAR_L();
        const int tok = rb64 * 64 + tt * 16 + t;
        float s = 0.f, q = 0.f;
        u32x2 hq[5];
#pragma unroll
        for (int cf = 0; cf < 5; ++cf) {
            const f32x4 v = acc[cf] + *(const LAS f32x4*)(red + cf * 1024);
            hq[cf] = (u32x2){cvtpk(v[0] + bflo(pre.xv[cf].x), v[1] + bfhi(pre.xv[cf].x)), cvtpk(v[2] + bflo(pre.xv[cf].y), v[3] + bfhi(pre.xv[cf].y))};
            const int col = ct * 80 + cf * 16 + 4 * g;
            st64(k.rs, WS_H + (tok >> 4) * OH_TILE + (col >> 5) * 1024 + ((col >> 3) & 3) * 256 + t * 16 + (col & 7) * 2, hq[cf]);
            const float h0 = bflo(hq[cf].x), h1 = bfhi(hq[cf].x), h2 = bflo(hq[cf].y), h3 = bfhi(hq[cf].y);
            s += (h0 + h1) + (h2 + h3); q += (h0 * h0 + h1 * h1) + (h2 * h2 + h3 * h3);
        }
        s = sum_x32(sum_x16(s)); q = sum_x32(sum_x16(q));
        if (g == 0) st64(k.rs, WS_PST + (tok >> 4) * 2048 + (ct * 2) * 128 + t * 8, (u32x2){__float_as_uint(s), __float_as_uint(q)});
        if (g == 1) st64(k.rs, WS_PST + (tok >> 4) * 2048 + (ct * 2 + 1) * 128 + t * 8, (u32x2){0u, 0u});
        hv[0] = hq[0]; hv[1] = hq[1]; hv[2] = hq[2];
        const lds_ptr stash = k.lds + RED_LDS + tt * 5120 + lane * 8;
        *(LAS u32x2*)(stash + 3072) = hq[3]; *(LAS u32x2*)(stash + 4096) = hq[4];
    }
}
DI void pair_reduce(lds_ptr red0, int tt, int kh, int lane, const f32x4 (&acc)[5], f32x4 (&own)[3]) {
    const lds_ptr red = red0 + tt * 5120 + lane * 16;
    if (kh == 0) { *(LAS f32x4*)(red + 3072) = acc[3]; *(LAS f32x4*)(red + 4096) = acc[4]; }
    else { *(LAS f32x4*)(red) = acc[0]; *(LAS f32x4*)(red + 1024) = acc[1]; *(LAS f32x4*)(red + 2048) = acc[2]; }
    BAR_L();
    if (kh == 0) {
#pragma unroll
        for (int u = 0; u < 3; ++u) own[u] = acc[u] + *(const LAS f32x4*)(red + u * 1024);
    } else {
        own[0] = acc[3] + *(const LAS f32x4*)(red + 3072); own[1] = acc[4] + *(const LAS f32x4*)(red + 4096); own[2] = own[1];
    }
}
template <int N, int NP>
DI void w2_stage(const Ctx& k, int ct, int b, int s0) {
#pragma unroll
    for (int j = 0; j < NP; ++j) {
        int q = k.w + 8 * j;
        if (q >= 10 * N) q -= 8;
        const int hf = q >= 5 * N, rem = q - hf * 5 * N;
        glds16<true>(k.lane * 16, k.ws + WS_W2 + (ct * 54 + hf * 27 + s0) * 5120 + rem * 1024, k.lds0 + b * 71680 + hf * 35840 + rem * 1024);
    }
}
constexpr int CNT_LDS = RED_LDS + 448, DUMMY_LDS = RED_LDS + 3584;
DI void pair_signal(const Ctx& k, int cnt, unsigned flagoff) {
    LAS unsigned* p = (LAS unsigned*)(k.lds + (k.lane == 0 ? CNT_LDS + cnt * 4 : DUMMY_LDS + k.lane * 4));
    const unsigned old = __hip_atomic_fetch_add(p, 1u, __ATOMIC_RELAXED, __HIP_MEMORY_SCOPE_WORKGROUP);
    const bool last = rfl(old) == 1;
    st64(k.rs, last && k.lane == 0 ? flagoff : 0x80000000u, (u32x2){(unsigned)k.tag, (unsigned)(k.tag >> 32)});
}
DI void p5_phase(const Ctx& k, int rb64, int ct, u32x2 (&hv)[3], unsigned f4b_pub) {
    const int lane = k.lane, tt = k.w & 3, kh = k.w >> 2, t = lane & 15, g = lane >> 4;
    const unsigned ap = WS_HH + (rb64 * 4 + tt) * HH_TILE + kh * 27 * 1024 + lane * 16;
    const unsigned f4b = WS_F4B + (((rb64 >> 2) * 32 + (lane & 31)) * 4 + (rb64 & 3)) * FLAG_STRIDE;
    constexpr int WIN = 6;
    u32x4 a[27];
#pragma unroll
    for (int i = 0; i < WIN; ++i) ald128(a[i], k.rsw, ap, i * 1024);
    w2_stage<7, 9>(k, ct, 1, 7);
    if (k.w >= 4) {
        WAIT_VM(15);
        pair_signal(k, 4 + (k.w >> 1), f4b_pub);
    }
    if (kh) {
        const lds_ptr stash = k.lds + RED_LDS + tt * 5120 + lane * 8;
        hv[0] = *(const LAS u32x2*)(stash + 3072); hv[1] = *(const LAS u32x2*)(stash + 4096); hv[2] = hv[1];
    }
    lds_ptr wb[2] = {k.lds + lane * 16 + kh * 35840, k.lds + lane * 16 + kh * 35840 + 71680};
    PIN(wb[0]); PIN(wb[1]);
    auto wread = [&](int n, int cf) { return as_frag(lds_r128(wb[(n / 7) & 1] + (n % 7) * 5120 + cf * 1024)); };
    bf16x8 wf[2][5];
#pragma unroll
    for (int cf = 0; cf < 5; ++cf) wf[0][cf] = wread(0, cf);
    f32x4 acc[5];
#pragma unroll
    for (int cf = 0; cf < 5; ++cf) { acc[cf] = (f32x4){0, 0, 0, 0}; PIN(acc[cf]); }
    u32x2 fl;
    mfma_fence();
    PRIO(1);
#define P5_STEP(i, NW) do { \
        if ((i) == 6) { WAIT_BAR(6); }                                                      \
        if ((i) == 7) { BAR_L(); ald64(fl, k.rsw, f4b); w2_stage<7, 9>(k, ct, 0, 14); } \
        if ((i) == 10) {     \
            asm volatile("s_waitcnt vmcnt(12)" : "+v"(fl) :: "memory"); \
            if (!__all(fl.x == (unsigned)k.tag && fl.y == (unsigned)(k.tag >> 32))) poll_flags(k, f4b); \
        } \
        if ((i) == 13) { WAIT_BAR(6); w2_stage<6, 8>(k, ct, 1, 21); } \
        if ((i) == 20) { WAIT_BAR(6); } \
        if ((i) + WIN < 27) ald128(a[(i) + WIN], k.rsw, ap, ((i) + WIN) * 1024); \
        ald_wait<NW>(a[i]); SBAR(); \
        _Pragma("unroll") for (int cf = 0; cf < 5; ++cf) { \
            mfma16_acc(acc[cf], wf[(i) & 1][cf], as_frag(a[i])); SBAR(); \
            if ((i) + 1 < 27) wf[((i) + 1) & 1][cf] = wread((i) + 1, cf); \
            if (cf < 4) PIN(wf[(i) & 1][cf + 1]); else if ((i) + 1 < 27) PIN(wf[((i) + 1) & 1][0]); \
            SBAR(); } } while (0)
    P5_STEP(0, 15); P5_STEP(1, 15); P5_STEP(2, 15); P5_STEP(3, 15); P5_STEP(4, 15); P5_STEP(5, 15); P5_STEP(6, 6);
    P5_STEP(7, 15); P5_STEP(8, 15); P5_STEP(9, 15); P5_STEP(10, 15); P5_STEP(11, 15); P5_STEP(12, 15);
    P5_STEP(13, 14); P5_STEP(14, 14); P5_STEP(15, 14); P5_STEP(16, 14); P5_STEP(17, 14); P5_STEP(18, 14); P5_STEP(19, 6);
    P5_STEP(20, 6); P5_STEP(21, 5); P5_STEP(22, 4); P5_STEP(23, 3); P5_STEP(24, 2); P5_STEP(25, 1); P5_STEP(26, 0);
#undef P5_STEP
    PRIO(0);
    mfma_fence();
    STAMP(16);
    f32x4 own[3];
    pair_reduce(k.lds + RED_LDS, tt, kh, lane, acc, own);
    const int tok = rb64 * 64 + tt * 16 + t;
#pragma unroll
    for (int u = 0; u < 3; ++u) {
        if (u < 2 || kh == 0) {
            const int col = ct * 80 + own_cf(kh, u) * 16 + 4 * g;
            *(u32x2*)(k.out + (size_t)tok * D + col) = (u32x2){cvtpk(own[u][0] + bflo(hv[u].x), own[u][1] + bfhi(hv[u].x)), cvtpk(own[u][2] + bflo(hv[u].y), own[u][3] + bfhi(hv[u].y))};
        }
    }
}

constexpr int P4_CS = 143360;
DI void w13_prefetch(const Ctx& k, int f0, int f) {
    const int p0 = f * 20;
#pragma unroll 4
    for (int j = 0; j < 20; ++j) glds16<true>(k.lane * 16, k.ws + WS_W13 + f0 * 20480 + (p0 + j) * 1024, k.lds0 + (p0 + j) * 1024);
}
DI void p4_colsum(const Ctx& k, int f) {
    const int t = k.lane & 15, g = k.lane >> 4;
    u32x4 wv[20];
#pragma unroll
    for (int s = 0; s < 20; ++s) wv[s] = lds_r128(k.lds + (f * 20 + s) * 1024 + k.lane * 16);
    f32x4 c0 = {0, 0, 0, 0}, c1 = {0, 0, 0, 0};
    const bf16x8 ones = {0x3F80, 0x3F80, 0x3F80, 0x3F80, 0x3F80, 0x3F80, 0x3F80, 0x3F80};
#pragma unroll
    for (int s = 0; s < 20; s += 2) { c0 = mfma16(as_frag(wv[s]), ones, c0); c1 = mfma16(as_frag(wv[s + 1]), ones, c1); }
    if (t == 0) *(LAS f32x4*)(k.lds + P4_CS + f * 64 + g * 16) = c0 + c1;
}
template <int NF>
DI void p4_swiglu(const Ctx& k, const f32x4 (&acc)[2][NF], int fbase, const float (&mean)[2], const float (&rstd)[2], const float (&vpe)[2], unsigned (&d)[2][4]) {
    const int g = k.lane >> 4;
#pragma unroll
    for (int f = 0; f < NF; ++f) {
        const f32x4 cs = *(const LAS f32x4*)(k.lds + P4_CS + (fbase + f) * 64 + g * 16);
#pragma unroll
        for (int i = 0; i < 2; ++i) {
            const float ce = -LOG2E * rstd[i], nm = -mean[i];
            const f32x2 ce2 = {ce, ce}, ir22 = {vpe[i], vpe[i]}, nm2 = {nm, nm};
            const f32x2 ga = __builtin_elementwise_fma(nm2, __builtin_shufflevector(cs, cs, 0, 1), __builtin_shufflevector(acc[i][f], acc[i][f], 0, 1));
            const f32x2 ua = __builtin_elementwise_fma(nm2, __builtin_shufflevector(cs, cs, 2, 3), __builtin_shufflevector(acc[i][f], acc[i][f], 2, 3));
            const f32x2 t2 = ga * ce2;
            const f32x2 e2 = {EX(t2.x), EX(t2.y)};
            const f32x2 d2 = __builtin_elementwise_fma(e2, ir22, ir22);
            const f32x2 r2 = {__builtin_amdgcn_rcpf(d2.x), __builtin_amdgcn_rcpf(d2.y)};
            const f32x2 o2 = (ga * ua) * r2;
            d[i][f] = cvtpk(o2.x, o2.y);
        }
    }
}
DI u32x4 p4_transpose(const unsigned (&d)[4]) {
    auto a = __builtin_amdgcn_permlane32_swap(d[0], d[2], false, false);
    auto b = __builtin_amdgcn_permlane32_swap(d[1], d[3], false, false);
    auto e = __builtin_amdgcn_permlane16_swap(a[0], b[0], false, false);
    auto f = __builtin_amdgcn_permlane16_swap(a[1], b[1], false, false);
    return (u32x4){e[0], e[1], f[0], f[1]};
}
DI void p4_phase(const Ctx& k, int rb, int cb, int ct5) {
    const int lane = k.lane, w = k.w, t = lane & 15, g = lane >> 4;
    const lds_ptr lds = k.lds;
    const unsigned ap = WS_H + (rb * 16 + w * 2) * OH_TILE + lane * 16;
    u32x4 a0[20], a1[20];
#define P4_LD(s) do { a0[s] = ld128(k.rs, ap + (s) * 1024); a1[s] = ld128(k.rs, ap + OH_TILE + (s) * 1024); } while (0)
    u32x2 pv[2][4];
#pragma unroll
    for (int i = 0; i < 2; ++i)
#pragma unroll
        for (int j = 0; j < 4; ++j) pv[i][j] = ld64(k.rs, WS_PST + (rb * 16 + w * 2 + i) * 2048 + (4 * g + j) * 128 + t * 8);
    P4_LD(0); P4_LD(1); P4_LD(2); P4_LD(3);
    lds_ptr wb[3] = {lds + lane * 16, lds + lane * 16 + 61440, lds + lane * 16 + 122880};
    PIN(wb[1]); PIN(wb[2]);
    auto wread = [&](int f, int s) { return as_frag(lds_r128(wb[f / 3] + (f % 3) * 20480 + s * 1024)); };
    bf16x8 wf[2][4];
#pragma unroll
    for (int f = 0; f < 4; ++f) wf[0][f] = wread(f, 0);
    f32x4 acc[2][4];
#pragma unroll
    for (int f = 0; f < 4; ++f) { acc[0][f] = (f32x4){0, 0, 0, 0}; acc[1][f] = (f32x4){0, 0, 0, 0}; PIN(acc[0][f]); PIN(acc[1][f]); }
    float mean[2], rstd[2], vpe[2];
    mfma_fence();
    STAMP(13);
    PRIO(1);
#pragma unroll
    for (int s = 0; s < 20; ++s) {
        if (s + 4 < 20) P4_LD(s + 4);
        SBAR();
        if (s == 4) {
#pragma unroll
            for (int i = 0; i < 2; ++i) {
                float sm = (__uint_as_float(pv[i][0].x) + __uint_as_float(pv[i][1].x)) + (__uint_as_float(pv[i][2].x) + __uint_as_float(pv[i][3].x));
                float q = (__uint_as_float(pv[i][0].y) + __uint_as_float(pv[i][1].y)) + (__uint_as_float(pv[i][2].y) + __uint_as_float(pv[i][3].y));
                sm = sum_x32(sum_x16(sm)); q = sum_x32(sum_x16(q));
                mean[i] = sm * (1.0f / D);
                vpe[i] = fmaxf(q * (1.0f / D) - mean[i] * mean[i], 0.f) + LN_EPS;
                rstd[i] = __builtin_amdgcn_rsqf(vpe[i]);
            }
            PIN(mean[0]); PIN(mean[1]); PIN(rstd[0]); PIN(rstd[1]); PIN(vpe[0]); PIN(vpe[1]);
            SBAR();
        }
#pragma unroll
        for (int f = 0; f < 4; ++f) {
            mfma16_acc(acc[0][f], wf[s & 1][f], as_frag(a0[s])); SBAR();
            if (s + 1 < 20) wf[(s + 1) & 1][f] = wread(f, s + 1); else if (f < 3) wf[0][f] = wread(4 + f, 0);
            if (f < 3) PIN(wf[s & 1][f + 1]); else if (s + 1 < 20) PIN(wf[(s + 1) & 1][0]);
            SBAR();
            mfma16_acc(acc[1][f], wf[s & 1][f], as_frag(a1[s])); SBAR();
        }
    }
#undef P4_LD
    PRIO(0);
    mfma_fence();
    STAMP(33);
    const unsigned hhb = WS_HH + (rb * 16 + w * 2) * HH_TILE + lane * 16;
    auto epilogue1 = [&]() {
        unsigned d[2][4];
        p4_swiglu<4>(k, acc, 0, mean, rstd, vpe, d);
#pragma unroll
        for (int i = 0; i < 2; ++i) st128(k.rs, hhb + i * HH_TILE + ((cb >> 4) * 27 + (cb & 15)) * 1024, p4_transpose(d[i]));
    };
    {
        LAS unsigned* p = (LAS unsigned*)(lds + (lane == 0 ? CNT_LDS + 32 : DUMMY_LDS + lane * 4));
        (void)__hip_atomic_fetch_add(p, 1u, __ATOMIC_RELAXED, __HIP_MEMORY_SCOPE_WORKGROUP);
    }
    epilogue1();
    SBAR();
    unsigned touched;
    f32x4 acc2[2][3];
#pragma unroll
    for (int f = 0; f < 3; ++f) { acc2[0][f] = (f32x4){0, 0, 0, 0}; acc2[1][f] = (f32x4){0, 0, 0, 0}; PIN(acc2[0][f]); PIN(acc2[1][f]); }
    mfma_fence();
    PRIO(1);
#pragma unroll
    for (int s = 0; s < 20; ++s) {
        if (s == 17) { WAIT_VM(1); pair_signal(k, w >> 1, WS_F4A + ((rb * 32 + cb) * 4 + (w >> 1)) * FLAG_STRIDE); SBAR(); }
        if (s == 2) {
            touch_lines(touched, k.rsw, WS_W2 + ct5 * 276480 + ((k.lb >> 3) * 540 + w * 64 + lane) * 128);
            SBAR();
        }
#pragma unroll
        for (int f = 0; f < 3; ++f) {
            mfma16_acc(acc2[0][f], wf[s & 1][f], as_frag(a0[s])); SBAR();
            if (s + 1 < 20) wf[(s + 1) & 1][f] = wread(4 + f, s + 1);
            if (f < 2) PIN(wf[s & 1][f + 1]); else if (s + 1 < 20) PIN(wf[(s + 1) & 1][0]);
            SBAR();
            mfma16_acc(acc2[1][f], wf[s & 1][f], as_frag(a1[s])); SBAR();
        }
    }
    PRIO(0);
    mfma_fence();
    asm volatile("s_waitcnt vmcnt(0)" :: "v"(touched) : "memory");
    STAMP(14);
    {
        unsigned d[2][4];
        p4_swiglu<3>(k, acc2, 4, mean, rstd, vpe, *(unsigned (*)[2][4])&d);
        d[0][3] = 0; d[1][3] = 0;
        const int n2 = cb < 24 ? 3 : 2, l = (cb < 24 ? 3 * cb : 72 + 2 * (cb - 24)) + g, ls = l >> 2;
        const unsigned pos = ((ls >= 11 ? 32 + ls : 16 + ls) * 4 + (l & 3)) * 256 + t * 16;
#pragma unroll
        for (int i = 0; i < 2; ++i) st128(k.rs, g < n2 ? WS_HH + (rb * 16 + w * 2 + i) * HH_TILE + pos : 0x80000000u, p4_transpose(d[i]));
    }
    if (w < 4) {
        for (unsigned spins = 0; spins < SPIN_CAP; ++spins) {
            if (rfl(*(volatile LAS unsigned*)(lds + CNT_LDS + 32)) == 8) break;
            __builtin_amdgcn_s_sleep(1);
        }
#pragma unroll 6
        for (int j = 0; j < 18; ++j) {
            int q = w + 4 * j;
            if (q >= 70) q -= 4;
            const int hf = q >= 35, rem = q - hf * 35;
            glds16<true>(lane * 16, k.ws + WS_W2 + (ct5 * 54 + hf * 27) * 5120 + rem * 1024, k.lds0 + hf * 35840 + rem * 1024);
        }
        WAIT_VM(18);
        pair_signal(k, 4 + (w >> 1), WS_F4B + ((rb * 32 + cb) * 4 + (w >> 1)) * FLAG_STRIDE);
        WAIT_VM(0);
    }
}

constexpr int LDS_BYTES = 163840;
__global__ __launch_bounds__(512, 2) void fused_block(const uint16_t* __restrict__ x, const uint16_t* __restrict__ wqkv, const uint16_t* __restrict__ wo,
                                                      const uint16_t* __restrict__ w1, const uint16_t* __restrict__ w3, const uint16_t* __restrict__ w2,
                                                      char* __restrict__ ws, uint16_t* __restrict__ out) {
    extern __shared__ __attribute__((aligned(16))) char smem[];
    Ctx k;
    k.x = x; k.wqkv = wqkv; k.wo = wo; k.w1 = w1; k.w3 = w3; k.w2 = w2; k.ws = ws; k.out = out;
    k.rs = __builtin_amdgcn_make_buffer_rsrc(ws, 0, WS_END + 20480, 0x00020000);
    k.lds = (lds_ptr)smem; k.lds0 = (unsigned)(uintptr_t)smem;
    k.c = blockIdx.x; k.xcd = k.c & 7; k.lb = k.c >> 3;
    k.tid = threadIdx.x; k.lane = k.tid & 63; k.w = rfl(k.tid >> 6);
    k.rsw = (u32x4){(unsigned)(uintptr_t)ws, (unsigned)((uintptr_t)ws >> 32) & 0xffffu, WS_END + 20480, 0x00020000u};
    k.tag = 0;
    k.sc = 0; k.sr = 0;
    STAMP(0);
    startup_load(k);
    const int rb1 = (k.xcd >> 2) * 8 + (k.lb & 7), cb1 = (k.xcd & 3) * 4 + (k.lb >> 3);
    if (cb1 < 15) {
        if (cb1 >= 10) p1_body<true>(k, rb1, cb1); else p1_body<false>(k, rb1, cb1);
        STAMP(3);
        WAIT_BAR(0);
        if (k.tid == 0) st_flag(ws + WS_F1 + (rb1 * 16 + cb1) * FLAG_STRIDE, k.tag);
    } else {
        WAIT_VM(0);
        startup_tag(k);
        ln1_stats(k, rb1);
        if (rb1 < 15) colsum_qkv(k, rb1);
        WAIT_BAR(0);
    }
    STAMP(4);
    if (k.w) pretile_load(k);
    const int h = k.xcd * 4 + (k.lb & 3), qb = k.lb >> 2;
    const int rb64 = k.xcd * 4 + (k.lb >> 3), ct = k.lb & 7;
    const int rb4 = (k.xcd >> 1) * 2 + (k.lb >> 4), cb4 = (k.xcd & 1) * 16 + (k.lb & 15);
    const int f0 = p4_f0(cb4);
    AttnCtx ac;
    attn_setup(k, ac, h);
    if (k.w == 0) {
        const int c0 = (20 * h) >> 7, c1 = (20 * h + 19) >> 7, j = k.lane & 15, sel = k.lane >> 4;
        const int cbk = (sel & 1) ? c1 : c0;
        const unsigned o0 = WS_F1 + (j * 16 + (sel < 2 ? 5 : 10) + cbk) * FLAG_STRIDE;
        const unsigned o1 = WS_F1 + ((2 * qb + (k.lane & 1)) * 16 + ((k.lane & 2) ? c1 : c0)) * FLAG_STRIDE;
        poll_flags2(k, o0, o1);
    }
    WAIT_BAR(0);
    STAMP(5);
    p2_phase(k, ac, h, qb, ct);
    STAMP(8);
    WAIT_BAR(0);
    if (k.tid == 0) st_flag(ws + WS_F2 + (qb * 32 + h) * FLAG_STRIDE, k.tag);
    P3Pre pre;
    p3_pre(k, rb64, ct, pre);
    if (k.w == 0) poll_flags(k, WS_F2 + (k.xcd * 32 + (k.lane & 31)) * FLAG_STRIDE);
    else if (k.w <= 3) w13_prefetch(k, f0, k.w - 1);
    BAR_L();
    if (k.c == 0 && k.tid == 0) st_flag(ws + WS_EPOCH, k.tag);
    STAMP(9);
    u32x2 hv[3];
    p3_phase(k, rb64, ct, f0, pre, hv);
    STAMP(11);
    if (k.w < 4) WAIT_VM(0);
    BAR_L();
    if (k.tid == 0) st_flag(ws + WS_F3 + (k.xcd * 32 + k.lb) * FLAG_STRIDE, k.tag);
    if (k.w == 0) {
        if (k.lane < 9) *(LAS unsigned*)(k.lds + CNT_LDS + k.lane * 4) = 0;
        poll_flags(k, WS_F3 + (rb4 * 32 + (k.lane & 31)) * FLAG_STRIDE);
    } else {
        WAIT_VM(0);
        p4_colsum(k, k.w - 1);
    }
    WAIT_BAR(0);
    STAMP(12);
    p4_phase(k, rb4, cb4, ct);
    STAMP(15);
    if (k.w == 0) poll_flags(k, WS_F4A + (((rb64 >> 2) * 32 + (k.lane & 31)) * 4 + (rb64 & 3)) * FLAG_STRIDE);
    BAR_L();
    STAMP(17);
    p5_phase(k, rb64, ct, hv, WS_F4B + ((rb4 * 32 + cb4) * 4 + (k.w >> 1)) * FLAG_STRIDE);
    STAMP(18);
    STAMP_FLUSH();
}

extern "C" void kernel_launch(void* const* d_in, const int* in_sizes, int n_in, void* d_out, int out_size, void* d_ws, size_t ws_size, hipStream_t stream) {
    (void)hipFuncSetAttribute((const void*)fused_block, hipFuncAttributeMaxDynamicSharedMemorySize, LDS_BYTES);
    fused_block<<<256, 512, LDS_BYTES, stream>>>((const uint16_t*)d_in[0], (const uint16_t*)d_in[1], (const uint16_t*)d_in[2], (const uint16_t*)d_in[4],
                                                 (const uint16_t*)d_in[5], (const uint16_t*)d_in[6], (char*)d_ws, (uint16_t*)d_out);
}
```

```cpp
#include <hip/hip_runtime.h>
#include <hip/hip_bf16.h>
#include <stdint.h>

#define DI __device__ __forceinline__
#define LAS __attribute__((address_space(3)))
#define GAS __attribute__((address_space(1)))
typedef short bf16x8 __attribute__((ext_vector_type(8)));
typedef short s16x4 __attribute__((ext_vector_type(4)));
typedef float f32x2 __attribute__((ext_vector_type(2)));
typedef float f32x4 __attribute__((ext_vector_type(4)));
typedef float f32x16 __attribute__((ext_vector_type(16)));
typedef float f32x32 __attribute__((ext_vector_type(32)));
typedef unsigned u32x2 __attribute__((ext_vector_type(2)));
typedef unsigned u32x4 __attribute__((ext_vector_type(4)));
typedef unsigned long long u64;
typedef __bf16 bf16x2_t __attribute__((ext_vector_type(2)));
typedef short v4i16_t __attribute__((ext_vector_type(4)));
typedef LAS char* lds_ptr;
typedef __amdgpu_buffer_rsrc_t rsrc_t;

constexpr int S = 2048, D = 640, H = 32, DH = 20, F = 1728, NQKV = 3 * D;
constexpr float LN_EPS = 1e-5f;
constexpr float LOG2E = 1.4426950408889634f;
constexpr float QSCALE = 0.22360679774997896f * LOG2E;

constexpr unsigned WS_EPOCH = 0;
constexpr unsigned FLAG_STRIDE = 64, FLAG_ARR = 256 * FLAG_STRIDE;
constexpr unsigned WS_F1 = 4096, WS_FP = WS_F1 + FLAG_ARR, WS_F2 = WS_FP + FLAG_ARR, WS_F3 = WS_F2 + FLAG_ARR;
constexpr unsigned WS_F4A = WS_F3 + FLAG_ARR, WS_F4B = WS_F4A + 4 * FLAG_ARR;
constexpr unsigned WS_ST1 = WS_F4B + 4 * FLAG_ARR;
constexpr unsigned WS_CSQ = WS_ST1 + 32768;
static_assert(WS_CSQ + 1920 * 8 <= 262144, "control region");
constexpr unsigned WS_PST = 262144;
constexpr unsigned WS_Q = 524288;
constexpr unsigned IMG = S * D * 2;
constexpr unsigned QK_TILE = 32 * 1280, QK_IMG = 64 * QK_TILE;
constexpr unsigned WS_K = WS_Q + QK_IMG;
constexpr unsigned WS_V = WS_K + QK_IMG;
constexpr unsigned OH_TILE = 21504, HH_TILE = 56320;
constexpr unsigned WS_O = WS_V + IMG;
constexpr unsigned WS_H = WS_O + 128 * OH_TILE;
constexpr unsigned WS_HH = WS_H + 128 * OH_TILE;
constexpr unsigned WS_WO = WS_HH + 128 * HH_TILE;
constexpr unsigned WS_W13 = WS_WO + D * D * 2;
constexpr unsigned WS_W2 = WS_W13 + D * F * 4;
constexpr unsigned WS_END = WS_W2 + F * D * 2;

DI unsigned cvtpk(float lo, float hi) { f32x2 v = {lo, hi}; bf16x2_t b = __builtin_convertvector(v, bf16x2_t); return __builtin_bit_cast(unsigned, b); }
DI float bflo(unsigned u) { return __uint_as_float(u << 16); }
DI float bfhi(unsigned u) { return __uint_as_float(u & 0xffff0000u); }
DI f32x4 mfma16(bf16x8 a, bf16x8 b, f32x4 c) { return __builtin_amdgcn_mfma_f32_16x16x32_bf16(a, b, c, 0, 0, 0); }
DI void mfma16_acc(f32x4& c, bf16x8 a, bf16x8 b) { asm volatile("v_mfma_f32_16x16x32_bf16 %0, %1, %2, %0" : "+v"(c) : "v"(a), "v"(b)); }
DI void mfma_fence() { __builtin_amdgcn_sched_barrier(0); asm volatile("s_nop 7\n\ts_nop 1" ::: "memory"); __builtin_amdgcn_sched_barrier(0); }
DI f32x16 mfma32(bf16x8 a, bf16x8 b, f32x16 c) { return __builtin_amdgcn_mfma_f32_32x32x16_bf16(a, b, c, 0, 0, 0); }
DI int rfl(int x) { return __builtin_amdgcn_readfirstlane(x); }
template <bool SC1>
DI void glds16(unsigned voff, const void* sbase, unsigned ldsdst) {
    unsigned keep;
    if (SC1) asm volatile("s_mov_b32 %0, m0\n\ts_mov_b32 m0, %3\n\ts_nop 0\n\tglobal_load_lds_dwordx4 %1, %2 sc1\n\ts_mov_b32 m0, %0" : "=&s"(keep) : "v"(voff), "s"(sbase), "s"(ldsdst) : "memory");
    else asm volatile("s_mov_b32 %0, m0\n\ts_mov_b32 m0, %3\n\ts_nop 0\n\tglobal_load_lds_dwordx4 %1, %2\n\ts_mov_b32 m0, %0" : "=&s"(keep) : "v"(voff), "s"(sbase), "s"(ldsdst) : "memory");
}
DI void glds4(const void* gptr, unsigned ldsdst) {
    unsigned keep;
    asm volatile("s_mov_b32 %0, m0\n\ts_mov_b32 m0, %2\n\ts_nop 0\n\tglobal_load_lds_dword %1, off\n\ts_mov_b32 m0, %0" : "=&s"(keep) : "v"(gptr), "s"(ldsdst) : "memory");
}
#define WAIT_VM(n) asm volatile("s_waitcnt vmcnt(" #n ")" ::: "memory")
#define WAIT_BAR(n) asm volatile("s_waitcnt vmcnt(" #n ") lgkmcnt(0)\n\ts_barrier" ::: "memory")
#define BAR_L() asm volatile("s_waitcnt lgkmcnt(0)\n\ts_barrier" ::: "memory")
#define SBAR() __builtin_amdgcn_sched_barrier(0)
#define PIN(x) asm volatile("" : "+v"(x))
#define PRIO(n) __builtin_amdgcn_s_setprio(n)
#define EX(v) __builtin_amdgcn_exp2f(v)
DI u32x4 lds_r128(lds_ptr p) { return *(const LAS u32x4*)p; }
DI s16x4 lds_tr(lds_ptr p) { return __builtin_bit_cast(s16x4, __builtin_amdgcn_ds_read_tr16_b64_v4i16((LAS v4i16_t*)p)); }
DI bf16x8 as_frag(u32x4 v) { return __builtin_bit_cast(bf16x8, v); }
DI bf16x8 cat_frag(s16x4 a, s16x4 b) { return (bf16x8){a[0], a[1], a[2], a[3], b[0], b[1], b[2], b[3]}; }
DI float swap32_max(float m) { auto r = __builtin_amdgcn_permlane32_swap(__float_as_uint(m), __float_as_uint(m), false, false); return fmaxf(__uint_as_float(r[0]), __uint_as_float(r[1])); }
DI float sum_x16(float v) { auto r = __builtin_amdgcn_permlane16_swap(__float_as_uint(v), __float_as_uint(v), false, false); return __uint_as_float(r[0]) + __uint_as_float(r[1]); }
DI float sum_x32(float v) { auto r = __builtin_amdgcn_permlane32_swap(__float_as_uint(v), __float_as_uint(v), false, false); return __uint_as_float(r[0]) + __uint_as_float(r[1]); }
DI u32x4 ld128(rsrc_t r, unsigned off) { return __builtin_amdgcn_raw_buffer_load_b128(r, off, 0, 16); }
DI u32x2 ld64(rsrc_t r, unsigned off) { return __builtin_amdgcn_raw_buffer_load_b64(r, off, 0, 16); }
DI unsigned ld32(rsrc_t r, unsigned off) { return __builtin_amdgcn_raw_buffer_load_b32(r, off, 0, 16); }
DI void st128(rsrc_t r, unsigned off, u32x4 v) { __builtin_amdgcn_raw_buffer_store_b128(v, r, off, 0, 16); }
DI void st64(rsrc_t r, unsigned off, u32x2 v) { __builtin_amdgcn_raw_buffer_store_b64(v, r, off, 0, 16); }

DI void ald128(u32x4& v, u32x4 rsw, unsigned voff, unsigned soff) { asm volatile("buffer_load_dwordx4 %0, %1, %2, %3 offen sc1" : "=&v"(v) : "v"(voff), "s"(rsw), "s"(soff) : "memory"); }
template <int N> DI void ald_wait(u32x4& v) { asm volatile("s_waitcnt vmcnt(%1)" : "+v"(v) : "i"(N) : "memory"); }
DI void ald64(u32x2& v, u32x4 rsw, unsigned voff) { asm volatile("buffer_load_dwordx2 %0, %1, %2, 0 offen sc1" : "=&v"(v) : "v"(voff), "s"(rsw) : "memory"); }
DI void touch_lines(unsigned& dummy, u32x4 rsw, unsigned voff) { asm volatile("buffer_load_dword %0, %1, %2, 0 offen sc1" : "=&v"(dummy) : "v"(voff), "s"(rsw) : "memory"); }
struct Ctx {
    const uint16_t *x, *wqkv, *wo, *w1, *w3, *w2;
    char* ws; uint16_t* out;
    rsrc_t rs; u32x4 rsw; u64 tag; lds_ptr lds; unsigned lds0;
    int c, xcd, lb, tid, lane, w;
    mutable unsigned sc, sr;
};

#define STAMP(i)
#define STAMP_FLUSH()

DI u64 ld_flag(const char* p) { return __hip_atomic_load((const GAS u64*)p, __ATOMIC_RELAXED, __HIP_MEMORY_SCOPE_AGENT); }
DI void st_flag(char* p, u64 v) { __hip_atomic_store((GAS u64*)p, v, __ATOMIC_RELAXED, __HIP_MEMORY_SCOPE_AGENT); }
constexpr unsigned SPIN_CAP = 1u << 18;
DI void poll_flags(const Ctx& k, unsigned off) {
    u64 a = ld_flag(k.ws + off);
    __builtin_amdgcn_s_sleep(8);
    for (unsigned spins = 0; spins < SPIN_CAP; ++spins) {
        const u64 b = ld_flag(k.ws + off);
        if (__all(a == k.tag)) break;
        a = ld_flag(k.ws + off);
        if (__all(b == k.tag)) break;
    }
}
DI void poll_flags2(const Ctx& k, unsigned off0, unsigned off1) {
    u64 a0 = ld_flag(k.ws + off0), a1 = ld_flag(k.ws + off1);
    __builtin_amdgcn_s_sleep(8);
    for (unsigned spins = 0; spins < SPIN_CAP; ++spins) {
        const u64 b0 = ld_flag(k.ws + off0), b1 = ld_flag(k.ws + off1);
        if (__all(a0 == k.tag && a1 == k.tag)) break;
        a0 = ld_flag(k.ws + off0); a1 = ld_flag(k.ws + off1);
        if (__all(b0 == k.tag && b1 == k.tag)) break;
    }
}
DI void poll_all(const Ctx& k, unsigned arr) {
    for (unsigned spins = 0; spins < SPIN_CAP; ++spins) {
        const unsigned o = arr + k.lane * FLAG_STRIDE;
        const u64 a = ld_flag(k.ws + o), b = ld_flag(k.ws + o + 64 * FLAG_STRIDE), c = ld_flag(k.ws + o + 128 * FLAG_STRIDE), d = ld_flag(k.ws + o + 192 * FLAG_STRIDE);
        if (__all(a == k.tag && b == k.tag && c == k.tag && d == k.tag)) break;
        __builtin_amdgcn_s_sleep(1);
    }
}

constexpr int SU_LDS = 131072;
DI void startup_load(const Ctx& k) { glds4(k.ws + WS_EPOCH + (k.lane & 1) * 4, k.lds0 + SU_LDS + k.w * 256); }
DI void startup_tag(Ctx& k) {
    const u32x2 e = *(const LAS u32x2*)(k.lds + SU_LDS + k.w * 256);
    k.tag = (((u64)(unsigned)rfl(e.y) << 32) | (unsigned)rfl(e.x)) + 1;
}
DI void ln1_stats(const Ctx& k, int j) {
    u32x4 v[2][10];
#pragma unroll
    for (int p = 0; p < 2; ++p)
#pragma unroll
        for (int i = 0; i < 10; ++i) v[p][i] = *(const u32x4*)(k.x + (size_t)(128 * j + 16 * k.w + 8 * p + (k.lane >> 3)) * D + ((k.lane & 7) + 8 * i) * 8);
#pragma unroll
    for (int p = 0; p < 2; ++p) {
        float s = 0.f, q = 0.f;
#pragma unroll
        for (int i = 0; i < 10; ++i)
#pragma unroll
            for (int e = 0; e < 4; ++e) { const float a = bflo(v[p][i][e]), b = bfhi(v[p][i][e]); s += a + b; q = fmaf(a, a, fmaf(b, b, q)); }
#pragma unroll
        for (int o = 4; o >= 1; o >>= 1) { s += __shfl_xor(s, o); q += __shfl_xor(q, o); }
        const float mean = s * (1.0f / D), var = fmaxf(q * (1.0f / D) - mean * mean, 0.f);
        const float val = (k.lane & 1) ? 1.0f / sqrtf(var + LN_EPS) : mean;
        const int row = 128 * j + 16 * k.w + 8 * p + (k.lane >> 3);
        if ((k.lane & 7) < 2) st64(k.rs, WS_ST1 + row * 16 + (k.lane & 1) * 8, (u32x2){__float_as_uint(val), (unsigned)k.tag});
    }
}
DI void colsum_qkv(const Ctx& k, int cb) {
    float a[8] = {0, 0, 0, 0, 0, 0, 0, 0};
    u32x4 v[20];
#pragma unroll
    for (int i = 0; i < 20; ++i) v[i] = *(const u32x4*)(k.wqkv + (size_t)(k.w * 80 + i * 4 + (k.lane >> 4)) * NQKV + cb * 128 + (k.lane & 15) * 8);
#pragma unroll
    for (int i = 0; i < 20; ++i)
#pragma unroll
        for (int j = 0; j < 4; ++j) { a[2 * j] += bflo(v[i][j]); a[2 * j + 1] += bfhi(v[i][j]); }
    LAS float* red = (LAS float*)k.lds;
#pragma unroll
    for (int j = 0; j < 8; ++j) { a[j] += __shfl_xor(a[j], 16); a[j] += __shfl_xor(a[j], 32); }
    if (k.lane < 16) {
#pragma unroll
        for (int j = 0; j < 8; ++j) red[k.w * 128 + k.lane * 8 + j] = a[j];
    }
    BAR_L();
    if (k.tid < 128) {
        float t = 0.f;
#pragma unroll
        for (int i = 0; i < 8; ++i) t += red[i * 128 + k.tid];
        st64(k.rs, WS_CSQ + (cb * 128 + k.tid) * 8, (u32x2){__float_as_uint(t), (unsigned)k.tag});
    }
}

DI int p4_f0(int cb) { return cb < 24 ? cb * 7 : 168 + (cb - 24) * 6; }
DI int hh_f8(int sigma, int gp) {
    const int kh = sigma >= 27, i = sigma - 27 * kh;
    if (i < 16) return p4_f0(16 * kh + i) + gp;
    const int l = 4 * (11 * kh + i - 16) + gp, cb = l < 72 ? l / 3 : 24 + (l - 72) / 2;
    return p4_f0(cb) + 4 + (l < 72 ? l % 3 : (l - 72) % 2);
}
constexpr int PT_LDS = 131072;
DI int pt_off(int i) { return PT_LDS + (i < 3 ? i * 8192 : 24576 + (i - 3) * 4096); }
DI void pretile_load(const Ctx& k) {
    const int wv = k.w - 1;
#pragma unroll
    for (int i = 0; i < 5; ++i) {
        const int u = k.c + 256 * i;
        if (u < 540) {
            const int s = u % 20, fb = u / 20;
            const uint16_t* src = ((k.lane & 1) ? k.w3 : k.w1) + fb * 64 + (k.lane >> 1) * 2 + (size_t)32 * s * F;
            for (int r = wv; r < 32; r += 7) glds4(src + (size_t)r * F, k.lds0 + pt_off(i) + r * 256);
        } else {
            const int pc = (i & 1) ? wv - 3 : wv;
            if (pc >= 0 && pc < 4) {
                const bool is2 = u < 1080;
                const int uu = is2 ? u - 540 : u - 1080, s = uu / 10, nb = uu % 10;
                const int r = (is2 ? 8 * hh_f8(s, pc) : 32 * s + 8 * pc) + (k.lane >> 3);
                glds16<false>(r * (D * 2) + nb * 128 + (k.lane & 7) * 16, is2 ? k.w2 : k.wo, k.lds0 + pt_off(i) + pc * 1024);
            }
        }
    }
}
struct PreTile { u32x4 v[5]; };
DI void pretile_read(const Ctx& k, PreTile& p) {
    const int g = k.lane >> 4, q = (k.lane >> 2) & 3, pp = k.lane & 3;
#pragma unroll
    for (int i = 0; i < 5; ++i) {
        const int u = k.c + 256 * i;
        const bool wide = u < 540;
        const int rs = wide ? 256 : 128, f = wide ? k.w : (k.w & 3);
        const lds_ptr a = k.lds + pt_off(i) + (8 * g + q) * rs + f * 32 + pp * 8;
        p.v[i] = __builtin_bit_cast(u32x4, cat_frag(lds_tr(a), lds_tr(a + 4 * rs)));
    }
}
DI void pretile_store(const Ctx& k, const PreTile& p, int i) {
    const int u = k.c + 256 * i;
    unsigned dst;
    if (u < 540) dst = WS_W13 + (((u / 20) * 8 + k.w) * 20 + u % 20) * 1024;
    else {
        const bool is2 = u < 1080;
        const int uu = is2 ? u - 540 : u - 1080, s = uu / 10, cfg = (uu % 10) * 4 + (k.w & 3), ct = cfg / 5, cf = cfg % 5;
        dst = is2 ? WS_W2 + ((ct * 54 + s) * 5 + cf) * 1024 : WS_WO + ((ct * 20 + s) * 5 + cf) * 1024;
    }
    st128(k.rs, dst + k.lane * 16, p.v[i]);
}

constexpr int P1_SLOT = 32768;
struct P1Frag { bf16x8 a[4], b[2]; };
template <bool ISV> DI void p1_mma(f32x4 (&acc)[4][2], const P1Frag& f, int m, int n) { if (ISV) mfma16_acc(acc[m][n], f.a[m], f.b[n]); else mfma16_acc(acc[m][n], f.b[n], f.a[m]); }
template <bool ISV>
DI void p1_body(Ctx& k, int rb, int cb) {
    const int lane = k.lane, w = k.w, th = w >> 2, cg = w & 3, t = lane & 15, g = lane >> 4;
    unsigned voffA[2], voffW[2];
#pragma unroll
    for (int i = 0; i < 2; ++i) {
        const int ra = 8 * (2 * w + i) + (lane >> 3), ca = (lane & 7) ^ ((ra >> 1) & 7);
        voffA[i] = ra * (D * 2) + ca * 16;
        const int rw = 4 * (2 * w + i) + (lane >> 4), fw = (rw & 3) | (((rw >> 3) & 1) << 2), cw = (lane & 15) ^ (fw << 1);
        voffW[i] = rw * (NQKV * 2) + cw * 16;
    }
    const char* gA = (const char*)k.x + (size_t)rb * 128 * D * 2;
    const char* gW = (const char*)k.wqkv + cb * 256;
    auto stage = [&](int kt) {
        const unsigned sl = k.lds0 + (kt & 3) * P1_SLOT + w * 2048;
#pragma unroll
        for (int i = 0; i < 2; ++i) glds16<false>(voffA[i], gA + kt * 128, sl + i * 1024);
#pragma unroll
        for (int i = 0; i < 2; ++i) glds16<false>(voffW[i], gW + (size_t)kt * 64 * NQKV * 2, sl + 16384 + i * 1024);
    };
    stage(0); stage(1); stage(2); stage(3);
    int aoff[2], woff[2];
    aoff[0] = (th * 64 + t) * 128 + ((g ^ (t >> 1)) << 4); aoff[1] = aoff[0] ^ 64;
    const int q4 = (lane >> 2) & 3, p = lane & 3, fw = q4 | ((g & 1) << 2);
#pragma unroll
    for (int n = 0; n < 2; ++n) woff[n] = 16384 + (8 * g + q4) * 256 + (((2 * cg + n) ^ fw) << 5) + p * 8;
    f32x4 acc[4][2];
#pragma unroll
    for (int m = 0; m < 4; ++m) { acc[m][0] = (f32x4){0, 0, 0, 0}; acc[m][1] = (f32x4){0, 0, 0, 0}; PIN(acc[m][0]); PIN(acc[m][1]); }
    mfma_fence();
    P1Frag f0, f1;
#define P1_RA(F_, sl, ks, m) F_.a[m] = as_frag(lds_r128(sl + aoff[ks] + (m) * 2048))
#define P1_RB(F_, sl, ks, n) F_.b[n] = cat_frag(lds_tr(sl + woff[n] + (ks) * 8192), lds_tr(sl + woff[n] + (ks) * 8192 + 1024))
#define P1_STEP(RD, FN, sl, ks, FC) do { \
        p1_mma<ISV>(acc, FC, 0, 0); SBAR(); if (RD) P1_RB(FN, sl, ks, 0); PIN(FC.b[1]); SBAR(); \
        p1_mma<ISV>(acc, FC, 0, 1); SBAR(); if (RD) P1_RA(FN, sl, ks, 0); PIN(FC.a[1]); SBAR(); \
        p1_mma<ISV>(acc, FC, 1, 0); SBAR(); if (RD) P1_RA(FN, sl, ks, 1); SBAR(); \
        p1_mma<ISV>(acc, FC, 1, 1); SBAR(); if (RD) P1_RB(FN, sl, ks, 1); PIN(FC.a[2]); SBAR(); \
        p1_mma<ISV>(acc, FC, 2, 0); SBAR(); if (RD) P1_RA(FN, sl, ks, 2); SBAR(); \
        p1_mma<ISV>(acc, FC, 2, 1); SBAR(); if (RD) P1_RA(FN, sl, ks, 3); PIN(FC.a[3]); SBAR(); \
        p1_mma<ISV>(acc, FC, 3, 0); SBAR(); \
        p1_mma<ISV>(acc, FC, 3, 1); SBAR(); } while (0)
    WAIT_BAR(12);
    STAMP(1);
    startup_tag(k);
    { const lds_ptr sl = k.lds; P1_RB(f0, sl, 0, 0); P1_RB(f0, sl, 0, 1); P1_RA(f0, sl, 0, 0); P1_RA(f0, sl, 0, 1); P1_RA(f0, sl, 0, 2); P1_RA(f0, sl, 0, 3); SBAR(); }
    u32x2 gr = {0, 0};
    PRIO(1);
    const unsigned groff = k.tid < 256 ? WS_ST1 + (rb * 128) * 16 + k.tid * 8 : WS_CSQ + (cb * 128 + (k.tid & 127)) * 8;
#pragma unroll
    for (int kt = 0; kt < 10; ++kt) {
        const lds_ptr sl = k.lds + (kt & 3) * P1_SLOT, sn = k.lds + ((kt + 1) & 3) * P1_SLOT;
        P1_STEP(true, f1, sl, 1, f0);
        if (kt < 9) {
            if (kt < 7) WAIT_BAR(8); else if (kt == 7) WAIT_BAR(4); else WAIT_BAR(0);
            if (kt == 6) gr = ld64(k.rs, groff);
            if (kt + 4 < 10) stage(kt + 4);
            SBAR();
        }
        P1_STEP(kt < 9, f0, sn, 0, f1);
    }
#undef P1_RA
#undef P1_RB
#undef P1_STEP
    PRIO(0);
    mfma_fence();
    STAMP(2);
    for (unsigned spins = 0; spins < SPIN_CAP; ++spins) {
        if (__all(gr.y == (unsigned)k.tag)) break;
        gr = ld64(k.rs, groff);
    }
    LAS float* sst = (LAS float*)k.lds;
    if (k.tid < 384) sst[k.tid] = __uint_as_float(gr.x);
    BAR_L();
    const LAS f32x2* st = (const LAS f32x2*)sst + th * 64;
    const LAS float* cs = sst + 256 + cg * 32;
    if (!ISV) {
        const float osc = cb < 5 ? QSCALE : 1.0f;
        const unsigned img = cb < 5 ? WS_Q : WS_K;
        const int cbs = cb % 5;
        unsigned dst[2]; f32x4 c4[2];
#pragma unroll
        for (int n = 0; n < 2; ++n) {
            const int cgi = cbs * 32 + cg * 8 + n * 4 + g, hd = cgi / 5, gi = cgi - 5 * hd;
            const int blk = gi == 4 ? 4 : ((gi & 1) << 1) | (gi >> 1);
            dst[n] = img + (rb * 4 + th * 2) * QK_TILE + hd * 1280 + blk * 256 + t * 8;
            c4[n] = *(const LAS f32x4*)(cs + n * 16 + 4 * g);
        }
#pragma unroll
        for (int m = 0; m < 4; ++m) {
            const f32x2 sm = st[m * 16 + t];
            const float r = sm.y * osc;
#pragma unroll
            for (int n = 0; n < 2; ++n) {
                const f32x4 y = (acc[m][n] - c4[n] * sm.x) * r;
                const unsigned o = dst[n] + (m >> 1) * QK_TILE + (m & 1) * 128;
                st64(k.rs, o, (u32x2){cvtpk(y[0], y[1]), cvtpk(y[2], y[3])});
            }
        }
    } else {
        const int pos = ((g & 1) << 1) | (g >> 1);
        const unsigned dst = WS_V + (rb * 8 + th * 4) * 20480 + ((cb - 10) * 128 + cg * 32 + t) * 32 + pos * 8;
        const float c1[2] = {cs[t], cs[16 + t]};
#pragma unroll
        for (int m = 0; m < 4; ++m) {
            f32x2 sm[4];
#pragma unroll
            for (int i = 0; i < 4; ++i) sm[i] = st[m * 16 + 4 * g + i];
#pragma unroll
            for (int n = 0; n < 2; ++n) {
                float y[4];
#pragma unroll
                for (int i = 0; i < 4; ++i) y[i] = (acc[m][n][i] - c1[n] * sm[i].x) * sm[i].y;
                st64(k.rs, dst + m * 20480 + n * 512, (u32x2){cvtpk(y[0], y[1]), cvtpk(y[2], y[3])});
            }
        }
    }
}

constexpr int AT_KT = 8192, AT_KB = 10240, AT_HALF = AT_KB + 10240, AT_BUF = 2 * AT_HALF;
constexpr int AT_ONES = AT_BUF, AT_ONES_BYTES = 10752, AT_FLAG = AT_ONES + AT_ONES_BYTES, WO_LDS = 61440, AT_RED = 0;
DI int at_slot(int i) { return i == 0 ? 0 : (i == 1 ? 61440 : 61440 + AT_BUF); }
struct AttnCtx {
    unsigned voff[5]; const char* gsrc; unsigned ldst, sstride;
    u32x4 qf0[2]; u32x2 qt;
    int kofs, kt, vlane; bool vdata;
};
DI void attn_stage(const AttnCtx& c, int sidx, int slot) {
#pragma unroll
    for (int i = 0; i < 5; ++i) glds16<true>(c.voff[i], c.gsrc + sidx * c.sstride, c.ldst + at_slot(slot) + i * 1024);
}
DI void attn_setup(const Ctx& k, AttnCtx& c, int h) {
    const int lane = k.lane, w = k.w, kh = w >> 2, r = lane & 31, hi = lane >> 5;
    const bool stV = (w >> 1) & 1;
#pragma unroll
    for (int i = 0; i < 5; ++i) {
        const int pc = 5 * (w & 1) + i;
        if (!stV) c.voff[i] = pc < 8 ? pc * QK_TILE + lane * 16 : ((pc - 8) * 4 + (lane >> 4)) * QK_TILE + 1024 + (lane & 15) * 16;
        else { const int ci = 64 * pc + lane, st = ci / 40, wi = ci % 40; c.voff[i] = st * 20480 + ((wi % 20) * 2 + wi / 20) * 16; }
    }
    c.gsrc = k.ws + (stV ? WS_V + h * 640 + kh * 64 * 20480 : WS_K + h * 1280 + kh * 32 * QK_TILE);
    c.sstride = stV ? 16 * 20480 : 8 * QK_TILE;
    c.ldst = k.lds0 + kh * AT_HALF + (stV ? AT_KB : 0) + (w & 1) * 5120;
    c.kofs = kh * AT_HALF + hi * 256 + r * 8;
    c.kt = kh * AT_HALF + AT_KT + r * 8;
    c.vdata = r < 20;
    c.vlane = c.vdata ? kh * AT_HALF + AT_KB + hi * 320 + r * 16 : AT_ONES + (r == 20 ? 128 : r < 28 ? 144 : hi ? 16 : 208);
    for (int i = k.tid; i < AT_ONES_BYTES / 4; i += 512) ((LAS unsigned*)(k.lds + AT_ONES))[i] = (i & 4) ? 0u : 0x3F803F80u;
    if (k.tid == 0) *(LAS unsigned*)(k.lds + AT_FLAG) = 0;
}
DI void attn_first_loads(const Ctx& k, AttnCtx& c, int h, int qb) {
    const int qg = k.w & 3, r = k.lane & 31, hi = k.lane >> 5;
    attn_stage(c, 0, 0);
#pragma unroll
    for (int i = 0; i < 2; ++i) {
        const unsigned qp = WS_Q + (qb * 8 + qg * 2 + i) * QK_TILE + h * 1280 + r * 8;
        const u32x2 a = ld64(k.rs, qp + hi * 256), b = ld64(k.rs, qp + hi * 256 + 512);
        c.qf0[i] = (u32x4){a.x, a.y, b.x, b.y};
    }
    c.qt = ld64(k.rs, WS_Q + (qb * 8 + qg * 2 + hi) * QK_TILE + h * 1280 + r * 8 + 1024);
    attn_stage(c, 1, 1);
}
DI bf16x8 kfrag0(lds_ptr p) { const u32x2 a = *(const LAS u32x2*)p, b = *(const LAS u32x2*)(p + 512); return as_frag((u32x4){a.x, a.y, b.x, b.y}); }
DI s16x4 kfrag1(lds_ptr p) { return __builtin_bit_cast(s16x4, *(const LAS u32x2*)p); }
DI s16x4 as_frag4(u32x2 v) { return __builtin_bit_cast(s16x4, v); }
DI f32x32 mfma2b(s16x4 a, s16x4 b, f32x32 c) { return __builtin_amdgcn_mfma_f32_32x32x4bf16_1k(a, b, c, 0, 0, 0); }
DI f32x16 lo16(const f32x32& v) { return __builtin_shufflevector(v, v, 0, 1, 2, 3, 4, 5, 6, 7, 8, 9, 10, 11, 12, 13, 14, 15); }
DI f32x16 hi16(const f32x32& v) { return __builtin_shufflevector(v, v, 16, 17, 18, 19, 20, 21, 22, 23, 24, 25, 26, 27, 28, 29, 30, 31); }
DI void wo_prefetch(const Ctx& k, int ct) {
#pragma unroll 5
    for (int j = 0; j < 15; ++j) { int pc = k.w - 1 + 7 * j; if (pc >= 100) pc -= 7; glds16<true>(k.lane * 16, k.ws + WS_WO + ct * 102400 + pc * 1024, k.lds0 + WO_LDS + pc * 1024); }
}
DI void attn_fast(const Ctx& k, AttnCtx& c, const PreTile& pt, f32x16& oA, f32x16& oB, int ct3, bool& unsafe) {
    const lds_ptr lds = k.lds;
    const int kh = k.w >> 2;
    const bf16x8 qA0 = as_frag(c.qf0[0]), qB0 = as_frag(c.qf0[1]); const s16x4 qT = as_frag4(c.qt);
    f32x32 negm;
    {
        const bf16x8 k0 = kfrag0(lds + c.kofs - kh * AT_HALF); const s16x4 k1 = kfrag1(lds + c.kt - kh * AT_HALF);
        const f32x32 t = mfma2b(k1, qT, (f32x32){});
        const f32x16 a = mfma32(k0, qA0, lo16(t)), b = mfma32(k0, qB0, hi16(t));
        float ma = a[0], mb = b[0];
#pragma unroll
        for (int j = 1; j < 16; ++j) { ma = fmaxf(ma, a[j]); mb = fmaxf(mb, b[j]); }
        ma = swap32_max(ma); mb = swap32_max(mb);
        unsafe = !(fabsf(ma) <= 1048576.f && fabsf(mb) <= 1048576.f);
#pragma unroll
        for (int j = 0; j < 16; ++j) { negm[j] = -ma; negm[16 + j] = -mb; }
        PIN(negm);
    }
    f32x16 sA, sB, nA, nB;
    oA = (f32x16){}; oB = (f32x16){};
    bf16x8 kf0 = kfrag0(lds + c.kofs); s16x4 kf1 = kfrag1(lds + c.kt);
    { const f32x32 t = mfma2b(kf1, qT, negm); sA = mfma32(kf0, qA0, lo16(t)); sB = mfma32(kf0, qB0, hi16(t)); }
    kf0 = kfrag0(lds + c.kofs + 1024); kf1 = kfrag1(lds + c.kt + 256);
    u32x4 pA0, pA1, pB0, pB1 = {0, 0, 0, 0};
    bf16x8 vf0, vf1 = {0, 0, 0, 0, 0, 0, 0, 0};
    u32x2 fa = {0, 0}, fb = fa, fc = fa, fd = fa;
#define GAP(MF, X, i, P, j) do { MF; SBAR(); X[i] = EX(X[i]); X[i + 1] = EX(X[i + 1]); X[i + 2] = EX(X[i + 2]); X[i + 3] = EX(X[i + 3]); \
        P[j] = cvtpk(X[i], X[i + 1]); P[j + 1] = cvtpk(X[i + 2], X[i + 3]); PIN(P); SBAR(); } while (0)
#define STEP(SA, SB, NA, NB, VP, KP, KT, HK) do { f32x32 tl_; \
        GAP(tl_ = mfma2b(kf1, qT, negm), SA, 0, pA0, 0); vf0 = as_frag(lds_r128(VP)); HK(0); SBAR(); \
        GAP(oB = mfma32(vf1, as_frag(pB1), oB), SA, 4, pA0, 2); vf1 = as_frag(lds_r128((VP) + 640)); HK(1); SBAR(); \
        GAP((void)0, SA, 8, pA1, 0); HK(2); SBAR(); \
        GAP(NA = mfma32(kf0, qA0, lo16(tl_)), SA, 12, pA1, 2); HK(3); SBAR(); \
        GAP(NB = mfma32(kf0, qB0, hi16(tl_)), SB, 0, pB0, 0); kf0 = kfrag0(KP); kf1 = kfrag1(KT); HK(4); SBAR(); \
        GAP(oA = mfma32(vf0, as_frag(pA0), oA), SB, 4, pB0, 2); HK(5); SBAR(); \
        GAP(oA = mfma32(vf1, as_frag(pA1), oA), SB, 8, pB1, 0); HK(6); SBAR(); \
        GAP(oB = mfma32(vf0, as_frag(pB0), oB), SB, 12, pB1, 2); HK(7); SBAR(); \
    } while (0)
#define HK_NONE(j)
#define STAGE(sidx, slot, nslot, PT, NST, HA, HB) do { \
        const int sb = at_slot(slot), sn = at_slot(nslot); \
        const lds_ptr vb = lds + (c.vdata ? c.vlane + sb : c.vlane); \
        lds_ptr k0 = lds + c.kofs + sb, k0n = lds + c.kofs + sn, k1 = lds + c.kt + sb, k1n = lds + c.kt + sn; \
        PIN(k0); PIN(k0n); PIN(k1); PIN(k1n); \
        STEP(sA, sB, nA, nB, vb, k0 + 2 * 1024, k1 + 2 * 256, HK_NONE); PT(0); \
        STEP(nA, nB, sA, sB, vb + 1280, k0 + 3 * 1024, k1 + 3 * 256, HK_NONE); PT(1); \
        STEP(sA, sB, nA, nB, vb + 2 * 1280, k0 + 4 * 1024, k1 + 4 * 256, HK_NONE); PT(2); \
        STEP(nA, nB, sA, sB, vb + 3 * 1280, k0 + 5 * 1024, k1 + 5 * 256, HK_NONE); PT(3); \
        STEP(sA, sB, nA, nB, vb + 4 * 1280, k0 + 6 * 1024, k1 + 6 * 256, HK_NONE); PT(4); \
        STEP(nA, nB, sA, sB, vb + 5 * 1280, k0 + 7 * 1024, k1 + 7 * 256, HK_NONE); \
        if ((sidx) == 3 && k.w == 0) {     \
            const unsigned tl = (unsigned)k.tag, th = (unsigned)(k.tag >> 32); \
            if (!__all(fa.x == tl && fa.y == th && fb.x == tl && fb.y == th && fc.x == tl && fc.y == th && fd.x == tl && fd.y == th)) poll_all(k, WS_FP); \
        } \
        WAIT_BAR(NST); \
        if ((sidx) < 2) attn_stage(c, (sidx) + 2, (nslot) == 2 ? 0 : (nslot) + 1); \
        if ((sidx) == 1 && k.tid == 0) st_flag(k.ws + WS_FP + k.c * FLAG_STRIDE, k.tag);     \
        if ((sidx) == 2 && k.w == 0) {     \
            const unsigned o = WS_FP + k.lane * FLAG_STRIDE; \
            fa = ld64(k.rs, o); fb = ld64(k.rs, o + 64 * FLAG_STRIDE); fc = ld64(k.rs, o + 128 * FLAG_STRIDE); fd = ld64(k.rs, o + 192 * FLAG_STRIDE); \
        } \
        SBAR(); \
        STEP(sA, sB, nA, nB, vb + 6 * 1280, k0n, k1n, HA); \
        STEP(nA, nB, sA, sB, vb + 7 * 1280, k0n + 1024, k1n + 256, HB); \
    } while (0)
#define PT_STORE(j) do { pretile_store(k, pt, j); SBAR(); } while (0)
#define PT_NONE(j)
    const char* wo_src = k.ws + WS_WO + ct3 * 102400;
#define WO_PIECE(n) do { int pc_ = k.w + 8 * (n); if (pc_ >= 100) pc_ -= 8; glds16<true>(k.lane * 16, wo_src + pc_ * 1024, k.lds0 + WO_LDS + pc_ * 1024); } while (0)
#define HK_WO_A(j) WO_PIECE(j)
#define HK_WO_B(j) do { if ((j) < 5) WO_PIECE(8 + (j)); } while (0)
    PRIO(1);
    STAGE(0, 0, 1, PT_STORE, 5, HK_NONE, HK_NONE);
    STAGE(1, 1, 2, PT_NONE, 0, HK_NONE, HK_NONE);
    STAGE(2, 2, 0, PT_NONE, 0, HK_NONE, HK_NONE);
    STAGE(3, 0, 1, PT_NONE, 0, HK_WO_A, HK_WO_B);
#undef STAGE
#undef STEP
#undef PT_STORE
#undef PT_NONE
#undef WO_PIECE
#undef HK_WO_A
#undef HK_WO_B
#undef HK_NONE
    PRIO(0);
    oB = mfma32(vf1, as_frag(pB1), oB);
#undef GAP
}
DI void attn_safe(const Ctx& k, AttnCtx& c, f32x16 (&o)[2], float (&mrun)[2]) {
    const lds_ptr lds = k.lds;
    attn_stage(c, 0, 0);
    o[0] = (f32x16){}; o[1] = (f32x16){};
    mrun[0] = -INFINITY; mrun[1] = -INFINITY;
    for (int sidx = 0; sidx < 4; ++sidx) {
        const int sb = at_slot(sidx & 1);
        WAIT_BAR(0);
        if (sidx < 3) attn_stage(c, sidx + 1, (sidx + 1) & 1);
        const lds_ptr vb = lds + (c.vdata ? c.vlane + sb : c.vlane);
        for (int kt = 0; kt < 8; ++kt) {
            const bf16x8 k0 = kfrag0(lds + c.kofs + sb + kt * 1024); const s16x4 k1 = kfrag1(lds + c.kt + sb + kt * 256);
            const bf16x8 v0 = as_frag(lds_r128(vb + kt * 1280)), v1 = as_frag(lds_r128(vb + kt * 1280 + 640));
            const f32x32 tl = mfma2b(k1, as_frag4(c.qt), (f32x32){});
#pragma unroll
            for (int i = 0; i < 2; ++i) {
                const f32x16 s = mfma32(k0, as_frag(c.qf0[i]), i ? hi16(tl) : lo16(tl));
                float m = s[0];
#pragma unroll
                for (int j = 1; j < 16; ++j) m = fmaxf(m, s[j]);
                m = fmaxf(swap32_max(m), mrun[i]);
                const float alpha = EX(mrun[i] - m);
                mrun[i] = m;
                unsigned pk[8];
#pragma unroll
                for (int j = 0; j < 16; ++j) o[i][j] *= alpha;
#pragma unroll
                for (int j = 0; j < 8; ++j) pk[j] = cvtpk(EX(s[2 * j] - m), EX(s[2 * j + 1] - m));
                o[i] = mfma32(v0, as_frag((u32x4){pk[0], pk[1], pk[2], pk[3]}), o[i]);
                o[i] = mfma32(v1, as_frag((u32x4){pk[4], pk[5], pk[6], pk[7]}), o[i]);
            }
        }
    }
}
DI void p2_phase(const Ctx& k, AttnCtx& c, int h, int qb, int ct3) {
    const int lane = k.lane, qg = k.w & 3, kh = k.w >> 2, r = lane & 31, hi = lane >> 5;
    const lds_ptr lds = k.lds;
    attn_first_loads(k, c, h, qb);
    PreTile pt;
    pretile_read(k, pt);
    WAIT_BAR(5);
    STAMP(6);
    f32x16 o[2];
    bool unsafe;
    attn_fast(k, c, pt, o[0], o[1], ct3, unsafe);
    STAMP(7);
    LAS unsigned* flag = (LAS unsigned*)(lds + AT_FLAG);
    if (__any(unsafe || (hi && !(o[0][8] <= 5.764607523034235e17f && o[1][8] <= 5.764607523034235e17f))) && lane == 0) *flag = 1;
    BAR_L();
    const lds_ptr red = lds + AT_RED + qg * 8192 + lane * 16;
    auto put = [&](const f32x16& v, int slot) {
#pragma unroll
        for (int u = 0; u < 3; ++u) *(LAS f32x4*)(red + slot * 4096 + u * 1024) = (f32x4){v[4 * u], v[4 * u + 1], v[4 * u + 2], v[4 * u + 3]};
    };
    auto get = [&](int slot, int u) { return *(const LAS f32x4*)(red + slot * 4096 + u * 1024); };
    f32x4 of[3];
    if (*flag == 0) {
        if (kh) put(o[0], 0); else put(o[1], 1);
        BAR_L();
#pragma unroll
        for (int u = 0; u < 3; ++u) {
            const f32x4 p = get(kh, u);
#pragma unroll
            for (int j = 0; j < 4; ++j) of[u][j] = p[j] + (kh ? o[1][4 * u + j] : o[0][4 * u + j]);
        }
    } else {
        float mref[2];
        WAIT_BAR(0);
        attn_safe(k, c, o, mref);
        BAR_L();
        if (kh) put(o[0], 0); else put(o[1], 1);
        *(LAS float*)(red + (kh ? 0 : 4096) + 3072) = kh ? mref[0] : mref[1];
        BAR_L();
        const float m0 = kh ? mref[1] : mref[0], m1 = *(const LAS float*)(red + kh * 4096 + 3072), m = fmaxf(m0, m1);
        const float a0 = EX(m0 - m), a1 = EX(m1 - m);
#pragma unroll
        for (int u = 0; u < 3; ++u) {
            const f32x4 p = get(kh, u);
#pragma unroll
            for (int j = 0; j < 4; ++j) of[u][j] = p[j] * a1 + (kh ? o[1][4 * u + j] : o[0][4 * u + j]) * a0;
        }
        BAR_L();
        if (k.w) wo_prefetch(k, ct3);
    }
    {
        auto lr = __builtin_amdgcn_permlane32_swap(__float_as_uint(of[2][0]), __float_as_uint(of[2][0]), false, false);
        const float inv = 1.0f / __uint_as_float(lr[1]);
        const int tok = qb * 256 + qg * 64 + kh * 32 + r;
        const unsigned base = WS_O + (tok >> 4) * OH_TILE + (tok & 15) * 16;
        unsigned pk[3][2];
#pragma unroll
        for (int u = 0; u < 3; ++u) { pk[u][0] = cvtpk(of[u][0] * inv, of[u][1] * inv); pk[u][1] = cvtpk(of[u][2] * inv, of[u][3] * inv); }
        auto oaddr = [&](int col) { return base + (col >> 5) * 1024 + ((col >> 3) & 3) * 256 + (col & 7) * 2; };
        if ((h & 1) == 0) {
            auto s0 = __builtin_amdgcn_permlane32_swap(pk[0][0], pk[1][0], false, false);
            auto s1 = __builtin_amdgcn_permlane32_swap(pk[0][1], pk[1][1], false, false);
            st128(k.rs, oaddr(h * 20 + 8 * hi), (u32x4){s0[0], s1[0], s0[1], s1[1]});
            if (!hi) st64(k.rs, oaddr(h * 20 + 16), (u32x2){pk[2][0], pk[2][1]});
        } else {
            auto s0 = __builtin_amdgcn_permlane32_swap(pk[0][0], pk[2][0], false, false);
            auto s1 = __builtin_amdgcn_permlane32_swap(pk[0][1], pk[2][1], false, false);
            st128(k.rs, oaddr(h * 20 + 4 + 8 * hi), hi ? (u32x4){pk[1][0], pk[1][1], s0[0], s1[0]} : (u32x4){s0[1], s1[1], pk[1][0], pk[1][1]});
            if (!hi) st64(k.rs, oaddr(h * 20), (u32x2){s0[0], s1[0]});
        }
    }
}

DI int own_cf(int kh, int u) { return kh ? (u < 2 ? 3 + u : 4) : u; }
constexpr int RED_LDS = 143360;
struct P3Pre { u32x2 xv[5]; };
DI void p3_pre(const Ctx& k, int rb64, int ct, P3Pre& p) {
    const int tt = k.w & 3, t = k.lane & 15, g = k.lane >> 4;
    if (k.w < 4) {
#pragma unroll
        for (int cf = 0; cf < 5; ++cf) p.xv[cf] = *(const u32x2*)(k.x + (size_t)(rb64 * 64 + tt * 16 + t) * D + ct * 80 + cf * 16 + 4 * g);
    }
}
DI void w13_prefetch(const Ctx& k, int f0, int f);
DI void p3_phase(const Ctx& k, int rb64, int ct, int f0, const P3Pre& pre, u32x2 (&hv)[3]) {
    const int lane = k.lane, tt = k.w & 3, kh = k.w >> 2, t = lane & 15, g = lane >> 4;
    const unsigned ap = WS_O + (rb64 * 4 + tt) * OH_TILE + kh * 10240 + lane * 16;
    const lds_ptr wl = k.lds + WO_LDS + kh * 51200 + lane * 16;
    constexpr int WIN = 5;
    u32x4 a[10];
#pragma unroll
    for (int i = 0; i < WIN; ++i) a[i] = ld128(k.rs, ap + i * 1024);
    bf16x8 wf[2][5];
#pragma unroll
    for (int cf = 0; cf < 5; ++cf) wf[0][cf] = as_frag(lds_r128(wl + cf * 1024));
    f32x4 acc[5];
#pragma unroll
    for (int cf = 0; cf < 5; ++cf) { acc[cf] = (f32x4){0, 0, 0, 0}; PIN(acc[cf]); }
    mfma_fence();
    PRIO(1);
#pragma unroll
    for (int i = 0; i < 10; ++i) {
        if (i + WIN < 10) { a[i + WIN] = ld128(k.rs, ap + (i + WIN) * 1024); SBAR(); }
#pragma unroll
        for (int cf = 0; cf < 5; ++cf) {
            mfma16_acc(acc[cf], wf[i & 1][cf], as_frag(a[i])); SBAR();
            if (i + 1 < 10) wf[(i + 1) & 1][cf] = as_frag(lds_r128(wl + (i + 1) * 5120 + cf * 1024));
            if (cf < 4) PIN(wf[i & 1][cf + 1]); else if (i + 1 < 10) PIN(wf[(i + 1) & 1][0]);
            SBAR();
        }
    }
    PRIO(0);
    mfma_fence();
    STAMP(10);
    BAR_L();
    const lds_ptr red = k.lds + RED_LDS + tt * 5120 + lane * 16;
    if (kh) {
#pragma unroll
        for (int cf = 0; cf < 5; ++cf) *(LAS f32x4*)(red + cf * 1024) = acc[cf];
        BAR_L();
        w13_prefetch(k, f0, k.w - 1);
    } else {
        BAR_L();
        const int tok = rb64 * 64 + tt * 16 + t;
        float s = 0.f, q = 0.f;
        u32x2 hq[5];
#pragma unroll
        for (int cf = 0; cf < 5; ++cf) {
            const f32x4 v = acc[cf] + *(const LAS f32x4*)(red + cf * 1024);
            hq[cf] = (u32x2){cvtpk(v[0] + bflo(pre.xv[cf].x), v[1] + bfhi(pre.xv[cf].x)), cvtpk(v[2] + bflo(pre.xv[cf].y), v[3] + bfhi(pre.xv[cf].y))};
            const int col = ct * 80 + cf * 16 + 4 * g;
            st64(k.rs, WS_H + (tok >> 4) * OH_TILE + (col >> 5) * 1024 + ((col >> 3) & 3) * 256 + t * 16 + (col & 7) * 2, hq[cf]);
            const float h0 = bflo(hq[cf].x), h1 = bfhi(hq[cf].x), h2 = bflo(hq[cf].y), h3 = bfhi(hq[cf].y);
            s += (h0 + h1) + (h2 + h3); q += (h0 * h0 + h1 * h1) + (h2 * h2 + h3 * h3);
        }
        s = sum_x32(sum_x16(s)); q = sum_x32(sum_x16(q));
        if (g == 0) st64(k.rs, WS_PST + (tok >> 4) * 2048 + (ct * 2) * 128 + t * 8, (u32x2){__float_as_uint(s), __float_as_uint(q)});
        if (g == 1) st64(k.rs, WS_PST + (tok >> 4) * 2048 + (ct * 2 + 1) * 128 + t * 8, (u32x2){0u, 0u});
        hv[0] = hq[0]; hv[1] = hq[1]; hv[2] = hq[2];
        const lds_ptr stash = k.lds + RED_LDS + tt * 5120 + lane * 8;
        *(LAS u32x2*)(stash + 3072) = hq[3]; *(LAS u32x2*)(stash + 4096) = hq[4];
    }
}
DI void pair_reduce(lds_ptr red0, int tt, int kh, int lane, const f32x4 (&acc)[5], f32x4 (&own)[3]) {
    const lds_ptr red = red0 + tt * 5120 + lane * 16;
    if (kh == 0) { *(LAS f32x4*)(red + 3072) = acc[3]; *(LAS f32x4*)(red + 4096) = acc[4]; }
    else { *(LAS f32x4*)(red) = acc[0]; *(LAS f32x4*)(red + 1024) = acc[1]; *(LAS f32x4*)(red + 2048) = acc[2]; }
    BAR_L();
    if (kh == 0) {
#pragma unroll
        for (int u = 0; u < 3; ++u) own[u] = acc[u] + *(const LAS f32x4*)(red + u * 1024);
    } else {
        own[0] = acc[3] + *(const LAS f32x4*)(red + 3072); own[1] = acc[4] + *(const LAS f32x4*)(red + 4096); own[2] = own[1];
    }
}
template <int N, int NP>
DI void w2_stage(const Ctx& k, int ct, int b, int s0) {
#pragma unroll
    for (int j = 0; j < NP; ++j) {
        int q = k.w + 8 * j;
        if (q >= 10 * N) q -= 8;
        const int hf = q >= 5 * N, rem = q - hf * 5 * N;
        glds16<true>(k.lane * 16, k.ws + WS_W2 + (ct * 54 + hf * 27 + s0) * 5120 + rem * 1024, k.lds0 + b * 71680 + hf * 35840 + rem * 1024);
    }
}
constexpr int CNT_LDS = RED_LDS + 448, DUMMY_LDS = RED_LDS + 3584;
DI void pair_signal(const Ctx& k, int cnt, unsigned flagoff) {
    LAS unsigned* p = (LAS unsigned*)(k.lds + (k.lane == 0 ? CNT_LDS + cnt * 4 : DUMMY_LDS + k.lane * 4));
    const unsigned old = __hip_atomic_fetch_add(p, 1u, __ATOMIC_RELAXED, __HIP_MEMORY_SCOPE_WORKGROUP);
    const bool last = rfl(old) == 1;
    st64(k.rs, last && k.lane == 0 ? flagoff : 0x80000000u, (u32x2){(unsigned)k.tag, (unsigned)(k.tag >> 32)});
}
DI void p5_phase(const Ctx& k, int rb64, int ct, u32x2 (&hv)[3], unsigned f4b_pub) {
    const int lane = k.lane, tt = k.w & 3, kh = k.w >> 2, t = lane & 15, g = lane >> 4;
    const unsigned ap = WS_HH + (rb64 * 4 + tt) * HH_TILE + kh * 27 * 1024 + lane * 16;
    const unsigned f4b = WS_F4B + (((rb64 >> 2) * 32 + (lane & 31)) * 4 + (rb64 & 3)) * FLAG_STRIDE;
    constexpr int WIN = 6;
    u32x4 a[27];
#pragma unroll
    for (int i = 0; i < WIN; ++i) ald128(a[i], k.rsw, ap, i * 1024);
    w2_stage<7, 9>(k, ct, 1, 7);
    if (k.w >= 4) {
        WAIT_VM(15);
        pair_signal(k, 4 + (k.w >> 1), f4b_pub);
    }
    if (kh) {
        const lds_ptr stash = k.lds + RED_LDS + tt * 5120 + lane * 8;
        hv[0] = *(const LAS u32x2*)(stash + 3072); hv[1] = *(const LAS u32x2*)(stash + 4096); hv[2] = hv[1];
    }
    lds_ptr wb[2] = {k.lds + lane * 16 + kh * 35840, k.lds + lane * 16 + kh * 35840 + 71680};
    PIN(wb[0]); PIN(wb[1]);
    auto wread = [&](int n, int cf) { return as_frag(lds_r128(wb[(n / 7) & 1] + (n % 7) * 5120 + cf * 1024)); };
    bf16x8 wf[2][5];
#pragma unroll
    for (int cf = 0; cf < 5; ++cf) wf[0][cf] = wread(0, cf);
    f32x4 acc[5];
#pragma unroll
    for (int cf = 0; cf < 5; ++cf) { acc[cf] = (f32x4){0, 0, 0, 0}; PIN(acc[cf]); }
    u32x2 fl;
    mfma_fence();
    PRIO(1);
#define P5_STEP(i, NW) do { \
        if ((i) == 6) { WAIT_BAR(6); }                                                      \
        if ((i) == 7) { BAR_L(); ald64(fl, k.rsw, f4b); w2_stage<7, 9>(k, ct, 0, 14); } \
        if ((i) == 10) {     \
            asm volatile("s_waitcnt vmcnt(12)" : "+v"(fl) :: "memory"); \
            if (!__all(fl.x == (unsigned)k.tag && fl.y == (unsigned)(k.tag >> 32))) poll_flags(k, f4b); \
        } \
        if ((i) == 13) { WAIT_BAR(6); w2_stage<6, 8>(k, ct, 1, 21); } \
        if ((i) == 20) { WAIT_BAR(6); } \
        if ((i) + WIN < 27) ald128(a[(i) + WIN], k.rsw, ap, ((i) + WIN) * 1024); \
        ald_wait<NW>(a[i]); SBAR(); \
        _Pragma("unroll") for (int cf = 0; cf < 5; ++cf) { \
            mfma16_acc(acc[cf], wf[(i) & 1][cf], as_frag(a[i])); SBAR(); \
            if ((i) + 1 < 27) wf[((i) + 1) & 1][cf] = wread((i) + 1, cf); \
            if (cf < 4) PIN(wf[(i) & 1][cf + 1]); else if ((i) + 1 < 27) PIN(wf[((i) + 1) & 1][0]); \
            SBAR(); } } while (0)
    P5_STEP(0, 15); P5_STEP(1, 15); P5_STEP(2, 15); P5_STEP(3, 15); P5_STEP(4, 15); P5_STEP(5, 15); P5_STEP(6, 6);
    P5_STEP(7, 15); P5_STEP(8, 15); P5_STEP(9, 15); P5_STEP(10, 15); P5_STEP(11, 15); P5_STEP(12, 15);
    P5_STEP(13, 14); P5_STEP(14, 14); P5_STEP(15, 14); P5_STEP(16, 14); P5_STEP(17, 14); P5_STEP(18, 14); P5_STEP(19, 6);
    P5_STEP(20, 6); P5_STEP(21, 5); P5_STEP(22, 4); P5_STEP(23, 3); P5_STEP(24, 2); P5_STEP(25, 1); P5_STEP(26, 0);
#undef P5_STEP
    PRIO(0);
    mfma_fence();
    STAMP(16);
    f32x4 own[3];
    pair_reduce(k.lds + RED_LDS, tt, kh, lane, acc, own);
    const int tok = rb64 * 64 + tt * 16 + t;
#pragma unroll
    for (int u = 0; u < 3; ++u) {
        if (u < 2 || kh == 0) {
            const int col = ct * 80 + own_cf(kh, u) * 16 + 4 * g;
            *(u32x2*)(k.out + (size_t)tok * D + col) = (u32x2){cvtpk(own[u][0] + bflo(hv[u].x), own[u][1] + bfhi(hv[u].x)), cvtpk(own[u][2] + bflo(hv[u].y), own[u][3] + bfhi(hv[u].y))};
        }
    }
}

constexpr int P4_CS = 143360;
DI void w13_prefetch(const Ctx& k, int f0, int f) {
    const int p0 = f * 20;
#pragma unroll 4
    for (int j = 0; j < 20; ++j) glds16<true>(k.lane * 16, k.ws + WS_W13 + f0 * 20480 + (p0 + j) * 1024, k.lds0 + (p0 + j) * 1024);
}
DI void p4_colsum(const Ctx& k, int f) {
    const int t = k.lane & 15, g = k.lane >> 4;
    u32x4 wv[20];
#pragma unroll
    for (int s = 0; s < 20; ++s) wv[s] = lds_r128(k.lds + (f * 20 + s) * 1024 + k.lane * 16);
    f32x4 c0 = {0, 0, 0, 0}, c1 = {0, 0, 0, 0};
    const bf16x8 ones = {0x3F80, 0x3F80, 0x3F80, 0x3F80, 0x3F80, 0x3F80, 0x3F80, 0x3F80};
#pragma unroll
    for (int s = 0; s < 20; s += 2) { c0 = mfma16(as_frag(wv[s]), ones, c0); c1 = mfma16(as_frag(wv[s + 1]), ones, c1); }
    if (t == 0) *(LAS f32x4*)(k.lds + P4_CS + f * 64 + g * 16) = c0 + c1;
}
template <int NF>
DI void p4_swiglu(const Ctx& k, const f32x4 (&acc)[2][NF], int fbase, const float (&mean)[2], const float (&rstd)[2], const float (&vpe)[2], unsigned (&d)[2][4]) {
    const int g = k.lane >> 4;
#pragma unroll
    for (int f = 0; f < NF; ++f) {
        const f32x4 cs = *(const LAS f32x4*)(k.lds + P4_CS + (fbase + f) * 64 + g * 16);
#pragma unroll
        for (int i = 0; i < 2; ++i) {
            const float ce = -LOG2E * rstd[i], nm = -mean[i];
            const f32x2 ce2 = {ce, ce}, ir22 = {vpe[i], vpe[i]}, nm2 = {nm, nm};
            const f32x2 ga = __builtin_elementwise_fma(nm2, __builtin_shufflevector(cs, cs, 0, 1), __builtin_shufflevector(acc[i][f], acc[i][f], 0, 1));
            const f32x2 ua = __builtin_elementwise_fma(nm2, __builtin_shufflevector(cs, cs, 2, 3), __builtin_shufflevector(acc[i][f], acc[i][f], 2, 3));
            const f32x2 t2 = ga * ce2;
            const f32x2 e2 = {EX(t2.x), EX(t2.y)};
            const f32x2 d2 = __builtin_elementwise_fma(e2, ir22, ir22);
            const f32x2 r2 = {__builtin_amdgcn_rcpf(d2.x), __builtin_amdgcn_rcpf(d2.y)};
            const f32x2 o2 = (ga * ua) * r2;
            d[i][f] = cvtpk(o2.x, o2.y);
        }
    }
}
DI u32x4 p4_transpose(const unsigned (&d)[4]) {
    auto a = __builtin_amdgcn_permlane32_swap(d[0], d[2], false, false);
    auto b = __builtin_amdgcn_permlane32_swap(d[1], d[3], false, false);
    auto e = __builtin_amdgcn_permlane16_swap(a[0], b[0], false, false);
    auto f = __builtin_amdgcn_permlane16_swap(a[1], b[1], false, false);
    return (u32x4){e[0], e[1], f[0], f[1]};
}
DI void p4_phase(const Ctx& k, int rb, int cb, int ct5) {
    const int lane = k.lane, w = k.w, t = lane & 15, g = lane >> 4;
    const lds_ptr lds = k.lds;
    const unsigned ap = WS_H + (rb * 16 + w * 2) * OH_TILE + lane * 16;
    u32x4 a0[20], a1[20];
#define P4_LD(s) do { a0[s] = ld128(k.rs, ap + (s) * 1024); a1[s] = ld128(k.rs, ap + OH_TILE + (s) * 1024); } while (0)
    u32x2 pv[2][4];
#pragma unroll
    for (int i = 0; i < 2; ++i)
#pragma unroll
        for (int j = 0; j < 4; ++j) pv[i][j] = ld64(k.rs, WS_PST + (rb * 16 + w * 2 + i) * 2048 + (4 * g + j) * 128 + t * 8);
    P4_LD(0); P4_LD(1); P4_LD(2); P4_LD(3);
    lds_ptr wb[3] = {lds + lane * 16, lds + lane * 16 + 61440, lds + lane * 16 + 122880};
    PIN(wb[1]); PIN(wb[2]);
    auto wread = [&](int f, int s) { return as_frag(lds_r128(wb[f / 3] + (f % 3) * 20480 + s * 1024)); };
    bf16x8 wf[2][4];
#pragma unroll
    for (int f = 0; f < 4; ++f) wf[0][f] = wread(f, 0);
    f32x4 acc[2][4];
#pragma unroll
    for (int f = 0; f < 4; ++f) { acc[0][f] = (f32x4){0, 0, 0, 0}; acc[1][f] = (f32x4){0, 0, 0, 0}; PIN(acc[0][f]); PIN(acc[1][f]); }
    float mean[2], rstd[2], vpe[2];
    mfma_fence();
    STAMP(13);
    PRIO(1);
#pragma unroll
    for (int s = 0; s < 20; ++s) {
        if (s + 4 < 20) P4_LD(s + 4);
        SBAR();
        if (s == 4) {
#pragma unroll
            for (int i = 0; i < 2; ++i) {
                float sm = (__uint_as_float(pv[i][0].x) + __uint_as_float(pv[i][1].x)) + (__uint_as_float(pv[i][2].x) + __uint_as_float(pv[i][3].x));
                float q = (__uint_as_float(pv[i][0].y) + __uint_as_float(pv[i][1].y)) + (__uint_as_float(pv[i][2].y) + __uint_as_float(pv[i][3].y));
                sm = sum_x32(sum_x16(sm)); q = sum_x32(sum_x16(q));
                mean[i] = sm * (1.0f / D);
                vpe[i] = fmaxf(q * (1.0f / D) - mean[i] * mean[i], 0.f) + LN_EPS;
                rstd[i] = __builtin_amdgcn_rsqf(vpe[i]);
            }
            PIN(mean[0]); PIN(mean[1]); PIN(rstd[0]); PIN(rstd[1]); PIN(vpe[0]); PIN(vpe[1]);
            SBAR();
        }
#pragma unroll
        for (int f = 0; f < 4; ++f) {
            mfma16_acc(acc[0][f], wf[s & 1][f], as_frag(a0[s])); SBAR();
            if (s + 1 < 20) wf[(s + 1) & 1][f] = wread(f, s + 1); else if (f < 3) wf[0][f] = wread(4 + f, 0);
            if (f < 3) PIN(wf[s & 1][f + 1]); else if (s + 1 < 20) PIN(wf[(s + 1) & 1][0]);
            SBAR();
            mfma16_acc(acc[1][f], wf[s & 1][f], as_frag(a1[s])); SBAR();
        }
    }
#undef P4_LD
    PRIO(0);
    mfma_fence();
    STAMP(33);
    const unsigned hhb = WS_HH + (rb * 16 + w * 2) * HH_TILE + lane * 16;
    auto epilogue1 = [&]() {
        unsigned d[2][4];
        p4_swiglu<4>(k, acc, 0, mean, rstd, vpe, d);
#pragma unroll
        for (int i = 0; i < 2; ++i) st128(k.rs, hhb + i * HH_TILE + ((cb >> 4) * 27 + (cb & 15)) * 1024, p4_transpose(d[i]));
    };
    {
        LAS unsigned* p = (LAS unsigned*)(lds + (lane == 0 ? CNT_LDS + 32 : DUMMY_LDS + lane * 4));
        (void)__hip_atomic_fetch_add(p, 1u, __ATOMIC_RELAXED, __HIP_MEMORY_SCOPE_WORKGROUP);
    }
    epilogue1();
    SBAR();
    unsigned touched;
    f32x4 acc2[2][3];
#pragma unroll
    for (int f = 0; f < 3; ++f) { acc2[0][f] = (f32x4){0, 0, 0, 0}; acc2[1][f] = (f32x4){0, 0, 0, 0}; PIN(acc2[0][f]); PIN(acc2[1][f]); }
    mfma_fence();
    PRIO(1);
#pragma unroll
    for (int s = 0; s < 20; ++s) {
        if (s == 17) { WAIT_VM(1); pair_signal(k, w >> 1, WS_F4A + ((rb * 32 + cb) * 4 + (w >> 1)) * FLAG_STRIDE); SBAR(); }
        if (s == 2) {
            touch_lines(touched, k.rsw, WS_W2 + ct5 * 276480 + ((k.lb >> 3) * 540 + w * 64 + lane) * 128);
            SBAR();
        }
#pragma unroll
        for (int f = 0; f < 3; ++f) {
            mfma16_acc(acc2[0][f], wf[s & 1][f], as_frag(a0[s])); SBAR();
            if (s + 1 < 20) wf[(s + 1) & 1][f] = wread(4 + f, s + 1);
            if (f < 2) PIN(wf[s & 1][f + 1]); else if (s + 1 < 20) PIN(wf[(s + 1) & 1][0]);
            SBAR();
            mfma16_acc(acc2[1][f], wf[s & 1][f], as_frag(a1[s])); SBAR();
        }
    }
    PRIO(0);
    mfma_fence();
    asm volatile("s_waitcnt vmcnt(0)" :: "v"(touched) : "memory");
    STAMP(14);
    {
        unsigned d[2][4];
        p4_swiglu<3>(k, acc2, 4, mean, rstd, vpe, *(unsigned (*)[2][4])&d);
        d[0][3] = 0; d[1][3] = 0;
        const int n2 = cb < 24 ? 3 : 2, l = (cb < 24 ? 3 * cb : 72 + 2 * (cb - 24)) + g, ls = l >> 2;
        const unsigned pos = ((ls >= 11 ? 32 + ls : 16 + ls) * 4 + (l & 3)) * 256 + t * 16;
#pragma unroll
        for (int i = 0; i < 2; ++i) st128(k.rs, g < n2 ? WS_HH + (rb * 16 + w * 2 + i) * HH_TILE + pos : 0x80000000u, p4_transpose(d[i]));
    }
    if (w < 4) {
        for (unsigned spins = 0; spins < SPIN_CAP; ++spins) {
            if (rfl(*(volatile LAS unsigned*)(lds + CNT_LDS + 32)) == 8) break;
            __builtin_amdgcn_s_sleep(1);
        }
#pragma unroll 6
        for (int j = 0; j < 18; ++j) {
            int q = w + 4 * j;
            if (q >= 70) q -= 4;
            const int hf = q >= 35, rem = q - hf * 35;
            glds16<true>(lane * 16, k.ws + WS_W2 + (ct5 * 54 + hf * 27) * 5120 + rem * 1024, k.lds0 + hf * 35840 + rem * 1024);
        }
        WAIT_VM(18);
        pair_signal(k, 4 + (w >> 1), WS_F4B + ((rb * 32 + cb) * 4 + (w >> 1)) * FLAG_STRIDE);
        WAIT_VM(0);
    }
}

constexpr int LDS_BYTES = 163840;
__global__ __launch_bounds__(512, 2) void fused_block(const uint16_t* __restrict__ x, const uint16_t* __restrict__ wqkv, const uint16_t* __restrict__ wo,
                                                      const uint16_t* __restrict__ w1, const uint16_t* __restrict__ w3, const uint16_t* __restrict__ w2,
                                                      char* __restrict__ ws, uint16_t* __restrict__ out) {
    extern __shared__ __attribute__((aligned(16))) char smem[];
    Ctx k;
    k.x = x; k.wqkv = wqkv; k.wo = wo; k.w1 = w1; k.w3 = w3; k.w2 = w2; k.ws = ws; k.out = out;
    k.rs = __builtin_amdgcn_make_buffer_rsrc(ws, 0, WS_END + 20480, 0x00020000);
    k.lds = (lds_ptr)smem; k.lds0 = (unsigned)(uintptr_t)smem;
    k.c = blockIdx.x; k.xcd = k.c & 7; k.lb = k.c >> 3;
    k.tid = threadIdx.x; k.lane = k.tid & 63; k.w = rfl(k.tid >> 6);
    k.rsw = (u32x4){(unsigned)(uintptr_t)ws, (unsigned)((uintptr_t)ws >> 32) & 0xffffu, WS_END + 20480, 0x00020000u};
    k.tag = 0;
    k.sc = 0; k.sr = 0;
    STAMP(0);
    startup_load(k);
    const int rb1 = (k.xcd >> 2) * 8 + (k.lb & 7), cb1 = (k.xcd & 3) * 4 + (k.lb >> 3);
    if (cb1 < 15) {
        if (cb1 >= 10) p1_body<true>(k, rb1, cb1); else p1_body<false>(k, rb1, cb1);
        STAMP(3);
        WAIT_BAR(0);
        if (k.tid == 0) st_flag(ws + WS_F1 + (rb1 * 16 + cb1) * FLAG_STRIDE, k.tag);
    } else {
        WAIT_VM(0);
        startup_tag(k);
        ln1_stats(k, rb1);
        if (rb1 < 15) colsum_qkv(k, rb1);
        WAIT_BAR(0);
    }
    STAMP(4);
    if (k.w) pretile_load(k);
    const int h = k.xcd * 4 + (k.lb & 3), qb = k.lb >> 2;
    const int rb64 = k.xcd * 4 + (k.lb >> 3), ct = k.lb & 7;
    const int rb4 = (k.xcd >> 1) * 2 + (k.lb >> 4), cb4 = (k.xcd & 1) * 16 + (k.lb & 15);
    const int f0 = p4_f0(cb4);
    AttnCtx ac;
    attn_setup(k, ac, h);
    if (k.w == 0) {
        const int c0 = (20 * h) >> 7, c1 = (20 * h + 19) >> 7, j = k.lane & 15, sel = k.lane >> 4;
        const int cbk = (sel & 1) ? c1 : c0;
        const unsigned o0 = WS_F1 + (j * 16 + (sel < 2 ? 5 : 10) + cbk) * FLAG_STRIDE;
        const unsigned o1 = WS_F1 + ((2 * qb + (k.lane & 1)) * 16 + ((k.lane & 2) ? c1 : c0)) * FLAG_STRIDE;
        poll_flags2(k, o0, o1);
    }
    WAIT_BAR(0);
    STAMP(5);
    p2_phase(k, ac, h, qb, ct);
    STAMP(8);
    WAIT_BAR(0);
    if (k.tid == 0) st_flag(ws + WS_F2 + (qb * 32 + h) * FLAG_STRIDE, k.tag);
    P3Pre pre;
    p3_pre(k, rb64, ct, pre);
    if (k.w == 0) poll_flags(k, WS_F2 + (k.xcd * 32 + (k.lane & 31)) * FLAG_STRIDE);
    else if (k.w <= 3) w13_prefetch(k, f0, k.w - 1);
    BAR_L();
    if (k.c == 0 && k.tid == 0) st_flag(ws + WS_EPOCH, k.tag);
    STAMP(9);
    u32x2 hv[3];
    p3_phase(k, rb64, ct, f0, pre, hv);
    STAMP(11);
    if (k.w < 4) WAIT_VM(0);
    BAR_L();
    if (k.tid == 0) st_flag(ws + WS_F3 + (k.xcd * 32 + k.lb) * FLAG_STRIDE, k.tag);
    if (k.w == 0) {
        if (k.lane < 9) *(LAS unsigned*)(k.lds + CNT_LDS + k.lane * 4) = 0;
        poll_flags(k, WS_F3 + (rb4 * 32 + (k.lane & 31)) * FLAG_STRIDE);
    } else {
        WAIT_VM(0);
        p4_colsum(k, k.w - 1);
    }
    WAIT_BAR(0);
    STAMP(12);
    p4_phase(k, rb4, cb4, ct);
    STAMP(15);
    if (k.w == 0) poll_flags(k, WS_F4A + (((rb64 >> 2) * 32 + (k.lane & 31)) * 4 + (rb64 & 3)) * FLAG_STRIDE);
    BAR_L();
    STAMP(17);
    p5_phase(k, rb64, ct, hv, WS_F4B + ((rb4 * 32 + cb4) * 4 + (k.w >> 1)) * FLAG_STRIDE);
    STAMP(18);
    STAMP_FLUSH();
}

extern "C" void kernel_launch(void* const* d_in, const int* in_sizes, int n_in, void* d_out, int out_size, void* d_ws, size_t ws_size, hipStream_t stream) {
    (void)hipFuncSetAttribute((const void*)fused_block, hipFuncAttributeMaxDynamicSharedMemorySize, LDS_BYTES);
    fused_block<<<256, 512, LDS_BYTES, stream>>>((const uint16_t*)d_in[0], (const uint16_t*)d_in[1], (const uint16_t*)d_in[2], (const uint16_t*)d_in[4],
                                                 (const uint16_t*)d_in[5], (const uint16_t*)d_in[6], (char*)d_ws, (uint16_t*)d_out);
}
```

```cpp
#include <hip/hip_runtime.h>
#include <hip/hip_bf16.h>
#include <stdint.h>

#define DI __device__ __forceinline__
#define LAS __attribute__((address_space(3)))
#define GAS __attribute__((address_space(1)))
typedef short bf16x8 __attribute__((ext_vector_type(8)));
typedef short s16x4 __attribute__((ext_vector_type(4)));
typedef float f32x2 __attribute__((ext_vector_type(2)));
typedef float f32x4 __attribute__((ext_vector_type(4)));
typedef float f32x16 __attribute__((ext_vector_type(16)));
typedef unsigned u32x2 __attribute__((ext_vector_type(2)));
typedef unsigned u32x4 __attribute__((ext_vector_type(4)));
typedef unsigned long long u64;
typedef __bf16 bf16x2_t __attribute__((ext_vector_type(2)));
typedef short v4i16_t __attribute__((ext_vector_type(4)));
typedef LAS char* lds_ptr;
typedef __amdgpu_buffer_rsrc_t rsrc_t;

constexpr int S = 2048, D = 640, F = 1728, NQKV = 3 * D;
constexpr float LN_EPS = 1e-5f;
constexpr float LOG2E = 1.4426950408889634f;
constexpr float QSCALE = 0.22360679774997896f * LOG2E;

constexpr unsigned WS_EPOCH = 0;
constexpr unsigned FLAG_STRIDE = 64, FLAG_ARR = 256 * FLAG_STRIDE;
constexpr unsigned WS_F1 = 4096, WS_FP = WS_F1 + FLAG_ARR, WS_F2 = WS_FP + FLAG_ARR, WS_F3 = WS_F2 + FLAG_ARR;
constexpr unsigned WS_F4A = WS_F3 + FLAG_ARR, WS_F4B = WS_F4A + 4 * FLAG_ARR;
constexpr unsigned WS_ST1 = WS_F4B + 4 * FLAG_ARR;
constexpr unsigned WS_CSQ = WS_ST1 + 32768;
static_assert(WS_CSQ + 1920 * 8 <= 262144, "control region");
constexpr unsigned WS_PST = 262144;
constexpr unsigned WS_Q = 524288;
constexpr unsigned IMG = S * D * 2;
constexpr unsigned QK_TILE = 32 * 1280, QK_IMG = 64 * QK_TILE;
constexpr unsigned WS_K = WS_Q + QK_IMG;
constexpr unsigned WS_V = WS_K + QK_IMG;
constexpr unsigned OH_TILE = 21504, HH_TILE = 56320;
constexpr unsigned WS_O = WS_V + IMG;
constexpr unsigned WS_H = WS_O + 128 * OH_TILE;
constexpr unsigned WS_HH = WS_H + 128 * OH_TILE;
constexpr unsigned WS_WO = WS_HH + 128 * HH_TILE;
constexpr unsigned WS_W13 = WS_WO + D * D * 2;
constexpr unsigned WS_W2 = WS_W13 + D * F * 4;
constexpr unsigned WS_END = WS_W2 + F * D * 2;

DI unsigned cvtpk(float lo, float hi) { f32x2 v = {lo, hi}; bf16x2_t b = __builtin_convertvector(v, bf16x2_t); return __builtin_bit_cast(unsigned, b); }
DI float bflo(unsigned u) { return __uint_as_float(u << 16); }
DI float bfhi(unsigned u) { return __uint_as_float(u & 0xffff0000u); }
DI f32x4 mfma16(bf16x8 a, bf16x8 b, f32x4 c) { return __builtin_amdgcn_mfma_f32_16x16x32_bf16(a, b, c, 0, 0, 0); }
DI void mfma16_acc(f32x4& c, bf16x8 a, bf16x8 b) { asm volatile("v_mfma_f32_16x16x32_bf16 %0, %1, %2, %0" : "+v"(c) : "v"(a), "v"(b)); }
DI void mfma_fence() { __builtin_amdgcn_sched_barrier(0); asm volatile("s_nop 7\n\ts_nop 1" ::: "memory"); __builtin_amdgcn_sched_barrier(0); }
DI f32x16 mfma32(bf16x8 a, bf16x8 b, f32x16 c) { return __builtin_amdgcn_mfma_f32_32x32x16_bf16(a, b, c, 0, 0, 0); }
DI int rfl(int x) { return __builtin_amdgcn_readfirstlane(x); }
template <bool SC1>
DI void glds16(unsigned voff, const void* sbase, unsigned ldsdst) {
    unsigned keep;
    if (SC1) asm volatile("s_mov_b32 %0, m0\n\ts_mov_b32 m0, %3\n\ts_nop 0\n\tglobal_load_lds_dwordx4 %1, %2 sc1\n\ts_mov_b32 m0, %0" : "=&s"(keep) : "v"(voff), "s"(sbase), "s"(ldsdst) : "memory");
    else asm volatile("s_mov_b32 %0, m0\n\ts_mov_b32 m0, %3\n\ts_nop 0\n\tglobal_load_lds_dwordx4 %1, %2\n\ts_mov_b32 m0, %0" : "=&s"(keep) : "v"(voff), "s"(sbase), "s"(ldsdst) : "memory");
}
DI void glds4(const void* gptr, unsigned ldsdst) {
    unsigned keep;
    asm volatile("s_mov_b32 %0, m0\n\ts_mov_b32 m0, %2\n\ts_nop 0\n\tglobal_load_lds_dword %1, off\n\ts_mov_b32 m0, %0" : "=&s"(keep) : "v"(gptr), "s"(ldsdst) : "memory");
}
#define WAIT_VM(n) asm volatile("s_waitcnt vmcnt(" #n ")" ::: "memory")
#define WAIT_BAR(n) asm volatile("s_waitcnt vmcnt(" #n ") lgkmcnt(0)\n\ts_barrier" ::: "memory")
#define BAR_L() asm volatile("s_waitcnt lgkmcnt(0)\n\ts_barrier" ::: "memory")
#define SBAR() __builtin_amdgcn_sched_barrier(0)
#define PIN(x) asm volatile("" : "+v"(x))
#define PRIO(n) __builtin_amdgcn_s_setprio(n)
#define EX(v) __builtin_amdgcn_exp2f(v)
DI u32x4 lds_r128(lds_ptr p) { return *(const LAS u32x4*)p; }
DI s16x4 lds_tr(lds_ptr p) { return __builtin_bit_cast(s16x4, __builtin_amdgcn_ds_read_tr16_b64_v4i16((LAS v4i16_t*)p)); }
DI bf16x8 as_frag(u32x4 v) { return __builtin_bit_cast(bf16x8, v); }
DI bf16x8 cat_frag(s16x4 a, s16x4 b) { return (bf16x8){a[0], a[1], a[2], a[3], b[0], b[1], b[2], b[3]}; }
DI float swap32_max(float m) { auto r = __builtin_amdgcn_permlane32_swap(__float_as_uint(m), __float_as_uint(m), false, false); return fmaxf(__uint_as_float(r[0]), __uint_as_float(r[1])); }
DI float sum_x16(float v) { auto r = __builtin_amdgcn_permlane16_swap(__float_as_uint(v), __float_as_uint(v), false, false); return __uint_as_float(r[0]) + __uint_as_float(r[1]); }
DI float sum_x32(float v) { auto r = __builtin_amdgcn_permlane32_swap(__float_as_uint(v), __float_as_uint(v), false, false); return __uint_as_float(r[0]) + __uint_as_float(r[1]); }
DI u32x4 ld128(rsrc_t r, unsigned off) { return __builtin_amdgcn_raw_buffer_load_b128(r, off, 0, 16); }
DI u32x2 ld64(rsrc_t r, unsigned off) { return __builtin_amdgcn_raw_buffer_load_b64(r, off, 0, 16); }
DI void st128(rsrc_t r, unsigned off, u32x4 v) { __builtin_amdgcn_raw_buffer_store_b128(v, r, off, 0, 16); }
DI void st64(rsrc_t r, unsigned off, u32x2 v) { __builtin_amdgcn_raw_buffer_store_b64(v, r, off, 0, 16); }

DI void ald128(u32x4& v, u32x4 rsw, unsigned voff, unsigned soff) { asm volatile("buffer_load_dwordx4 %0, %1, %2, %3 offen sc1" : "=&v"(v) : "v"(voff), "s"(rsw), "s"(soff) : "memory"); }
template <int N> DI void ald_wait(u32x4& v) { asm volatile("s_waitcnt vmcnt(%1)" : "+v"(v) : "i"(N) : "memory"); }
DI void ald64(u32x2& v, u32x4 rsw, unsigned voff) { asm volatile("buffer_load_dwordx2 %0, %1, %2, 0 offen sc1" : "=&v"(v) : "v"(voff), "s"(rsw) : "memory"); }
DI void touch_lines(unsigned& dummy, u32x4 rsw, unsigned voff) { asm volatile("buffer_load_dword %0, %1, %2, 0 offen sc1" : "=&v"(dummy) : "v"(voff), "s"(rsw) : "memory"); }
struct Ctx {
    const uint16_t *x, *wqkv, *wo, *w1, *w3, *w2;
    char* ws; uint16_t* out;
    rsrc_t rs; u32x4 rsw; u64 tag; lds_ptr lds; unsigned lds0;
    int c, xcd, lb, tid, lane, w;
};

DI u64 ld_flag(const char* p) { return __hip_atomic_load((const GAS u64*)p, __ATOMIC_RELAXED, __HIP_MEMORY_SCOPE_AGENT); }
DI void st_flag(char* p, u64 v) { __hip_atomic_store((GAS u64*)p, v, __ATOMIC_RELAXED, __HIP_MEMORY_SCOPE_AGENT); }
constexpr unsigned SPIN_CAP = 1u << 18;
DI void poll_flags(const Ctx& k, unsigned off) {
    u64 a = ld_flag(k.ws + off);
    __builtin_amdgcn_s_sleep(8);
    for (unsigned spins = 0; spins < SPIN_CAP; ++spins) {
        const u64 b = ld_flag(k.ws + off);
        if (__all(a == k.tag)) break;
        a = ld_flag(k.ws + off);
        if (__all(b == k.tag)) break;
    }
}
DI void poll_flags2(const Ctx& k, unsigned off0, unsigned off1) {
    u64 a0 = ld_flag(k.ws + off0), a1 = ld_flag(k.ws + off1);
    __builtin_amdgcn_s_sleep(8);
    for (unsigned spins = 0; spins < SPIN_CAP; ++spins) {
        const u64 b0 = ld_flag(k.ws + off0), b1 = ld_flag(k.ws + off1);
        if (__all(a0 == k.tag && a1 == k.tag)) break;
        a0 = ld_flag(k.ws + off0); a1 = ld_flag(k.ws + off1);
        if (__all(b0 == k.tag && b1 == k.tag)) break;
    }
}
DI void poll_all(const Ctx& k, unsigned arr) {
    for (unsigned spins = 0; spins < SPIN_CAP; ++spins) {
        const unsigned o = arr + k.lane * FLAG_STRIDE;
        const u64 a = ld_flag(k.ws + o), b = ld_flag(k.ws + o + 64 * FLAG_STRIDE), c = ld_flag(k.ws + o + 128 * FLAG_STRIDE), d = ld_flag(k.ws + o + 192 * FLAG_STRIDE);
        if (__all(a == k.tag && b == k.tag && c == k.tag && d == k.tag)) break;
        __builtin_amdgcn_s_sleep(1);
    }
}

constexpr int SU_LDS = 131072;
DI void startup_load(const Ctx& k) { glds4(k.ws + WS_EPOCH + (k.lane & 1) * 4, k.lds0 + SU_LDS + k.w * 256); }
DI void startup_tag(Ctx& k) {
    const u32x2 e = *(const LAS u32x2*)(k.lds + SU_LDS + k.w * 256);
    k.tag = (((u64)(unsigned)rfl(e.y) << 32) | (unsigned)rfl(e.x)) + 1;
}
DI void ln1_stats(const Ctx& k, int j) {
    u32x4 v[2][10];
#pragma unroll
    for (int p = 0; p < 2; ++p)
#pragma unroll
        for (int i = 0; i < 10; ++i) v[p][i] = *(const u32x4*)(k.x + (size_t)(128 * j + 16 * k.w + 8 * p + (k.lane >> 3)) * D + ((k.lane & 7) + 8 * i) * 8);
#pragma unroll
    for (int p = 0; p < 2; ++p) {
        float s = 0.f, q = 0.f;
#pragma unroll
        for (int i = 0; i < 10; ++i)
#pragma unroll
            for (int e = 0; e < 4; ++e) { const float a = bflo(v[p][i][e]), b = bfhi(v[p][i][e]); s += a + b; q = fmaf(a, a, fmaf(b, b, q)); }
#pragma unroll
        for (int o = 4; o >= 1; o >>= 1) { s += __shfl_xor(s, o); q += __shfl_xor(q, o); }
        const float mean = s * (1.0f / D), var = fmaxf(q * (1.0f / D) - mean * mean, 0.f);
        const float val = (k.lane & 1) ? 1.0f / sqrtf(var + LN_EPS) : mean;
        const int row = 128 * j + 16 * k.w + 8 * p + (k.lane >> 3);
        if ((k.lane & 7) < 2) st64(k.rs, WS_ST1 + row * 16 + (k.lane & 1) * 8, (u32x2){__float_as_uint(val), (unsigned)k.tag});
    }
}
DI void colsum_qkv(const Ctx& k, int cb) {
    float a[8] = {0, 0, 0, 0, 0, 0, 0, 0};
    u32x4 v[20];
#pragma unroll
    for (int i = 0; i < 20; ++i) v[i] = *(const u32x4*)(k.wqkv + (size_t)(k.w * 80 + i * 4 + (k.lane >> 4)) * NQKV + cb * 128 + (k.lane & 15) * 8);
#pragma unroll
    for (int i = 0; i < 20; ++i)
#pragma unroll
        for (int j = 0; j < 4; ++j) { a[2 * j] += bflo(v[i][j]); a[2 * j + 1] += bfhi(v[i][j]); }
    LAS float* red = (LAS float*)k.lds;
#pragma unroll
    for (int j = 0; j < 8; ++j) { a[j] += __shfl_xor(a[j], 16); a[j] += __shfl_xor(a[j], 32); }
    if (k.lane < 16) {
#pragma unroll
        for (int j = 0; j < 8; ++j) red[k.w * 128 + k.lane * 8 + j] = a[j];
    }
    BAR_L();
    if (k.tid < 128) {
        float t = 0.f;
#pragma unroll
        for (int i = 0; i < 8; ++i) t += red[i * 128 + k.tid];
        st64(k.rs, WS_CSQ + (cb * 128 + k.tid) * 8, (u32x2){__float_as_uint(t), (unsigned)k.tag});
    }
}

DI int p4_f0(int cb) { return cb < 24 ? cb * 7 : 168 + (cb - 24) * 6; }
DI int hh_f8(int sigma, int gp) {
    const int kh = sigma >= 27, i = sigma - 27 * kh;
    if (i < 16) return p4_f0(16 * kh + i) + gp;
    const int l = 4 * (11 * kh + i - 16) + gp, cb = l < 72 ? l / 3 : 24 + (l - 72) / 2;
    return p4_f0(cb) + 4 + (l < 72 ? l % 3 : (l - 72) % 2);
}
constexpr int PT_LDS = 131072;
DI int pt_off(int i) { return PT_LDS + (i < 3 ? i * 8192 : 24576 + (i - 3) * 4096); }
DI void pretile_load(const Ctx& k) {
    const int wv = k.w - 1;
#pragma unroll
    for (int i = 0; i < 5; ++i) {
        const int u = k.c + 256 * i;
        if (u < 540) {
            const int s = u % 20, fb = u / 20;
            const uint16_t* src = ((k.lane & 1) ? k.w3 : k.w1) + fb * 64 + (k.lane >> 1) * 2 + (size_t)32 * s * F;
            for (int r = wv; r < 32; r += 7) glds4(src + (size_t)r * F, k.lds0 + pt_off(i) + r * 256);
        } else {
            const int pc = (i & 1) ? wv - 3 : wv;
            if (pc >= 0 && pc < 4) {
                const bool is2 = u < 1080;
                const int uu = is2 ? u - 540 : u - 1080, s = uu / 10, nb = uu % 10;
                const int r = (is2 ? 8 * hh_f8(s, pc) : 32 * s + 8 * pc) + (k.lane >> 3);
                glds16<false>(r * (D * 2) + nb * 128 + (k.lane & 7) * 16, is2 ? k.w2 : k.wo, k.lds0 + pt_off(i) + pc * 1024);
            }
        }
    }
}
struct PreTile { u32x4 v[5]; };
DI void pretile_read(const Ctx& k, PreTile& p) {
    const int g = k.lane >> 4, q = (k.lane >> 2) & 3, pp = k.lane & 3;
#pragma unroll
    for (int i = 0; i < 5; ++i) {
        const int u = k.c + 256 * i;
        const bool wide = u < 540;
        const int rs = wide ? 256 : 128, f = wide ? k.w : (k.w & 3);
        const lds_ptr a = k.lds + pt_off(i) + (8 * g + q) * rs + f * 32 + pp * 8;
        p.v[i] = __builtin_bit_cast(u32x4, cat_frag(lds_tr(a), lds_tr(a + 4 * rs)));
    }
}
DI void pretile_store(const Ctx& k, const PreTile& p, int i) {
    const int u = k.c + 256 * i;
    unsigned dst;
    if (u < 540) dst = WS_W13 + (((u / 20) * 8 + k.w) * 20 + u % 20) * 1024;
    else {
        const bool is2 = u < 1080;
        const int uu = is2 ? u - 540 : u - 1080, s = uu / 10, cfg = (uu % 10) * 4 + (k.w & 3), ct = cfg / 5, cf = cfg % 5;
        dst = is2 ? WS_W2 + ((ct * 54 + s) * 5 + cf) * 1024 : WS_WO + ((ct * 20 + s) * 5 + cf) * 1024;
    }
    st128(k.rs, dst + k.lane * 16, p.v[i]);
}

constexpr int P1_SLOT = 32768;
struct P1Frag { bf16x8 a[4], b[2]; };
template <bool ISV> DI void p1_mma(f32x4 (&acc)[4][2], const P1Frag& f, int m, int n) { if (ISV) mfma16_acc(acc[m][n], f.a[m], f.b[n]); else mfma16_acc(acc[m][n], f.b[n], f.a[m]); }
template <bool ISV>
DI void p1_body(Ctx& k, int rb, int cb) {
    const int lane = k.lane, w = k.w, th = w >> 2, cg = w & 3, t = lane & 15, g = lane >> 4;
    unsigned voffA[2], voffW[2];
#pragma unroll
    for (int i = 0; i < 2; ++i) {
        const int ra = 8 * (2 * w + i) + (lane >> 3), ca = (lane & 7) ^ ((ra >> 1) & 7);
        voffA[i] = ra * (D * 2) + ca * 16;
        const int rw = 4 * (2 * w + i) + (lane >> 4), fw = (rw & 3) | (((rw >> 3) & 1) << 2), cw = (lane & 15) ^ (fw << 1);
        voffW[i] = rw * (NQKV * 2) + cw * 16;
    }
    const char* gA = (const char*)k.x + (size_t)rb * 128 * D * 2;
    const char* gW = (const char*)k.wqkv + cb * 256;
    auto stage = [&](int kt) {
        const unsigned sl = k.lds0 + (kt & 3) * P1_SLOT + w * 2048;
#pragma unroll
        for (int i = 0; i < 2; ++i) glds16<false>(voffA[i], gA + kt * 128, sl + i * 1024);
#pragma unroll
        for (int i = 0; i < 2; ++i) glds16<false>(voffW[i], gW + (size_t)kt * 64 * NQKV * 2, sl + 16384 + i * 1024);
    };
    stage(0); stage(1); stage(2); stage(3);
    int aoff[2], woff[2];
    aoff[0] = (th * 64 + t) * 128 + ((g ^ (t >> 1)) << 4); aoff[1] = aoff[0] ^ 64;
    const int q4 = (lane >> 2) & 3, p = lane & 3, fw = q4 | ((g & 1) << 2);
#pragma unroll
    for (int n = 0; n < 2; ++n) woff[n] = 16384 + (8 * g + q4) * 256 + (((2 * cg + n) ^ fw) << 5) + p * 8;
    f32x4 acc[4][2];
#pragma unroll
    for (int m = 0; m < 4; ++m) { acc[m][0] = (f32x4){0, 0, 0, 0}; acc[m][1] = (f32x4){0, 0, 0, 0}; PIN(acc[m][0]); PIN(acc[m][1]); }
    mfma_fence();
    P1Frag f0, f1;
#define P1_RA(F_, sl, ks, m) F_.a[m] = as_frag(lds_r128(sl + aoff[ks] + (m) * 2048))
#define P1_RB(F_, sl, ks, n) F_.b[n] = cat_frag(lds_tr(sl + woff[n] + (ks) * 8192), lds_tr(sl + woff[n] + (ks) * 8192 + 1024))
#define P1_STEP(RD, FN, sl, ks, FC) do { \
        p1_mma<ISV>(acc, FC, 0, 0); SBAR(); if (RD) P1_RB(FN, sl, ks, 0); PIN(FC.b[1]); SBAR(); \
        p1_mma<ISV>(acc, FC, 0, 1); SBAR(); if (RD) P1_RA(FN, sl, ks, 0); PIN(FC.a[1]); SBAR(); \
        p1_mma<ISV>(acc, FC, 1, 0); SBAR(); if (RD) P1_RA(FN, sl, ks, 1); SBAR(); \
        p1_mma<ISV>(acc, FC, 1, 1); SBAR(); if (RD) P1_RB(FN, sl, ks, 1); PIN(FC.a[2]); SBAR(); \
        p1_mma<ISV>(acc, FC, 2, 0); SBAR(); if (RD) P1_RA(FN, sl, ks, 2); SBAR(); \
        p1_mma<ISV>(acc, FC, 2, 1); SBAR(); if (RD) P1_RA(FN, sl, ks, 3); PIN(FC.a[3]); SBAR(); \
        p1_mma<ISV>(acc, FC, 3, 0); SBAR(); \
        p1_mma<ISV>(acc, FC, 3, 1); SBAR(); } while (0)
    WAIT_BAR(12);
    startup_tag(k);
    { const lds_ptr sl = k.lds; P1_RB(f0, sl, 0, 0); P1_RB(f0, sl, 0, 1); P1_RA(f0, sl, 0, 0); P1_RA(f0, sl, 0, 1); P1_RA(f0, sl, 0, 2); P1_RA(f0, sl, 0, 3); SBAR(); }
    u32x2 gr = {0, 0};
    PRIO(1);
    const unsigned groff = k.tid < 256 ? WS_ST1 + (rb * 128) * 16 + k.tid * 8 : WS_CSQ + (cb * 128 + (k.tid & 127)) * 8;
#pragma unroll
    for (int kt = 0; kt < 10; ++kt) {
        const lds_ptr sl = k.lds + (kt & 3) * P1_SLOT, sn = k.lds + ((kt + 1) & 3) * P1_SLOT;
        P1_STEP(true, f1, sl, 1, f0);
        if (kt < 9) {
            if (kt < 7) WAIT_BAR(8); else if (kt == 7) WAIT_BAR(4); else WAIT_BAR(0);
            if (kt == 6) gr = ld64(k.rs, groff);
            if (kt + 4 < 10) stage(kt + 4);
            SBAR();
        }
        P1_STEP(kt < 9, f0, sn, 0, f1);
    }
#undef P1_RA
#undef P1_RB
#undef P1_STEP
    PRIO(0);
    mfma_fence();
    for (unsigned spins = 0; spins < SPIN_CAP; ++spins) {
        if (__all(gr.y == (unsigned)k.tag)) break;
        gr = ld64(k.rs, groff);
    }
    LAS float* sst = (LAS float*)k.lds;
    if (k.tid < 384) sst[k.tid] = __uint_as_float(gr.x);
    BAR_L();
    const LAS f32x2* st = (const LAS f32x2*)sst + th * 64;
    const LAS float* cs = sst + 256 + cg * 32;
    if (!ISV) {
        const float osc = cb < 5 ? QSCALE : 1.0f;
        const unsigned img = cb < 5 ? WS_Q : WS_K;
        const int cbs = cb % 5;
        unsigned dst[2]; f32x4 c4[2];
#pragma unroll
        for (int n = 0; n < 2; ++n) {
            const int cgi = cbs * 32 + cg * 8 + n * 4 + g, hd = cgi / 5, gi = cgi - 5 * hd;
            const int blk = gi == 4 ? 4 : ((gi & 1) << 1) | (gi >> 1);
            dst[n] = img + (rb * 4 + th * 2) * QK_TILE + hd * 1280 + blk * 256 + t * 8;
            c4[n] = *(const LAS f32x4*)(cs + n * 16 + 4 * g);
        }
#pragma unroll
        for (int m = 0; m < 4; ++m) {
            const f32x2 sm = st[m * 16 + t];
            const float r = sm.y * osc;
#pragma unroll
            for (int n = 0; n < 2; ++n) {
                const f32x4 y = (acc[m][n] - c4[n] * sm.x) * r;
                const unsigned o = dst[n] + (m >> 1) * QK_TILE + (m & 1) * 128;
                st64(k.rs, o, (u32x2){cvtpk(y[0], y[1]), cvtpk(y[2], y[3])});
            }
        }
    } else {
        const int pos = ((g & 1) << 1) | (g >> 1);
        const unsigned dst = WS_V + (rb * 8 + th * 4) * 20480 + ((cb - 10) * 128 + cg * 32 + t) * 32 + pos * 8;
        const float c1[2] = {cs[t], cs[16 + t]};
#pragma unroll
        for (int m = 0; m < 4; ++m) {
            f32x2 sm[4];
#pragma unroll
            for (int i = 0; i < 4; ++i) sm[i] = st[m * 16 + 4 * g + i];
#pragma unroll
            for (int n = 0; n < 2; ++n) {
                float y[4];
#pragma unroll
                for (int i = 0; i < 4; ++i) y[i] = (acc[m][n][i] - c1[n] * sm[i].x) * sm[i].y;
                st64(k.rs, dst + m * 20480 + n * 512, (u32x2){cvtpk(y[0], y[1]), cvtpk(y[2], y[3])});
            }
        }
    }
}

constexpr int AT_KT = 8192, AT_KB = 10240, AT_HALF = AT_KB + 10240, AT_BUF = 2 * AT_HALF;
constexpr int AT_ONES = AT_BUF, AT_ONES_BYTES = 10752, AT_KC = AT_ONES + AT_ONES_BYTES, AT_KC_BYTES = 2048, AT_FLAG = AT_KC + AT_KC_BYTES, WO_LDS = 61440, AT_RED = 0;
DI int at_slot(int i) { return i == 0 ? 0 : (i == 1 ? 61440 : 61440 + AT_BUF); }
struct AttnCtx {
    unsigned voff[5]; const char* gsrc; unsigned ldst, sstride;
    u32x4 qf0[2]; u32x2 qf1[2];
    int kofs, kt, vlane; bool vdata, hi;
};
DI void attn_stage(const AttnCtx& c, int sidx, int slot) {
#pragma unroll
    for (int i = 0; i < 5; ++i) glds16<true>(c.voff[i], c.gsrc + sidx * c.sstride, c.ldst + at_slot(slot) + i * 1024);
}
DI void attn_setup(const Ctx& k, AttnCtx& c, int h) {
    const int lane = k.lane, w = k.w, kh = w >> 2, r = lane & 31, hi = lane >> 5;
    const bool stV = (w >> 1) & 1;
#pragma unroll
    for (int i = 0; i < 5; ++i) {
        const int pc = 5 * (w & 1) + i;
        if (!stV) c.voff[i] = pc < 8 ? pc * QK_TILE + lane * 16 : ((pc - 8) * 4 + (lane >> 4)) * QK_TILE + 1024 + (lane & 15) * 16;
        else { const int ci = 64 * pc + lane, st = ci / 40, wi = ci % 40; c.voff[i] = st * 20480 + ((wi % 20) * 2 + wi / 20) * 16; }
    }
    c.gsrc = k.ws + (stV ? WS_V + h * 640 + kh * 64 * 20480 : WS_K + h * 1280 + kh * 32 * QK_TILE);
    c.sstride = stV ? 16 * 20480 : 8 * QK_TILE;
    c.ldst = k.lds0 + kh * AT_HALF + (stV ? AT_KB : 0) + (w & 1) * 5120;
    c.kofs = kh * AT_HALF + hi * 256 + r * 8;
    c.hi = hi;
    c.kt = hi ? AT_KC + r * 8 : kh * AT_HALF + AT_KT + r * 8;
    c.vdata = r < 20;
    c.vlane = c.vdata ? kh * AT_HALF + AT_KB + hi * 320 + r * 16 : AT_ONES + (r == 20 ? 128 : r < 28 ? 144 : hi ? 16 : 208);
    for (int i = k.tid; i < AT_ONES_BYTES / 4; i += 512) ((LAS unsigned*)(k.lds + AT_ONES))[i] = (i & 4) ? 0u : 0x3F803F80u;
    ((LAS unsigned*)(k.lds + AT_KC))[k.tid] = (k.tid & 1) ? 0u : 0x00003F80u;
    if (k.tid == 0) *(LAS unsigned*)(k.lds + AT_FLAG) = 0;
}
DI void attn_first_loads(const Ctx& k, AttnCtx& c, int h, int qb) {
    const int qg = k.w & 3, r = k.lane & 31, hi = k.lane >> 5;
    attn_stage(c, 0, 0);
#pragma unroll
    for (int i = 0; i < 2; ++i) {
        const unsigned qp = WS_Q + (qb * 8 + qg * 2 + i) * QK_TILE + h * 1280 + r * 8;
        const u32x2 a = ld64(k.rs, qp + hi * 256), b = ld64(k.rs, qp + hi * 256 + 512), e = ld64(k.rs, qp + 1024);
        c.qf0[i] = (u32x4){a.x, a.y, b.x, b.y};
        c.qf1[i] = hi ? (u32x2){0, 0} : e;
    }
    attn_stage(c, 1, 1);
}
DI bf16x8 kfrag0(lds_ptr p) { const u32x2 a = *(const LAS u32x2*)p, b = *(const LAS u32x2*)(p + 512); return as_frag((u32x4){a.x, a.y, b.x, b.y}); }
DI s16x4 kfrag1(lds_ptr p) { return __builtin_bit_cast(s16x4, *(const LAS u32x2*)p); }
DI s16x4 as_frag4(u32x2 v) { return __builtin_bit_cast(s16x4, v); }
DI f32x16 mfma32x8(s16x4 a, s16x4 b, f32x16 c) { return __builtin_amdgcn_mfma_f32_32x32x8bf16_1k(a, b, c, 0, 0, 0); }
DI void wo_prefetch(const Ctx& k, int ct) {
#pragma unroll 5
    for (int j = 0; j < 15; ++j) { int pc = k.w - 1 + 7 * j; if (pc >= 100) pc -= 7; glds16<true>(k.lane * 16, k.ws + WS_WO + ct * 102400 + pc * 1024, k.lds0 + WO_LDS + pc * 1024); }
}
DI void attn_fast(const Ctx& k, AttnCtx& c, const PreTile& pt, f32x16& oA, f32x16& oB, int ct3, bool& unsafe) {
    const lds_ptr lds = k.lds;
    const int hi = k.lane >> 5, kh = k.w >> 2;
    unsafe = false;
    {
        const bf16x8 k0 = kfrag0(lds + c.kofs - kh * AT_HALF); const s16x4 k1 = kfrag1(lds + (hi ? c.kt : c.kt - kh * AT_HALF));
#pragma unroll
        for (int i = 0; i < 2; ++i) {
            f32x16 s = mfma32(k0, as_frag(c.qf0[i]), (f32x16){});
            s = mfma32x8(k1, as_frag4(c.qf1[i]), s);
            float m = s[0];
#pragma unroll
            for (int j = 1; j < 16; ++j) m = fmaxf(m, s[j]);
            m = swap32_max(m);
            unsafe |= !(fabsf(m) <= 4096.f);
            if (hi) c.qf1[i].x = cvtpk(-m, 0.f) & 0xffffu;
        }
    }
    const bf16x8 qA0 = as_frag(c.qf0[0]), qB0 = as_frag(c.qf0[1]); const s16x4 qA1 = as_frag4(c.qf1[0]), qB1 = as_frag4(c.qf1[1]);
    f32x16 sA, sB, nA, nB;
    oA = (f32x16){}; oB = (f32x16){};
    bf16x8 kf0 = kfrag0(lds + c.kofs); s16x4 kf1 = kfrag1(lds + c.kt);
    sA = mfma32(kf0, qA0, (f32x16){}); sB = mfma32(kf0, qB0, (f32x16){});
    sA = mfma32x8(kf1, qA1, sA); sB = mfma32x8(kf1, qB1, sB);
    kf0 = kfrag0(lds + c.kofs + 1024); kf1 = kfrag1(lds + c.kt + 256);
    u32x4 pA0, pA1, pB0, pB1 = {0, 0, 0, 0};
    bf16x8 vf0, vf1 = {0, 0, 0, 0, 0, 0, 0, 0};
    u32x2 fa = {0, 0}, fb = fa, fc = fa, fd = fa;
#define GAP(MF, X, i, P, j) do { MF; SBAR(); X[i] = EX(X[i]); X[i + 1] = EX(X[i + 1]); X[i + 2] = EX(X[i + 2]); X[i + 3] = EX(X[i + 3]); \
        P[j] = cvtpk(X[i], X[i + 1]); P[j + 1] = cvtpk(X[i + 2], X[i + 3]); PIN(P); SBAR(); } while (0)
#define STEP(SA, SB, NA, NB, VP, KP, KT, HK) do { \
        GAP(NA = mfma32(kf0, qA0, (f32x16){}), SA, 0, pA0, 0); vf0 = as_frag(lds_r128(VP)); HK(0); SBAR(); \
        GAP(oB = mfma32(vf1, as_frag(pB1), oB), SA, 4, pA0, 2); vf1 = as_frag(lds_r128((VP) + 640)); HK(1); SBAR(); \
        GAP(NB = mfma32(kf0, qB0, (f32x16){}), SA, 8, pA1, 0); HK(2); SBAR(); \
        GAP(NA = mfma32x8(kf1, qA1, NA), SA, 12, pA1, 2); HK(3); SBAR(); \
        GAP(NB = mfma32x8(kf1, qB1, NB), SB, 0, pB0, 0); kf0 = kfrag0(KP); kf1 = kfrag1(KT); HK(4); SBAR(); \
        GAP(oA = mfma32(vf0, as_frag(pA0), oA), SB, 4, pB0, 2); HK(5); SBAR(); \
        GAP(oA = mfma32(vf1, as_frag(pA1), oA), SB, 8, pB1, 0); HK(6); SBAR(); \
        GAP(oB = mfma32(vf0, as_frag(pB0), oB), SB, 12, pB1, 2); HK(7); SBAR(); \
    } while (0)
#define HK_NONE(j)
#define STAGE(sidx, slot, nslot, PT, NST, HA, HB) do { \
        const int sb = at_slot(slot), sn = at_slot(nslot); \
        const lds_ptr vb = lds + (c.vdata ? c.vlane + sb : c.vlane); \
        lds_ptr k0 = lds + c.kofs + sb, k0n = lds + c.kofs + sn, k1 = lds + (hi ? c.kt : c.kt + sb), k1n = lds + (hi ? c.kt : c.kt + sn); \
        PIN(k0); PIN(k0n); PIN(k1); PIN(k1n); \
        STEP(sA, sB, nA, nB, vb, k0 + 2 * 1024, k1 + 2 * 256, HK_NONE); PT(0); \
        STEP(nA, nB, sA, sB, vb + 1280, k0 + 3 * 1024, k1 + 3 * 256, HK_NONE); PT(1); \
        STEP(sA, sB, nA, nB, vb + 2 * 1280, k0 + 4 * 1024, k1 + 4 * 256, HK_NONE); PT(2); \
        STEP(nA, nB, sA, sB, vb + 3 * 1280, k0 + 5 * 1024, k1 + 5 * 256, HK_NONE); PT(3); \
        STEP(sA, sB, nA, nB, vb + 4 * 1280, k0 + 6 * 1024, k1 + 6 * 256, HK_NONE); PT(4); \
        STEP(nA, nB, sA, sB, vb + 5 * 1280, k0 + 7 * 1024, k1 + 7 * 256, HK_NONE); \
        if ((sidx) == 3 && k.w == 0) {     \
            const unsigned tl = (unsigned)k.tag, th = (unsigned)(k.tag >> 32); \
            if (!__all(fa.x == tl && fa.y == th && fb.x == tl && fb.y == th && fc.x == tl && fc.y == th && fd.x == tl && fd.y == th)) poll_all(k, WS_FP); \
        } \
        WAIT_BAR(NST); \
        if ((sidx) < 2) attn_stage(c, (sidx) + 2, (nslot) == 2 ? 0 : (nslot) + 1); \
        if ((sidx) == 1 && k.tid == 0) st_flag(k.ws + WS_FP + k.c * FLAG_STRIDE, k.tag);     \
        if ((sidx) == 2 && k.w == 0) {     \
            const unsigned o = WS_FP + k.lane * FLAG_STRIDE; \
            fa = ld64(k.rs, o); fb = ld64(k.rs, o + 64 * FLAG_STRIDE); fc = ld64(k.rs, o + 128 * FLAG_STRIDE); fd = ld64(k.rs, o + 192 * FLAG_STRIDE); \
        } \
        SBAR(); \
        STEP(sA, sB, nA, nB, vb + 6 * 1280, k0n, k1n, HA); \
        STEP(nA, nB, sA, sB, vb + 7 * 1280, k0n + 1024, k1n + 256, HB); \
    } while (0)
#define PT_STORE(j) do { pretile_store(k, pt, j); SBAR(); } while (0)
#define PT_NONE(j)
    const char* wo_src = k.ws + WS_WO + ct3 * 102400;
#define WO_PIECE(n) do { int pc_ = k.w + 8 * (n); if (pc_ >= 100) pc_ -= 8; glds16<true>(k.lane * 16, wo_src + pc_ * 1024, k.lds0 + WO_LDS + pc_ * 1024); } while (0)
#define HK_WO_A(j) WO_PIECE(j)
#define HK_WO_B(j) do { if ((j) < 5) WO_PIECE(8 + (j)); } while (0)
    PRIO(1);
    STAGE(0, 0, 1, PT_STORE, 5, HK_NONE, HK_NONE);
    STAGE(1, 1, 2, PT_NONE, 0, HK_NONE, HK_NONE);
    STAGE(2, 2, 0, PT_NONE, 0, HK_NONE, HK_NONE);
    STAGE(3, 0, 1, PT_NONE, 0, HK_WO_A, HK_WO_B);
#undef STAGE
#undef STEP
#undef PT_STORE
#undef PT_NONE
#undef WO_PIECE
#undef HK_WO_A
#undef HK_WO_B
#undef HK_NONE
    PRIO(0);
    oB = mfma32(vf1, as_frag(pB1), oB);
#undef GAP
}
DI void attn_safe(const Ctx& k, AttnCtx& c, f32x16 (&o)[2], float (&mrun)[2]) {
    const lds_ptr lds = k.lds;
    attn_stage(c, 0, 0);
    if (k.lane >> 5) { c.qf1[0].x = 0; c.qf1[1].x = 0; }
    o[0] = (f32x16){}; o[1] = (f32x16){};
    mrun[0] = -INFINITY; mrun[1] = -INFINITY;
    for (int sidx = 0; sidx < 4; ++sidx) {
        const int sb = at_slot(sidx & 1);
        WAIT_BAR(0);
        if (sidx < 3) attn_stage(c, sidx + 1, (sidx + 1) & 1);
        const lds_ptr vb = lds + (c.vdata ? c.vlane + sb : c.vlane);
        for (int kt = 0; kt < 8; ++kt) {
            const bf16x8 k0 = kfrag0(lds + c.kofs + sb + kt * 1024); const s16x4 k1 = kfrag1(lds + (c.hi ? c.kt : c.kt + sb) + kt * 256);
            const bf16x8 v0 = as_frag(lds_r128(vb + kt * 1280)), v1 = as_frag(lds_r128(vb + kt * 1280 + 640));
#pragma unroll
            for (int i = 0; i < 2; ++i) {
                f32x16 s = mfma32(k0, as_frag(c.qf0[i]), (f32x16){});
                s = mfma32x8(k1, as_frag4(c.qf1[i]), s);
                float m = s[0];
#pragma unroll
                for (int j = 1; j < 16; ++j) m = fmaxf(m, s[j]);
                m = fmaxf(swap32_max(m), mrun[i]);
                const float alpha = EX(mrun[i] - m);
                mrun[i] = m;
                unsigned pk[8];
#pragma unroll
                for (int j = 0; j < 16; ++j) o[i][j] *= alpha;
#pragma unroll
                for (int j = 0; j < 8; ++j) pk[j] = cvtpk(EX(s[2 * j] - m), EX(s[2 * j + 1] - m));
                o[i] = mfma32(v0, as_frag((u32x4){pk[0], pk[1], pk[2], pk[3]}), o[i]);
                o[i] = mfma32(v1, as_frag((u32x4){pk[4], pk[5], pk[6], pk[7]}), o[i]);
            }
        }
    }
}
DI void p2_phase(const Ctx& k, AttnCtx& c, int h, int qb, int ct3) {
    const int lane = k.lane, qg = k.w & 3, kh = k.w >> 2, r = lane & 31, hi = lane >> 5;
    const lds_ptr lds = k.lds;
    attn_first_loads(k, c, h, qb);
    PreTile pt;
    pretile_read(k, pt);
    WAIT_BAR(5);
    f32x16 o[2];
    bool unsafe;
    attn_fast(k, c, pt, o[0], o[1], ct3, unsafe);
    LAS unsigned* flag = (LAS unsigned*)(lds + AT_FLAG);
    if (__any(unsafe || (hi && !(o[0][8] <= 5.764607523034235e17f && o[1][8] <= 5.764607523034235e17f))) && lane == 0) *flag = 1;
    BAR_L();
    const lds_ptr red = lds + AT_RED + qg * 8192 + lane * 16;
    auto put = [&](const f32x16& v, int slot) {
#pragma unroll
        for (int u = 0; u < 3; ++u) *(LAS f32x4*)(red + slot * 4096 + u * 1024) = (f32x4){v[4 * u], v[4 * u + 1], v[4 * u + 2], v[4 * u + 3]};
    };
    auto get = [&](int slot, int u) { return *(const LAS f32x4*)(red + slot * 4096 + u * 1024); };
    f32x4 of[3];
    if (*flag == 0) {
        if (kh) put(o[0], 0); else put(o[1], 1);
        BAR_L();
#pragma unroll
        for (int u = 0; u < 3; ++u) {
            const f32x4 p = get(kh, u);
#pragma unroll
            for (int j = 0; j < 4; ++j) of[u][j] = p[j] + (kh ? o[1][4 * u + j] : o[0][4 * u + j]);
        }
    } else {
        float mref[2];
        WAIT_BAR(0);
        attn_safe(k, c, o, mref);
        BAR_L();
        if (kh) put(o[0], 0); else put(o[1], 1);
        *(LAS float*)(red + (kh ? 0 : 4096) + 3072) = kh ? mref[0] : mref[1];
        BAR_L();
        const float m0 = kh ? mref[1] : mref[0], m1 = *(const LAS float*)(red + kh * 4096 + 3072), m = fmaxf(m0, m1);
        const float a0 = EX(m0 - m), a1 = EX(m1 - m);
#pragma unroll
        for (int u = 0; u < 3; ++u) {
            const f32x4 p = get(kh, u);
#pragma unroll
            for (int j = 0; j < 4; ++j) of[u][j] = p[j] * a1 + (kh ? o[1][4 * u + j] : o[0][4 * u + j]) * a0;
        }
        BAR_L();
        if (k.w) wo_prefetch(k, ct3);
    }
    {
        auto lr = __builtin_amdgcn_permlane32_swap(__float_as_uint(of[2][0]), __float_as_uint(of[2][0]), false, false);
        const float inv = 1.0f / __uint_as_float(lr[1]);
        const int tok = qb * 256 + qg * 64 + kh * 32 + r;
        const unsigned base = WS_O + (tok >> 4) * OH_TILE + (tok & 15) * 16;
        unsigned pk[3][2];
#pragma unroll
        for (int u = 0; u < 3; ++u) { pk[u][0] = cvtpk(of[u][0] * inv, of[u][1] * inv); pk[u][1] = cvtpk(of[u][2] * inv, of[u][3] * inv); }
        auto oaddr = [&](int col) { return base + (col >> 5) * 1024 + ((col >> 3) & 3) * 256 + (col & 7) * 2; };
        if ((h & 1) == 0) {
            auto s0 = __builtin_amdgcn_permlane32_swap(pk[0][0], pk[1][0], false, false);
            auto s1 = __builtin_amdgcn_permlane32_swap(pk[0][1], pk[1][1], false, false);
            st128(k.rs, oaddr(h * 20 + 8 * hi), (u32x4){s0[0], s1[0], s0[1], s1[1]});
            if (!hi) st64(k.rs, oaddr(h * 20 + 16), (u32x2){pk[2][0], pk[2][1]});
        } else {
            auto s0 = __builtin_amdgcn_permlane32_swap(pk[0][0], pk[2][0], false, false);
            auto s1 = __builtin_amdgcn_permlane32_swap(pk[0][1], pk[2][1], false, false);
            st128(k.rs, oaddr(h * 20 + 4 + 8 * hi), hi ? (u32x4){pk[1][0], pk[1][1], s0[0], s1[0]} : (u32x4){s0[1], s1[1], pk[1][0], pk[1][1]});
            if (!hi) st64(k.rs, oaddr(h * 20), (u32x2){s0[0], s1[0]});
        }
    }
}

DI int own_cf(int kh, int u) { return kh ? (u < 2 ? 3 + u : 4) : u; }
constexpr int RED_LDS = 143360;
struct P3Pre { u32x2 xv[5]; };
DI void p3_pre(const Ctx& k, int rb64, int ct, P3Pre& p) {
    const int tt = k.w & 3, t = k.lane & 15, g = k.lane >> 4;
    if (k.w < 4) {
#pragma unroll
        for (int cf = 0; cf < 5; ++cf) p.xv[cf] = *(const u32x2*)(k.x + (size_t)(rb64 * 64 + tt * 16 + t) * D + ct * 80 + cf * 16 + 4 * g);
    }
}
DI void w13_prefetch(const Ctx& k, int f0, int f);
DI void p3_phase(const Ctx& k, int rb64, int ct, int f0, const P3Pre& pre, u32x2 (&hv)[3]) {
    const int lane = k.lane, tt = k.w & 3, kh = k.w >> 2, t = lane & 15, g = lane >> 4;
    const unsigned ap = WS_O + (rb64 * 4 + tt) * OH_TILE + kh * 10240 + lane * 16;
    const lds_ptr wl = k.lds + WO_LDS + kh * 51200 + lane * 16;
    constexpr int WIN = 5;
    u32x4 a[10];
#pragma unroll
    for (int i = 0; i < WIN; ++i) a[i] = ld128(k.rs, ap + i * 1024);
    bf16x8 wf[2][5];
#pragma unroll
    for (int cf = 0; cf < 5; ++cf) wf[0][cf] = as_frag(lds_r128(wl + cf * 1024));
    f32x4 acc[5];
#pragma unroll
    for (int cf = 0; cf < 5; ++cf) { acc[cf] = (f32x4){0, 0, 0, 0}; PIN(acc[cf]); }
    mfma_fence();
    PRIO(1);
#pragma unroll
    for (int i = 0; i < 10; ++i) {
        if (i + WIN < 10) { a[i + WIN] = ld128(k.rs, ap + (i + WIN) * 1024); SBAR(); }
#pragma unroll
        for (int cf = 0; cf < 5; ++cf) {
            mfma16_acc(acc[cf], wf[i & 1][cf], as_frag(a[i])); SBAR();
            if (i + 1 < 10) wf[(i + 1) & 1][cf] = as_frag(lds_r128(wl + (i + 1) * 5120 + cf * 1024));
            if (cf < 4) PIN(wf[i & 1][cf + 1]); else if (i + 1 < 10) PIN(wf[(i + 1) & 1][0]);
            SBAR();
        }
    }
    PRIO(0);
    mfma_fence();
    BAR_L();
    const lds_ptr red = k.lds + RED_LDS + tt * 5120 + lane * 16;
    if (kh) {
#pragma unroll
        for (int cf = 0; cf < 5; ++cf) *(LAS f32x4*)(red + cf * 1024) = acc[cf];
        BAR_L();
        w13_prefetch(k, f0, k.w - 1);
    } else {
        BAR_L();
        const int tok = rb64 * 64 + tt * 16 + t;
        float s = 0.f, q = 0.f;
        u32x2 hq[5];
#pragma unroll
        for (int cf = 0; cf < 5; ++cf) {
            const f32x4 v = acc[cf] + *(const LAS f32x4*)(red + cf * 1024);
            hq[cf] = (u32x2){cvtpk(v[0] + bflo(pre.xv[cf].x), v[1] + bfhi(pre.xv[cf].x)), cvtpk(v[2] + bflo(pre.xv[cf].y), v[3] + bfhi(pre.xv[cf].y))};
            const int col = ct * 80 + cf * 16 + 4 * g;
            st64(k.rs, WS_H + (tok >> 4) * OH_TILE + (col >> 5) * 1024 + ((col >> 3) & 3) * 256 + t * 16 + (col & 7) * 2, hq[cf]);
            const float h0 = bflo(hq[cf].x), h1 = bfhi(hq[cf].x), h2 = bflo(hq[cf].y), h3 = bfhi(hq[cf].y);
            s += (h0 + h1) + (h2 + h3); q += (h0 * h0 + h1 * h1) + (h2 * h2 + h3 * h3);
        }
        s = sum_x32(sum_x16(s)); q = sum_x32(sum_x16(q));
        if (g == 0) st64(k.rs, WS_PST + (tok >> 4) * 2048 + (ct * 2) * 128 + t * 8, (u32x2){__float_as_uint(s), __float_as_uint(q)});
        if (g == 1) st64(k.rs, WS_PST + (tok >> 4) * 2048 + (ct * 2 + 1) * 128 + t * 8, (u32x2){0u, 0u});
        hv[0] = hq[0]; hv[1] = hq[1]; hv[2] = hq[2];
        const lds_ptr stash = k.lds + RED_LDS + tt * 5120 + lane * 8;
        *(LAS u32x2*)(stash + 3072) = hq[3]; *(LAS u32x2*)(stash + 4096) = hq[4];
    }
}
DI void pair_reduce(lds_ptr red0, int tt, int kh, int lane, const f32x4 (&acc)[5], f32x4 (&own)[3]) {
    const lds_ptr red = red0 + tt * 5120 + lane * 16;
    if (kh == 0) { *(LAS f32x4*)(red + 3072) = acc[3]; *(LAS f32x4*)(red + 4096) = acc[4]; }
    else { *(LAS f32x4*)(red) = acc[0]; *(LAS f32x4*)(red + 1024) = acc[1]; *(LAS f32x4*)(red + 2048) = acc[2]; }
    BAR_L();
    if (kh == 0) {
#pragma unroll
        for (int u = 0; u < 3; ++u) own[u] = acc[u] + *(const LAS f32x4*)(red + u * 1024);
    } else {
        own[0] = acc[3] + *(const LAS f32x4*)(red + 3072); own[1] = acc[4] + *(const LAS f32x4*)(red + 4096); own[2] = own[1];
    }
}
template <int N, int NP>
DI void w2_stage(const Ctx& k, int ct, int b, int s0) {
#pragma unroll
    for (int j = 0; j < NP; ++j) {
        int q = k.w + 8 * j;
        if (q >= 10 * N) q -= 8;
        const int hf = q >= 5 * N, rem = q - hf * 5 * N;
        glds16<true>(k.lane * 16, k.ws + WS_W2 + (ct * 54 + hf * 27 + s0) * 5120 + rem * 1024, k.lds0 + b * 71680 + hf * 35840 + rem * 1024);
    }
}
constexpr int CNT_LDS = RED_LDS + 448, DUMMY_LDS = RED_LDS + 3584;
DI void pair_signal(const Ctx& k, int cnt, unsigned flagoff) {
    LAS unsigned* p = (LAS unsigned*)(k.lds + (k.lane == 0 ? CNT_LDS + cnt * 4 : DUMMY_LDS + k.lane * 4));
    const unsigned old = __hip_atomic_fetch_add(p, 1u, __ATOMIC_RELAXED, __HIP_MEMORY_SCOPE_WORKGROUP);
    const bool last = rfl(old) == 1;
    st64(k.rs, last && k.lane == 0 ? flagoff : 0x80000000u, (u32x2){(unsigned)k.tag, (unsigned)(k.tag >> 32)});
}
DI void p5_phase(const Ctx& k, int rb64, int ct, u32x2 (&hv)[3], unsigned f4b_pub) {
    const int lane = k.lane, tt = k.w & 3, kh = k.w >> 2, t = lane & 15, g = lane >> 4;
    const unsigned ap = WS_HH + (rb64 * 4 + tt) * HH_TILE + kh * 27 * 1024 + lane * 16;
    const unsigned f4b = WS_F4B + (((rb64 >> 2) * 32 + (lane & 31)) * 4 + (rb64 & 3)) * FLAG_STRIDE;
    constexpr int WIN = 6;
    u32x4 a[27];
#pragma unroll
    for (int i = 0; i < WIN; ++i) ald128(a[i], k.rsw, ap, i * 1024);
    w2_stage<7, 9>(k, ct, 1, 7);
    if (k.w >= 4) {
        WAIT_VM(15);
        pair_signal(k, 4 + (k.w >> 1), f4b_pub);
    }
    if (kh) {
        const lds_ptr stash = k.lds + RED_LDS + tt * 5120 + lane * 8;
        hv[0] = *(const LAS u32x2*)(stash + 3072); hv[1] = *(const LAS u32x2*)(stash + 4096); hv[2] = hv[1];
    }
    lds_ptr wb[2] = {k.lds + lane * 16 + kh * 35840, k.lds + lane * 16 + kh * 35840 + 71680};
    PIN(wb[0]); PIN(wb[1]);
    auto wread = [&](int n, int cf) { return as_frag(lds_r128(wb[(n / 7) & 1] + (n % 7) * 5120 + cf * 1024)); };
    bf16x8 wf[2][5];
#pragma unroll
    for (int cf = 0; cf < 5; ++cf) wf[0][cf] = wread(0, cf);
    f32x4 acc[5];
#pragma unroll
    for (int cf = 0; cf < 5; ++cf) { acc[cf] = (f32x4){0, 0, 0, 0}; PIN(acc[cf]); }
    u32x2 fl;
    mfma_fence();
    PRIO(1);
#define P5_STEP(i, NW) do { \
        if ((i) == 6) { WAIT_BAR(6); }                                                      \
        if ((i) == 7) { BAR_L(); ald64(fl, k.rsw, f4b); w2_stage<7, 9>(k, ct, 0, 14); } \
        if ((i) == 10) {     \
            asm volatile("s_waitcnt vmcnt(12)" : "+v"(fl) :: "memory"); \
            if (!__all(fl.x == (unsigned)k.tag && fl.y == (unsigned)(k.tag >> 32))) poll_flags(k, f4b); \
        } \
        if ((i) == 13) { WAIT_BAR(6); w2_stage<6, 8>(k, ct, 1, 21); } \
        if ((i) == 20) { WAIT_BAR(6); } \
        if ((i) + WIN < 27) ald128(a[(i) + WIN], k.rsw, ap, ((i) + WIN) * 1024); \
        ald_wait<NW>(a[i]); SBAR(); \
        _Pragma("unroll") for (int cf = 0; cf < 5; ++cf) { \
            mfma16_acc(acc[cf], wf[(i) & 1][cf], as_frag(a[i])); SBAR(); \
            if ((i) + 1 < 27) wf[((i) + 1) & 1][cf] = wread((i) + 1, cf); \
            if (cf < 4) PIN(wf[(i) & 1][cf + 1]); else if ((i) + 1 < 27) PIN(wf[((i) + 1) & 1][0]); \
            SBAR(); } } while (0)
    P5_STEP(0, 15); P5_STEP(1, 15); P5_STEP(2, 15); P5_STEP(3, 15); P5_STEP(4, 15); P5_STEP(5, 15); P5_STEP(6, 6);
    P5_STEP(7, 15); P5_STEP(8, 15); P5_STEP(9, 15); P5_STEP(10, 15); P5_STEP(11, 15); P5_STEP(12, 15);
    P5_STEP(13, 14); P5_STEP(14, 14); P5_STEP(15, 14); P5_STEP(16, 14); P5_STEP(17, 14); P5_STEP(18, 14); P5_STEP(19, 6);
    P5_STEP(20, 6); P5_STEP(21, 5); P5_STEP(22, 4); P5_STEP(23, 3); P5_STEP(24, 2); P5_STEP(25, 1); P5_STEP(26, 0);
#undef P5_STEP
    PRIO(0);
    mfma_fence();
    f32x4 own[3];
    pair_reduce(k.lds + RED_LDS, tt, kh, lane, acc, own);
    const int tok = rb64 * 64 + tt * 16 + t;
#pragma unroll
    for (int u = 0; u < 3; ++u) {
        if (u < 2 || kh == 0) {
            const int col = ct * 80 + own_cf(kh, u) * 16 + 4 * g;
            *(u32x2*)(k.out + (size_t)tok * D + col) = (u32x2){cvtpk(own[u][0] + bflo(hv[u].x), own[u][1] + bfhi(hv[u].x)), cvtpk(own[u][2] + bflo(hv[u].y), own[u][3] + bfhi(hv[u].y))};
        }
    }
}

constexpr int P4_CS = 143360;
DI void w13_prefetch(const Ctx& k, int f0, int f) {
    const int p0 = f * 20;
#pragma unroll 4
    for (int j = 0; j < 20; ++j) glds16<true>(k.lane * 16, k.ws + WS_W13 + f0 * 20480 + (p0 + j) * 1024, k.lds0 + (p0 + j) * 1024);
}
DI void p4_colsum(const Ctx& k, int f) {
    const int t = k.lane & 15, g = k.lane >> 4;
    u32x4 wv[20];
#pragma unroll
    for (int s = 0; s < 20; ++s) wv[s] = lds_r128(k.lds + (f * 20 + s) * 1024 + k.lane * 16);
    f32x4 c0 = {0, 0, 0, 0}, c1 = {0, 0, 0, 0};
    const bf16x8 ones = {0x3F80, 0x3F80, 0x3F80, 0x3F80, 0x3F80, 0x3F80, 0x3F80, 0x3F80};
#pragma unroll
    for (int s = 0; s < 20; s += 2) { c0 = mfma16(as_frag(wv[s]), ones, c0); c1 = mfma16(as_frag(wv[s + 1]), ones, c1); }
    if (t == 0) *(LAS f32x4*)(k.lds + P4_CS + f * 64 + g * 16) = c0 + c1;
}
template <int NF>
DI void p4_swiglu(const Ctx& k, const f32x4 (&acc)[2][NF], int fbase, const float (&mean)[2], const float (&rstd)[2], const float (&vpe)[2], unsigned (&d)[2][4]) {
    const int g = k.lane >> 4;
#pragma unroll
    for (int f = 0; f < NF; ++f) {
        const f32x4 cs = *(const LAS f32x4*)(k.lds + P4_CS + (fbase + f) * 64 + g * 16);
#pragma unroll
        for (int i = 0; i < 2; ++i) {
            const float ce = -LOG2E * rstd[i], nm = -mean[i];
            const f32x2 ce2 = {ce, ce}, ir22 = {vpe[i], vpe[i]}, nm2 = {nm, nm};
            const f32x2 ga = __builtin_elementwise_fma(nm2, __builtin_shufflevector(cs, cs, 0, 1), __builtin_shufflevector(acc[i][f], acc[i][f], 0, 1));
            const f32x2 ua = __builtin_elementwise_fma(nm2, __builtin_shufflevector(cs, cs, 2, 3), __builtin_shufflevector(acc[i][f], acc[i][f], 2, 3));
            const f32x2 t2 = ga * ce2;
            const f32x2 e2 = {EX(t2.x), EX(t2.y)};
            const f32x2 d2 = __builtin_elementwise_fma(e2, ir22, ir22);
            const f32x2 r2 = {__builtin_amdgcn_rcpf(d2.x), __builtin_amdgcn_rcpf(d2.y)};
            const f32x2 o2 = (ga * ua) * r2;
            d[i][f] = cvtpk(o2.x, o2.y);
        }
    }
}
DI u32x4 p4_transpose(const unsigned (&d)[4]) {
    auto a = __builtin_amdgcn_permlane32_swap(d[0], d[2], false, false);
    auto b = __builtin_amdgcn_permlane32_swap(d[1], d[3], false, false);
    auto e = __builtin_amdgcn_permlane16_swap(a[0], b[0], false, false);
    auto f = __builtin_amdgcn_permlane16_swap(a[1], b[1], false, false);
    return (u32x4){e[0], e[1], f[0], f[1]};
}
DI void p4_phase(const Ctx& k, int rb, int cb, int ct5) {
    const int lane = k.lane, w = k.w, t = lane & 15, g = lane >> 4;
    const lds_ptr lds = k.lds;
    const unsigned ap = WS_H + (rb * 16 + w * 2) * OH_TILE + lane * 16;
    u32x4 a0[20], a1[20];
#define P4_LD(s) do { a0[s] = ld128(k.rs, ap + (s) * 1024); a1[s] = ld128(k.rs, ap + OH_TILE + (s) * 1024); } while (0)
    u32x2 pv[2][4];
#pragma unroll
    for (int i = 0; i < 2; ++i)
#pragma unroll
        for (int j = 0; j < 4; ++j) pv[i][j] = ld64(k.rs, WS_PST + (rb * 16 + w * 2 + i) * 2048 + (4 * g + j) * 128 + t * 8);
    P4_LD(0); P4_LD(1); P4_LD(2); P4_LD(3);
    lds_ptr wb[3] = {lds + lane * 16, lds + lane * 16 + 61440, lds + lane * 16 + 122880};
    PIN(wb[1]); PIN(wb[2]);
    auto wread = [&](int f, int s) { return as_frag(lds_r128(wb[f / 3] + (f % 3) * 20480 + s * 1024)); };
    bf16x8 wf[2][4];
#pragma unroll
    for (int f = 0; f < 4; ++f) wf[0][f] = wread(f, 0);
    f32x4 acc[2][4];
#pragma unroll
    for (int f = 0; f < 4; ++f) { acc[0][f] = (f32x4){0, 0, 0, 0}; acc[1][f] = (f32x4){0, 0, 0, 0}; PIN(acc[0][f]); PIN(acc[1][f]); }
    float mean[2], rstd[2], vpe[2];
    mfma_fence();
    PRIO(1);
#pragma unroll
    for (int s = 0; s < 20; ++s) {
        if (s + 4 < 20) P4_LD(s + 4);
        SBAR();
        if (s == 4) {
#pragma unroll
            for (int i = 0; i < 2; ++i) {
                float sm = (__uint_as_float(pv[i][0].x) + __uint_as_float(pv[i][1].x)) + (__uint_as_float(pv[i][2].x) + __uint_as_float(pv[i][3].x));
                float q = (__uint_as_float(pv[i][0].y) + __uint_as_float(pv[i][1].y)) + (__uint_as_float(pv[i][2].y) + __uint_as_float(pv[i][3].y));
                sm = sum_x32(sum_x16(sm)); q = sum_x32(sum_x16(q));
                mean[i] = sm * (1.0f / D);
                vpe[i] = fmaxf(q * (1.0f / D) - mean[i] * mean[i], 0.f) + LN_EPS;
                rstd[i] = __builtin_amdgcn_rsqf(vpe[i]);
            }
            PIN(mean[0]); PIN(mean[1]); PIN(rstd[0]); PIN(rstd[1]); PIN(vpe[0]); PIN(vpe[1]);
            SBAR();
        }
#pragma unroll
        for (int f = 0; f < 4; ++f) {
            mfma16_acc(acc[0][f], wf[s & 1][f], as_frag(a0[s])); SBAR();
            if (s + 1 < 20) wf[(s + 1) & 1][f] = wread(f, s + 1); else if (f < 3) wf[0][f] = wread(4 + f, 0);
            if (f < 3) PIN(wf[s & 1][f + 1]); else if (s + 1 < 20) PIN(wf[(s + 1) & 1][0]);
            SBAR();
            mfma16_acc(acc[1][f], wf[s & 1][f], as_frag(a1[s])); SBAR();
        }
    }
#undef P4_LD
    PRIO(0);
    mfma_fence();
    const unsigned hhb = WS_HH + (rb * 16 + w * 2) * HH_TILE + lane * 16;
    auto epilogue1 = [&]() {
        unsigned d[2][4];
        p4_swiglu<4>(k, acc, 0, mean, rstd, vpe, d);
#pragma unroll
        for (int i = 0; i < 2; ++i) st128(k.rs, hhb + i * HH_TILE + ((cb >> 4) * 27 + (cb & 15)) * 1024, p4_transpose(d[i]));
    };
    {
        LAS unsigned* p = (LAS unsigned*)(lds + (lane == 0 ? CNT_LDS + 32 : DUMMY_LDS + lane * 4));
        (void)__hip_atomic_fetch_add(p, 1u, __ATOMIC_RELAXED, __HIP_MEMORY_SCOPE_WORKGROUP);
    }
    epilogue1();
    SBAR();
    unsigned touched;
    f32x4 acc2[2][3];
#pragma unroll
    for (int f = 0; f < 3; ++f) { acc2[0][f] = (f32x4){0, 0, 0, 0}; acc2[1][f] = (f32x4){0, 0, 0, 0}; PIN(acc2[0][f]); PIN(acc2[1][f]); }
    mfma_fence();
    PRIO(1);
#pragma unroll
    for (int s = 0; s < 20; ++s) {
        if (s == 17) { WAIT_VM(1); pair_signal(k, w >> 1, WS_F4A + ((rb * 32 + cb) * 4 + (w >> 1)) * FLAG_STRIDE); SBAR(); }
        if (s == 2) {
            touch_lines(touched, k.rsw, WS_W2 + ct5 * 276480 + ((k.lb >> 3) * 540 + w * 64 + lane) * 128);
            SBAR();
        }
#pragma unroll
        for (int f = 0; f < 3; ++f) {
            mfma16_acc(acc2[0][f], wf[s & 1][f], as_frag(a0[s])); SBAR();
            if (s + 1 < 20) wf[(s + 1) & 1][f] = wread(4 + f, s + 1);
            if (f < 2) PIN(wf[s & 1][f + 1]); else if (s + 1 < 20) PIN(wf[(s + 1) & 1][0]);
            SBAR();
            mfma16_acc(acc2[1][f], wf[s & 1][f], as_frag(a1[s])); SBAR();
        }
    }
    PRIO(0);
    mfma_fence();
    asm volatile("s_waitcnt vmcnt(0)" :: "v"(touched) : "memory");
    {
        unsigned d[2][4];
        p4_swiglu<3>(k, acc2, 4, mean, rstd, vpe, *(unsigned (*)[2][4])&d);
        d[0][3] = 0; d[1][3] = 0;
        const int n2 = cb < 24 ? 3 : 2, l = (cb < 24 ? 3 * cb : 72 + 2 * (cb - 24)) + g, ls = l >> 2;
        const unsigned pos = ((ls >= 11 ? 32 + ls : 16 + ls) * 4 + (l & 3)) * 256 + t * 16;
#pragma unroll
        for (int i = 0; i < 2; ++i) st128(k.rs, g < n2 ? WS_HH + (rb * 16 + w * 2 + i) * HH_TILE + pos : 0x80000000u, p4_transpose(d[i]));
    }
    if (w < 4) {
        for (unsigned spins = 0; spins < SPIN_CAP; ++spins) {
            if (rfl(*(volatile LAS unsigned*)(lds + CNT_LDS + 32)) == 8) break;
            __builtin_amdgcn_s_sleep(1);
        }
#pragma unroll 6
        for (int j = 0; j < 18; ++j) {
            int q = w + 4 * j;
            if (q >= 70) q -= 4;
            const int hf = q >= 35, rem = q - hf * 35;
            glds16<true>(lane * 16, k.ws + WS_W2 + (ct5 * 54 + hf * 27) * 5120 + rem * 1024, k.lds0 + hf * 35840 + rem * 1024);
        }
        WAIT_VM(18);
        pair_signal(k, 4 + (w >> 1), WS_F4B + ((rb * 32 + cb) * 4 + (w >> 1)) * FLAG_STRIDE);
        WAIT_VM(0);
    }
}

constexpr int LDS_BYTES = 163840;
__global__ __launch_bounds__(512, 2) void fused_block(const uint16_t* __restrict__ x, const uint16_t* __restrict__ wqkv, const uint16_t* __restrict__ wo,
                                                      const uint16_t* __restrict__ w1, const uint16_t* __restrict__ w3, const uint16_t* __restrict__ w2,
                                                      char* __restrict__ ws, uint16_t* __restrict__ out) {
    extern __shared__ __attribute__((aligned(16))) char smem[];
    Ctx k;
    k.x = x; k.wqkv = wqkv; k.wo = wo; k.w1 = w1; k.w3 = w3; k.w2 = w2; k.ws = ws; k.out = out;
    k.rs = __builtin_amdgcn_make_buffer_rsrc(ws, 0, WS_END + 20480, 0x00020000);
    k.lds = (lds_ptr)smem; k.lds0 = (unsigned)(uintptr_t)smem;
    k.c = blockIdx.x; k.xcd = k.c & 7; k.lb = k.c >> 3;
    k.tid = threadIdx.x; k.lane = k.tid & 63; k.w = rfl(k.tid >> 6);
    k.rsw = (u32x4){(unsigned)(uintptr_t)ws, (unsigned)((uintptr_t)ws >> 32) & 0xffffu, WS_END + 20480, 0x00020000u};
    k.tag = 0;
    startup_load(k);
    const int rb1 = (k.xcd >> 2) * 8 + (k.lb & 7), cb1 = (k.xcd & 3) * 4 + (k.lb >> 3);
    if (cb1 < 15) {
        if (cb1 >= 10) p1_body<true>(k, rb1, cb1); else p1_body<false>(k, rb1, cb1);
        WAIT_BAR(0);
        if (k.tid == 0) st_flag(ws + WS_F1 + (rb1 * 16 + cb1) * FLAG_STRIDE, k.tag);
    } else {
        WAIT_VM(0);
        startup_tag(k);
        ln1_stats(k, rb1);
        if (rb1 < 15) colsum_qkv(k, rb1);
        WAIT_BAR(0);
    }
    if (k.w) pretile_load(k);
    const int h = k.xcd * 4 + (k.lb & 3), qb = k.lb >> 2;
    const int rb64 = k.xcd * 4 + (k.lb >> 3), ct = k.lb & 7;
    const int rb4 = (k.xcd >> 1) * 2 + (k.lb >> 4), cb4 = (k.xcd & 1) * 16 + (k.lb & 15);
    const int f0 = p4_f0(cb4);
    AttnCtx ac;
    attn_setup(k, ac, h);
    if (k.w == 0) {
        const int c0 = (20 * h) >> 7, c1 = (20 * h + 19) >> 7, j = k.lane & 15, sel = k.lane >> 4;
        const int cbk = (sel & 1) ? c1 : c0;
        const unsigned o0 = WS_F1 + (j * 16 + (sel < 2 ? 5 : 10) + cbk) * FLAG_STRIDE;
        const unsigned o1 = WS_F1 + ((2 * qb + (k.lane & 1)) * 16 + ((k.lane & 2) ? c1 : c0)) * FLAG_STRIDE;
        poll_flags2(k, o0, o1);
    }
    WAIT_BAR(0);
    p2_phase(k, ac, h, qb, ct);
    WAIT_BAR(0);
    if (k.tid == 0) st_flag(ws + WS_F2 + (qb * 32 + h) * FLAG_STRIDE, k.tag);
    P3Pre pre;
    p3_pre(k, rb64, ct, pre);
    if (k.w == 0) poll_flags(k, WS_F2 + (k.xcd * 32 + (k.lane & 31)) * FLAG_STRIDE);
    else if (k.w <= 3) w13_prefetch(k, f0, k.w - 1);
    BAR_L();
    if (k.c == 0 && k.tid == 0) st_flag(ws + WS_EPOCH, k.tag);
    u32x2 hv[3];
    p3_phase(k, rb64, ct, f0, pre, hv);
    if (k.w < 4) WAIT_VM(0);
    BAR_L();
    if (k.tid == 0) st_flag(ws + WS_F3 + (k.xcd * 32 + k.lb) * FLAG_STRIDE, k.tag);
    if (k.w == 0) {
        if (k.lane < 9) *(LAS unsigned*)(k.lds + CNT_LDS + k.lane * 4) = 0;
        poll_flags(k, WS_F3 + (rb4 * 32 + (k.lane & 31)) * FLAG_STRIDE);
    } else {
        WAIT_VM(0);
        p4_colsum(k, k.w - 1);
    }
    WAIT_BAR(0);
    p4_phase(k, rb4, cb4, ct);
    if (k.w == 0) poll_flags(k, WS_F4A + (((rb64 >> 2) * 32 + (k.lane & 31)) * 4 + (rb64 & 3)) * FLAG_STRIDE);
    BAR_L();
    p5_phase(k, rb64, ct, hv, WS_F4B + ((rb4 * 32 + cb4) * 4 + (k.w >> 1)) * FLAG_STRIDE);
}

extern "C" void kernel_launch(void* const* d_in, const int* in_sizes, int n_in, void* d_out, int out_size, void* d_ws, size_t ws_size, hipStream_t stream) {
    (void)hipFuncSetAttribute((const void*)fused_block, hipFuncAttributeMaxDynamicSharedMemorySize, LDS_BYTES);
    fused_block<<<256, 512, LDS_BYTES, stream>>>((const uint16_t*)d_in[0], (const uint16_t*)d_in[1], (const uint16_t*)d_in[2], (const uint16_t*)d_in[4],
                                                 (const uint16_t*)d_in[5], (const uint16_t*)d_in[6], (char*)d_ws, (uint16_t*)d_out);
}
```
